```python
import torch

def _softmax(x, axis=-1):
    return torch.softmax(x, dim=axis)

def _gelu(x):
    return torch.nn.functional.gelu(x, approximate='tanh')

def _silu(x):
    return torch.nn.functional.silu(x)

def _sigmoid(x):
    return torch.sigmoid(x)

def _layernorm(x, axis=-1, eps=1e-05):
    m = x.mean(dim=axis, keepdim=True)
    v = x.var(dim=axis, keepdim=True, unbiased=False)
    return (x - m) / torch.sqrt(v + eps)

def _scatter_add(tgt, idx, src, axis=0):
    return tgt.clone().index_add_(axis, idx, src)

def _segment_sum(src, seg, n):
    out = torch.zeros((n,) + src.shape[1:], dtype=src.dtype)
    return out.index_add_(0, seg, src)

def _pad(x, pw):
    flat = tuple((v for pair in reversed(pw) for v in pair))
    return torch.nn.functional.pad(x, flat)
kernel_name = 'interleave_layers__memory_aug_recurrent_mem_tr__glu_gated_geglu__prompt'

def setup_inputs():

    _w = lambda a: (lambda t: t.to(torch.bfloat16) if t.is_floating_point() else t)(torch.as_tensor(a))

    def _A_setup():
        B, S, Dm = (1, 2048, 512)
        H, Dh = (32, 128)
        M, seg = (8, 32)
        seg = min(seg, S)
        S = S // seg * seg
        return {'x': _w((lambda *s: torch.randn(*s, dtype=torch.bfloat16) * 0.05)(B, S, Dm)), 'mem0': _w((lambda *s: torch.randn(*s, dtype=torch.bfloat16) * 0.4)(B, M, Dm)), 'Wq': _w((lambda *s: torch.randn(*s, dtype=torch.bfloat16) * 0.8)(Dm, H, Dh) * Dm ** (-0.5)), 'Wk': _w((lambda *s: torch.randn(*s, dtype=torch.bfloat16) * 0.8)(Dm, H, Dh) * Dm ** (-0.5)), 'Wv': _w((lambda *s: torch.randn(*s, dtype=torch.bfloat16) * 0.4)(Dm, H, Dh) * Dm ** (-0.5)), 'Wo': _w((lambda *s: torch.randn(*s, dtype=torch.bfloat16) * 0.4)(H, Dh, Dm) * (H * Dh) ** (-0.5)), 'seg_len': seg}
    _A_in = _A_setup()

    def _B_setup():
        B, S, Dm, Df = (1, 2048, 512, 1792)
        return {'x': _w((lambda *s: torch.randn(*s, dtype=torch.bfloat16) * 0.05)(B, S, Dm)), 'w_gate': _w((lambda *s: torch.randn(*s, dtype=torch.bfloat16) * 0.4)(Dm, Df) * Dm ** (-0.5)), 'w_up': _w((lambda *s: torch.randn(*s, dtype=torch.bfloat16) * 0.4)(Dm, Df) * Dm ** (-0.5)), 'w_down': _w((lambda *s: torch.randn(*s, dtype=torch.bfloat16) * 0.4)(Df, Dm) * Df ** (-0.5))}
    _B_in = _B_setup()
    return {'A_x': _A_in['x'], 'A_mem0': _A_in['mem0'], 'A_Wq': _A_in['Wq'], 'A_Wk': _A_in['Wk'], 'A_Wv': _A_in['Wv'], 'A_Wo': _A_in['Wo'], 'A_seg_len': _A_in['seg_len'], 'B_x': _B_in['x'], 'B_w_gate': _B_in['w_gate'], 'B_w_up': _B_in['w_up'], 'B_w_down': _B_in['w_down']}

def reference(A_x, A_mem0, A_Wq, A_Wk, A_Wv, A_Wo, A_seg_len, B_x, B_w_gate, B_w_up, B_w_down):

    def _A_ref(x, mem0, Wq, Wk, Wv, Wo, seg_len):
        B, S, Dm = x.shape
        H, Dh = (Wq.shape[1], Wq.shape[2])
        M = mem0.shape[1]
        scale = Dh ** (-0.5)
        n_seg = S // seg_len
        L = M + seg_len
        idx = torch.arange(L)
        mask = (idx[:, None] < M) | (idx[:, None] >= idx[None, :])
        mem = mem0
        outs = []
        for s in range(n_seg):
            xs = x[:, s * seg_len:(s + 1) * seg_len, :].clone()
            h = torch.concatenate([mem, xs], axis=1)
            q = torch.einsum('btd,dhe->bhte', h, Wq)
            k = torch.einsum('btd,dhe->bhte', h, Wk)
            v = torch.einsum('btd,dhe->bhte', h, Wv)
            scores = torch.einsum('bhqe,bhke->bhqk', q, k) * scale
            scores = torch.where(mask[None, None], scores, -1000000000.0)
            p = _softmax(scores, axis=-1)
            o = torch.einsum('bhqk,bhke->bhqe', p, v)
            o = torch.einsum('bhte,hed->btd', o, Wo)
            mem = o[:, :M, :].clone()
            outs.append(o[:, M:, :])
        return torch.concatenate(outs, axis=1)

    def _B_ref(x, w_gate, w_up, w_down):
        g = torch.einsum('bsd,df->bsf', x, w_gate)
        u = torch.einsum('bsd,df->bsf', x, w_up)
        h = _gelu(g) * u
        return torch.einsum('bsf,fd->bsd', h, w_down)
    x = A_x
    x = x + _A_ref(_layernorm(x, axis=-1), A_mem0, A_Wq, A_Wk, A_Wv, A_Wo, A_seg_len)
    x = x + _B_ref(_layernorm(x, axis=-1), B_w_gate, B_w_up, B_w_down)
    x = x + _A_ref(_layernorm(x, axis=-1), A_mem0, A_Wq, A_Wk, A_Wv, A_Wo, A_seg_len)
    x = x + _B_ref(_layernorm(x, axis=-1), B_w_gate, B_w_up, B_w_down)
    return x
```

```cpp
#include <hip/hip_runtime.h>
#include <hip/hip_bf16.h>
typedef unsigned short u16;
__device__ __forceinline__ float bf2f(u16 b){ return __uint_as_float(((unsigned)b)<<16); }
__device__ __forceinline__ u16 f2bf(float f){ __hip_bfloat16 h = __float2bfloat16(f); return *(u16*)&h; }
__device__ __forceinline__ float rb(float f){ return bf2f(f2bf(f)); }

__global__ void ln_k(const u16* x, u16* y, int rows){
  int row = blockIdx.x*4 + (threadIdx.x>>6); int l = threadIdx.x&63;
  if(row>=rows) return;
  float v[8]; float s=0;
  for(int i=0;i<8;i++){ v[i]=bf2f(x[row*512+l*8+i]); s+=v[i]; }
  for(int m=32;m>=1;m>>=1) s+=__shfl_xor(s,m);
  float mean=s/512.f; float q=0;
  for(int i=0;i<8;i++){ float d=v[i]-mean; q+=d*d; }
  for(int m=32;m>=1;m>>=1) q+=__shfl_xor(q,m);
  float var=q/512.f;
  float mb=rb(mean), vb=rb(var);
  float den=rb(sqrtf(rb(vb+1e-5f)));
  for(int i=0;i<8;i++) y[row*512+l*8+i]=f2bf(rb(v[i]-mb)/den);
}
__global__ void gemm_k(const u16* A, int lda, const u16* B, int ldb, u16* C, int ldc, const u16* res, int M, int N, int K){
  __shared__ float As[16][65], Bs[16][65];
  int tx=threadIdx.x&15, ty=threadIdx.x>>4;
  int m0=blockIdx.y*64, n0=blockIdx.x*64;
  float acc[4][4]={};
  for(int k0=0;k0<K;k0+=16){
    for(int i=threadIdx.x;i<1024;i+=256){ int r=i>>4,c=i&15; As[c][r]=(m0+r<M)?bf2f(A[(size_t)(m0+r)*lda+k0+c]):0.f; }
    for(int i=threadIdx.x;i<1024;i+=256){ int r=i>>6,c=i&63; Bs[r][c]=bf2f(B[(size_t)(k0+r)*ldb+n0+c]); }
    __syncthreads();
    for(int k=0;k<16;k++){ float a[4],b[4];
      for(int i=0;i<4;i++){a[i]=As[k][ty*4+i]; b[i]=Bs[k][tx*4+i];}
      for(int i=0;i<4;i++)for(int j=0;j<4;j++)acc[i][j]+=a[i]*b[j]; }
    __syncthreads();
  }
  for(int i=0;i<4;i++)for(int j=0;j<4;j++){ int m=m0+ty*4+i,n=n0+tx*4+j; if(m<M){ float v=acc[i][j]; if(res) v=rb(v)+bf2f(res[(size_t)m*ldc+n]); C[(size_t)m*ldc+n]=f2bf(v);} }
}
__global__ void attn_k(const u16* Q, const u16* Km, const u16* Vm, const u16* Kx, const u16* Vx, u16* O, int q0, int nq, long long segstrideQ, long long segstrideM){
  int h=blockIdx.x, s=blockIdx.y; int t=threadIdx.x;
  Q+=s*segstrideQ; O+=s*segstrideQ; Km+=s*segstrideM; Vm+=s*segstrideM; Kx+=(long long)s*32*4096; Vx+=(long long)s*32*4096;
  __shared__ float P[40][41];
  const float scale=0.08838834764831845f;
  for(int i=t;i<nq*40;i+=blockDim.x){ int r=i/40,c=i%40; const u16* kp=(c<8)?Km+c*4096:Kx+(c-8)*4096; float a=0;
    for(int e=0;e<128;e++) a+=bf2f(Q[r*4096+h*128+e])*bf2f(kp[h*128+e]);
    float sc=rb(rb(a)*scale); int qi=q0+r; bool ok=(qi<8)||(qi>=c); P[r][c]=ok?sc:rb(-1e9f); }
  __syncthreads();
  if(t<nq){ float m=-INFINITY; for(int c=0;c<40;c++) m=fmaxf(m,P[t][c]); float l=0; for(int c=0;c<40;c++){ float e=__expf(P[t][c]-m); P[t][c]=e; l+=e;} for(int c=0;c<40;c++) P[t][c]=rb(P[t][c]/l); }
  __syncthreads();
  for(int i=t;i<nq*128;i+=blockDim.x){ int r=i>>7,e=i&127; float a=0; for(int c=0;c<40;c++){ const u16* vp=(c<8)?Vm+c*4096:Vx+(c-8)*4096; a+=P[r][c]*bf2f(vp[h*128+e]); } O[r*4096+h*128+e]=f2bf(a); }
}
__global__ void geglu_k(const u16* g, const u16* u, u16* h, int n){
  int i=blockIdx.x*256+threadIdx.x; if(i>=n) return; float x=bf2f(g[i]);
  float t=tanhf(0.7978845608028654f*(x+0.044715f*x*x*x)); float ge=rb(0.5f*x*(1.f+t)); h[i]=f2bf(ge*bf2f(u[i]));
}
extern "C" void kernel_launch(void* const* d_in, const int* in_sizes, int n_in, void* d_out, int out_size, void* d_ws, size_t ws_size, hipStream_t stream){
  const u16* x=(const u16*)d_in[0]; const u16* mem0=(const u16*)d_in[1]; const u16* Wq=(const u16*)d_in[2]; const u16* Wk=(const u16*)d_in[3]; const u16* Wv=(const u16*)d_in[4]; const u16* Wo=(const u16*)d_in[5];
  const u16* Wg=(const u16*)d_in[8]; const u16* Wu=(const u16*)d_in[9]; const u16* Wd=(const u16*)d_in[10];
  u16* ws=(u16*)d_ws; size_t o=0; auto al=[&](size_t n){ u16* p=ws+o; o+=n; return p; };
  u16* xn=al(2048*512); u16* Q=al(2048*4096); u16* K=al(2048*4096); u16* V=al(2048*4096); u16* O=al(2048*4096);
  u16* mem=al(65*8*512); u16* Qm=al(64*8*4096); u16* Km=al(64*8*4096); u16* Vm=al(64*8*4096); u16* Om=al(8*4096);
  u16* x1=al(2048*512); u16* x2=al(2048*512); u16* G=al(2048*1792); u16* U=al(2048*1792); u16* Hh=al(2048*1792);
  const u16* cur=x;
  for(int pass=0;pass<2;pass++){
    ln_k<<<512,256,0,stream>>>(cur,xn,2048);
    gemm_k<<<dim3(64,32),256,0,stream>>>(xn,512,Wq,4096,Q,4096,nullptr,2048,4096,512);
    gemm_k<<<dim3(64,32),256,0,stream>>>(xn,512,Wk,4096,K,4096,nullptr,2048,4096,512);
    gemm_k<<<dim3(64,32),256,0,stream>>>(xn,512,Wv,4096,V,4096,nullptr,2048,4096,512);
    for(int s=0;s<64;s++){
      const u16* m=(s==0)?mem0:mem+s*4096;
      gemm_k<<<dim3(64,1),256,0,stream>>>(m,512,Wq,4096,Qm+s*8*4096,4096,nullptr,8,4096,512);
      gemm_k<<<dim3(64,1),256,0,stream>>>(m,512,Wk,4096,Km+s*8*4096,4096,nullptr,8,4096,512);
      gemm_k<<<dim3(64,1),256,0,stream>>>(m,512,Wv,4096,Vm+s*8*4096,4096,nullptr,8,4096,512);
      if(s<63){
        attn_k<<<dim3(32,1),256,0,stream>>>(Qm+s*8*4096,Km+s*8*4096,Vm+s*8*4096,K+(size_t)s*32*4096,V+(size_t)s*32*4096,Om,0,8,0,0);
        gemm_k<<<dim3(8,1),256,0,stream>>>(Om,4096,Wo,512,mem+(s+1)*4096,512,nullptr,8,512,4096);
      }
    }
    attn_k<<<dim3(32,64),256,0,stream>>>(Q,Km,Vm,K,V,O,8,32,32*4096,8*4096);
    gemm_k<<<dim3(8,32),256,0,stream>>>(O,4096,Wo,512,x1,512,cur,2048,512,4096);
    ln_k<<<512,256,0,stream>>>(x1,xn,2048);
    gemm_k<<<dim3(28,32),256,0,stream>>>(xn,512,Wg,1792,G,1792,nullptr,2048,1792,512);
    gemm_k<<<dim3(28,32),256,0,stream>>>(xn,512,Wu,1792,U,1792,nullptr,2048,1792,512);
    geglu_k<<<2048*1792/256,256,0,stream>>>(G,U,Hh,2048*1792);
    u16* dst=(pass==0)?x2:(u16*)d_out;
    gemm_k<<<dim3(8,32),256,0,stream>>>(Hh,1792,Wd,512,dst,512,x1,2048,512,1792);
    cur=x2;
  }
}
```

```cpp
#include <hip/hip_runtime.h>
typedef unsigned short u16;
typedef unsigned v2u __attribute__((ext_vector_type(2)));
typedef unsigned v4u __attribute__((ext_vector_type(4)));
typedef unsigned v8u __attribute__((ext_vector_type(8)));
typedef unsigned v16u __attribute__((ext_vector_type(16)));
constexpr unsigned EPC = 2135587861u;
constexpr unsigned EPK = 2654435761u;
constexpr unsigned FLAG_BYTES = 854400u;
constexpr unsigned FS = 128u;
constexpr unsigned F_EP = 723200u;
constexpr unsigned F_H = 671744u;
constexpr unsigned F_KV = 0u;
constexpr unsigned F_O = 540672u;
constexpr unsigned F_PAR = 722944u;
constexpr unsigned F_UP = 723328u;
constexpr unsigned F_X0 = 720896u;
constexpr unsigned F_X1 = 606208u;
constexpr unsigned F_X2 = 704512u;
constexpr unsigned OFF_CH = 0u;
constexpr unsigned OFF_DBG = 6291456u;
constexpr unsigned OFF_FLAG = 4194304u;
constexpr unsigned OFF_H = 12582912u;
constexpr unsigned OFF_K = 12582912u;
constexpr unsigned OFF_OR = 8388608u;
constexpr unsigned OFF_Q = 79691776u;
constexpr unsigned OFF_SA = 5242880u;
constexpr unsigned OFF_V = 46137344u;
constexpr unsigned SPINCAP = 131072u;
constexpr unsigned WS_USED = 113246208u;
constexpr unsigned L_A = 131072u;
constexpr unsigned L_K = 139264u;
constexpr unsigned L_QK = 155648u;
constexpr unsigned L_O = 131072u;
constexpr unsigned L_END = 161536u;
constexpr unsigned R_MEM = 1572864u;
constexpr unsigned R_AN = 1835008u;
constexpr int ROTM = 7;
constexpr int DLY_FF = 950;
constexpr int CALT = 450;
constexpr int CPF = 1;
constexpr int NCP = 2;
constexpr int SDO = 0;
constexpr int SD1 = 3;
constexpr int SDH = 1;
constexpr int SD2 = 1;
constexpr int WSO = 0;
constexpr int ANO = 1;
constexpr int SDK = 1;
constexpr int LL1 = 1, LL2 = 1, LL3 = 1;
constexpr int SC1 = 16;
constexpr unsigned OOB = 0xf0000000u;
constexpr int LDS_BYTES = 163840;
#define DI __device__ __forceinline__
#define CLOB "memory","vcc","scc","s99","v255","a255"
#define Q4(S,i) v4u{U(S[i]),U(S[i+1]),U(S[i+2]),U(S[i+3])}
#define OPS "s_endpgm\n" : : "{s[40:43]}"(Q4(SA,0)), "{s[44:47]}"(Q4(SA,4)), "{s[48:51]}"(Q4(SA,8)), "{s[52:55]}"(Q4(SA,12)), "{s[56:59]}"(Q4(SB,0)), "{s[60:63]}"(Q4(SB,4)), "{s[64:67]}"(Q4(SB,8)), \
  "{s[72:75]}"(Q4(SC,0)), "{s[76:79]}"(Q4(SC,4)), "{s[80:83]}"(Q4(SC,8)), "{s35}"(U(w==0? OFF_DBG+0x22000+b*64 : OOB)), "{s34}"(U(ep)), "{s36}"(U(tag)), "{s37}"(U(bx)), "{s38}"(U(j&ROTM)), "{s39}"(U(trb)), "{v[0:15]}"(VC), "{v[16:23]}"(VD), "{v24}"(tid*128) : CLOB); __builtin_unreachable(

DI unsigned U(unsigned x){ return __builtin_amdgcn_readfirstlane(x); }
DI void setrs(unsigned* S, int i, const void* p, unsigned bytes){ unsigned long long q=(unsigned long long)p; S[i]=(unsigned)q; S[i+1]=(unsigned)(q>>32)&0xffffu; S[i+2]=bytes; S[i+3]=0x00020000u; }
DI unsigned c3(const void* p){ return (0x180u-(unsigned)(unsigned long long)p)&0x3ffu; }
DI unsigned long long clk(){ unsigned long long t; asm volatile("s_memtime %0\n\ts_waitcnt lgkmcnt(0)" : "=s"(t) :: "memory"); return t; }

struct Tab { unsigned t[256]; };
__global__ void __launch_bounds__(256,1) mega(const u16* __restrict__ x, const u16* __restrict__ mem0, const u16* __restrict__ Wq, const u16* __restrict__ Wk, const u16* __restrict__ Wv,
    const u16* __restrict__ Wo, const u16* __restrict__ Wg, const u16* __restrict__ Wu, const u16* __restrict__ Wd, u16* out, unsigned char* ws, const Tab tab){
  extern __shared__ __attribute__((aligned(16))) unsigned char smem[];
  const unsigned tE=(unsigned)__builtin_amdgcn_s_memrealtime();
  if(CPF){ unsigned long long pc; unsigned o, e;
    asm volatile("s_getpc_b64 %0\nLkpc:\n\ts_mov_b32 %2, LKCH-Lkpc\n\ts_max_i32 %2, %2, LKQKV-Lkpc\n\ts_max_i32 %2, %2, LKGU-Lkpc\n\ts_max_i32 %2, %2, LKWO-Lkpc\n\ts_max_i32 %2, %2, LKDN-Lkpc\n\t"
      "v_min_u32 %1, %2, %3\n\tglobal_load_dword a255, %1, %0\n\tv_add_u32 %1, 0x8000, %3\n\tv_min_u32 %1, %2, %1\n\tglobal_load_dword a255, %1, %0"
      : "=&s"(pc), "=&v"(o), "=&s"(e) : "v"(threadIdx.x*128u) : "memory","a255","scc"); }
  const int b=blockIdx.x; unsigned te, d0, d1;
  { const unsigned long long kp=(unsigned long long)__builtin_amdgcn_kernarg_segment_ptr();
    asm volatile("s_load_dword %0, %3, 0x58\n\ts_load_dword %1, %4, 0x0\n\ts_load_dword %2, %4, 0x40\n\ts_waitcnt lgkmcnt(0)" : "=&s"(te), "=&s"(d0), "=&s"(d1) : "s"(kp+4ull*b), "s"(kp) : "memory"); }
  const int role=te&15, p=(te>>4)&1, hf=(te>>5)&1, cp=(te>>6)&3, j=(te>>8)&255;
  const int tid=threadIdx.x, lane=tid&63, w=__builtin_amdgcn_readfirstlane(tid>>6);
  const int c=lane&15, g=lane>>4, i32=lane&31, hh=lane>>5, q4=(lane>>2)&3, p4=lane&3;
  const __amdgpu_buffer_rsrc_t rs=__builtin_amdgcn_make_buffer_rsrc(ws,0,(int)WS_USED,0x00020000);
  unsigned SA[16], SB[16], SC[16]; v16u VC; v8u VD={0,0,0,0,0,0,0,0};
#pragma unroll
  for(int i=0;i<16;i++){ SA[i]=0; SB[i]=0; SC[i]=0; VC[i]=0; }
  setrs(SA,0,ws,WS_USED);
  const unsigned bx=(role==2||role==3), pv=p*10*16384, phh=p*0x1000000u;
  const unsigned trb=0;
  unsigned tag=OFF_FLAG+F_UP+(b*4+w)*128, ep=0;
  { const int W=b*4+w, k=((te>>16)&255)*4+w; const bool sp=(role==4||role==7), hp=(role>=1 && role<=3);
#define PULL(Wm,o) asm volatile("s_nop 4\n\tglobal_load_dwordx4 a[252:255], %0, %1\n\tglobal_load_dwordx4 a[252:255], %0, %1 offset:1024\n\tglobal_load_dwordx4 a[252:255], %0, %1 offset:2048\n\tglobal_load_dwordx4 a[252:255], %0, %1 offset:3072" :: "v"((o)*4096+lane*16), "s"(Wm) : "memory","a252","a253","a254","a255")
#define DRAIN asm volatile("s_waitcnt vmcnt(0)" ::: "memory")
    if(hp){ PULL(Wq,k); PULL(Wq,k+512); }
    const v4u ones={~0u,~0u,~0u,~0u};
    if((lane>>3)*1024+W<(int)(F_X0/FS) || (lane>=56 && sp)) __builtin_amdgcn_raw_buffer_store_b128(ones,rs,(lane>=56? F_X0+(w*4+cp)*FS : ((lane>>3)*1024+W)*FS)+(lane&7)*16,OFF_FLAG,SC1);
    auto lines=[&](unsigned cell, unsigned u){ const unsigned o=OFF_V+(u>>1)*0x80000+cell*16384+(u&1)*8192;
      __builtin_amdgcn_raw_buffer_store_b128(ones,rs,lane*16,o,SC1); __builtin_amdgcn_raw_buffer_store_b128(ones,rs,lane*16+1024,o,SC1); };
    auto boxes=[&](unsigned rb, int h){
#pragma unroll
      for(int i=0;i<4;i++) __builtin_amdgcn_raw_buffer_store_b128(ones,rs,lane*16+i*1024,OFF_OR+rb+h*16384+w*4096,SC1);
      if(w<NCP){ const unsigned m0=*(const unsigned*)(mem0+(2*(h>>3)+((lane>>2)&1))*512+(h&3)*128+16*((h&4)+(lane&3))+8*hh+2*((lane>>3)&3));
        __builtin_amdgcn_raw_buffer_store_b128(ones,rs,(lane&7)*16+((lane>>3)&1)*8192+(lane>>4)*0x10000,rb+R_AN+w*16384+h*128,SC1);
#pragma unroll
        for(int i=5;i>=0;i--) __builtin_amdgcn_raw_buffer_store_b32(i<2? m0 : ~0u,rs,lane*4,rb+R_MEM+w*16384+h*256+(i>>1)*0x10000+(i&1)*8192,SC1); } };
    if(hp && k>=368) lines(8,k-368);
    if(hp && k<128) boxes(OFF_CH,k>>2);
    if(role<5 || role>6){ const unsigned ev=__builtin_amdgcn_raw_buffer_load_b32(rs,0,OFF_FLAG+F_EP,SC1);
      DRAIN; ep=U(ev);
      if(lane<32) __builtin_amdgcn_raw_buffer_store_b32(ep*EPK+EPC,rs,lane*4,tag,SC1);
      tag=ep*EPK+EPC;
      if(hp){
        PULL(Wk,k); DRAIN; PULL(Wk,k+512); DRAIN; PULL(Wv,k); DRAIN; PULL(Wv,k+512); DRAIN;
        if(role!=1){ PULL(Wo,k); PULL(Wo,k+256); DRAIN; PULL(Wo,k+512); PULL(Wo,k+768); DRAIN; }
        if(role!=1){ while((unsigned)__builtin_amdgcn_s_memrealtime()-tE<DLY_FF) __builtin_amdgcn_s_sleep(4);
          if(!p){
            const int u=(role==2)? j : 14+hf*8+j;
            for(int h=u;h<32;h+=30) boxes(OFF_CH+0x200000u,h);
            v2u go={~tag,0};
            for(unsigned it=0; it<SPINCAP && go[0]!=tag; it++) go=__builtin_amdgcn_raw_buffer_load_b64(rs,0,OFF_FLAG+F_EP,SC1);
            if(go[0]==tag) for(unsigned i=(role==2? j : u-14)*4+w; i<64; i+=(role==2? 56 : 64)) lines(role==2? 9 : 18, 2*i+((((OFF_V>>21)+(i>>2))^U(go[1])^1)&1));
            if(k>=224) DRAIN; } }
        if(role!=1 && k<224){
          PULL(Wg,k); PULL(Wu,k); DRAIN; PULL(Wg,k+224); PULL(Wu,k+224); DRAIN; if(role==3){ PULL(Wd,k-112); PULL(Wd,k); DRAIN; PULL(Wd,k+112); PULL(Wd,k+224); DRAIN; } }
      } } }
  if(role==5){
    const int h=j; const unsigned rb=OFF_CH+p*0x200000u;
    const int tokA=2*(c>>2)+(c&1), hfA=(c>>1)&1;
    VC[0]=(c&2)?0xEEEE:0x4444; VC[1]=LL2? (2*(c>>2)+(g&1))*1024+(g>>1)*64+(c&3)*16+w*256 : ((tokA&~1)+hfA)*1024+g*32+(tokA&1)*16+w*256; VC[2]=L_QK+tokA*544+g*16; VC[3]=L_QK+2*g*544+(w>>1)*128+c*8+(w&1)*4;
    VC[4]=L_O+(4*w+g)*128+tokA*16; VC[5]=L_O+(2*g+hfA)*128+tokA*16; VC[6]=w*32768+lane*16;
    VC[7]=rb+(8*g+w)*16384+(c>>3)*4096+h*128+(c&7)*16; VC[8]=rb+h*16384+(w>>1)*4096+(LL1? lane*16 : (2*g+hfA)*128+tokA*16); VC[9]=lane*16; VC[10]=L_A+lane*16; VC[11]=(w&1)? (lane<8? lane*4 : 0x7f000000) : (LL1? 2*(g&1)+(g>>1) : g)*32+(c&7)*4+(c>>3)*16384; VD[7]=c-8*g+16*(w>>1);
#pragma unroll
    for(int q=0;q<4;q++) VC[12+q]=(((c&7)==2*q)?0x3f80u:0u)|(((c&7)==2*q+1)?0x3f800000u:0u);
    VD[1]=(lane>>3)*8192+h*256+(w>>1)*128+(lane&7)*16; VD[2]=(h*128+g)*1024+w*256+((c^((g&1)<<1))*16);
    { const int rq=32*(g&1)+2*(g>>1)+64*(q4>>1)+(q4&1), rw=16*(g&1)+8*(g>>1)+4*(q4>>1)+(q4&1);
      VD[4]=LL2? w*32768+(32*g+q4)*128+((g^(2*(w&1)))<<5)+8*p4 : w*32768+rq*128+((((g&1)+2*(q4>>1))^(2*(w&1)))<<5)+8*p4; VD[5]=w*32768+rw*256+(((q4&1)|((q4>>1)<<1)|((g&1)<<2))<<5)+8*p4; }
    SA[5]=OFF_FLAG+F_KV+SDK*8192+h*FS+p*32; SA[6]=rb+R_MEM+cp*16384; SA[9]=(w<NCP)? rb+R_MEM+w*16384+h*256 : OOB;
    SA[10]=OFF_OR+rb+h*16384+w*2048; SA[11]=OFF_K+h*16384+w*2048; SA[12]=L_K+w*2048; SA[13]=rb+R_MEM+(2*h+(w>>1))*128;
    SA[14]=(w&1)? OFF_FLAG+F_O+(p*2+(w>>1))*16384+((SDO>>(w>>1))&1)*8192+h*2*FS : OOB;
    SB[0]=0x33333333u; SB[1]=0x33333333u; SB[2]=(w==0)? rb+R_AN+h*128 : 0x80000000u; SB[3]=(ANO && (w&1))? 0x80000000u : rb+R_AN+cp*16384; SB[4]=w; SB[5]=OFF_FLAG+F_PAR+p*128;
    SB[7]=(w==0)? OFF_DBG+(p*32+h)*1024 : (w==1)? OFF_DBG+0x40000+b*2048 : OOB;
    setrs(SC,0,Wq,512*4096*2); setrs(SC,4,Wk,512*4096*2); setrs(SC,8,Wo,512*4096*2); setrs(SB,8,Wv,0);
    asm volatile(
"s_waitcnt lgkmcnt(0)\n"
"s_nop 4\n"
"s_lshl_b32 s95, s60, 20\n"
"s_lshl_b32 s84, s60, 15\n"
"s_mov_b32 s33, 4917504\n"
"buffer_load_dword v125, off, s[40:43], s33 sc1\n"
"v_mbcnt_lo_u32_b32 v124, -1, 0\n"
"v_lshlrev_b32 v124, 2, v124\n"
"v_cmp_gt_i32 s[0:1], 0, v23\n"
"v_cmp_gt_i32 s[2:3], 1, v23\n"
"v_cmp_gt_i32 s[4:5], 2, v23\n"
"v_cmp_gt_i32 s[6:7], 3, v23\n"
"v_cmp_gt_i32 s[8:9], 4, v23\n"
"v_cmp_gt_i32 s[10:11], 5, v23\n"
"v_cmp_gt_i32 s[12:13], 6, v23\n"
"v_cmp_gt_i32 s[14:15], 7, v23\n"
"s_bitcmp1_b32 s51, 16\n"
"s_cselect_b32 s18, 32, 20\n"
"s_mov_b32 s17, 0x80000000\n"
"s_bitcmp1_b32 s60, 0\n"
"s_cselect_b32 s16, 67106816, s17\n"
"s_cselect_b32 s22, s45, s17\n"
"v_xor_b32 v22, 0, v17\n"
"s_add_u32 m0, s84, 0\n"
"s_xor_b32 s66, s95, 0x0\n"
"buffer_load_dwordx4 v22, s[72:75], s66 offen lds\n"
"s_add_u32 m0, s84, 1024\n"
"s_xor_b32 s66, s95, 0x10000\n"
"buffer_load_dwordx4 v22, s[72:75], s66 offen lds\n"
"s_add_u32 m0, s84, 2048\n"
"s_xor_b32 s66, s95, 0x20000\n"
"buffer_load_dwordx4 v22, s[72:75], s66 offen lds\n"
"s_add_u32 m0, s84, 3072\n"
"s_xor_b32 s66, s95, 0x30000\n"
"buffer_load_dwordx4 v22, s[72:75], s66 offen lds\n"
"v_xor_b32 v22, 32, v17\n"
"s_add_u32 m0, s84, 4096\n"
"s_xor_b32 s66, s95, 0x40000\n"
"buffer_load_dwordx4 v22, s[72:75], s66 offen lds\n"
"s_add_u32 m0, s84, 5120\n"
"s_xor_b32 s66, s95, 0x50000\n"
"buffer_load_dwordx4 v22, s[72:75], s66 offen lds\n"
"s_add_u32 m0, s84, 6144\n"
"s_xor_b32 s66, s95, 0x60000\n"
"buffer_load_dwordx4 v22, s[72:75], s66 offen lds\n"
"s_add_u32 m0, s84, 7168\n"
"s_xor_b32 s66, s95, 0x70000\n"
"buffer_load_dwordx4 v22, s[72:75], s66 offen lds\n"
"v_xor_b32 v22, 64, v17\n"
"s_add_u32 m0, s84, 8192\n"
"s_xor_b32 s66, s95, 0x80000\n"
"buffer_load_dwordx4 v22, s[72:75], s66 offen lds\n"
"s_add_u32 m0, s84, 9216\n"
"s_xor_b32 s66, s95, 0x90000\n"
"buffer_load_dwordx4 v22, s[72:75], s66 offen lds\n"
"s_add_u32 m0, s84, 10240\n"
"s_xor_b32 s66, s95, 0xa0000\n"
"buffer_load_dwordx4 v22, s[72:75], s66 offen lds\n"
"s_add_u32 m0, s84, 11264\n"
"s_xor_b32 s66, s95, 0xb0000\n"
"buffer_load_dwordx4 v22, s[72:75], s66 offen lds\n"
"v_xor_b32 v22, 96, v17\n"
"s_add_u32 m0, s84, 12288\n"
"s_xor_b32 s66, s95, 0xc0000\n"
"buffer_load_dwordx4 v22, s[72:75], s66 offen lds\n"
"s_add_u32 m0, s84, 13312\n"
"s_xor_b32 s66, s95, 0xd0000\n"
"buffer_load_dwordx4 v22, s[72:75], s66 offen lds\n"
"s_add_u32 m0, s84, 14336\n"
"s_xor_b32 s66, s95, 0xe0000\n"
"buffer_load_dwordx4 v22, s[72:75], s66 offen lds\n"
"s_add_u32 m0, s84, 15360\n"
"s_xor_b32 s66, s95, 0xf0000\n"
"buffer_load_dwordx4 v22, s[72:75], s66 offen lds\n"
"v_xor_b32 v22, 0, v17\n"
"s_add_u32 m0, s84, 16384\n"
"s_xor_b32 s66, s95, 0x200000\n"
"buffer_load_dwordx4 v22, s[72:75], s66 offen lds\n"
"s_add_u32 m0, s84, 17408\n"
"s_xor_b32 s66, s95, 0x210000\n"
"buffer_load_dwordx4 v22, s[72:75], s66 offen lds\n"
"s_add_u32 m0, s84, 18432\n"
"s_xor_b32 s66, s95, 0x220000\n"
"buffer_load_dwordx4 v22, s[72:75], s66 offen lds\n"
"s_add_u32 m0, s84, 19456\n"
"s_xor_b32 s66, s95, 0x230000\n"
"buffer_load_dwordx4 v22, s[72:75], s66 offen lds\n"
"v_xor_b32 v22, 32, v17\n"
"s_add_u32 m0, s84, 20480\n"
"s_xor_b32 s66, s95, 0x240000\n"
"buffer_load_dwordx4 v22, s[72:75], s66 offen lds\n"
"s_add_u32 m0, s84, 21504\n"
"s_xor_b32 s66, s95, 0x250000\n"
"buffer_load_dwordx4 v22, s[72:75], s66 offen lds\n"
"s_add_u32 m0, s84, 22528\n"
"s_xor_b32 s66, s95, 0x260000\n"
"buffer_load_dwordx4 v22, s[72:75], s66 offen lds\n"
"s_add_u32 m0, s84, 23552\n"
"s_xor_b32 s66, s95, 0x270000\n"
"buffer_load_dwordx4 v22, s[72:75], s66 offen lds\n"
"v_xor_b32 v22, 64, v17\n"
"s_add_u32 m0, s84, 24576\n"
"s_xor_b32 s66, s95, 0x280000\n"
"buffer_load_dwordx4 v22, s[72:75], s66 offen lds\n"
"s_add_u32 m0, s84, 25600\n"
"s_xor_b32 s66, s95, 0x290000\n"
"buffer_load_dwordx4 v22, s[72:75], s66 offen lds\n"
"s_add_u32 m0, s84, 26624\n"
"s_xor_b32 s66, s95, 0x2a0000\n"
"buffer_load_dwordx4 v22, s[72:75], s66 offen lds\n"
"s_add_u32 m0, s84, 27648\n"
"s_xor_b32 s66, s95, 0x2b0000\n"
"buffer_load_dwordx4 v22, s[72:75], s66 offen lds\n"
"v_xor_b32 v22, 96, v17\n"
"s_add_u32 m0, s84, 28672\n"
"s_xor_b32 s66, s95, 0x2c0000\n"
"buffer_load_dwordx4 v22, s[72:75], s66 offen lds\n"
"s_add_u32 m0, s84, 29696\n"
"s_xor_b32 s66, s95, 0x2d0000\n"
"buffer_load_dwordx4 v22, s[72:75], s66 offen lds\n"
"s_add_u32 m0, s84, 30720\n"
"s_xor_b32 s66, s95, 0x2e0000\n"
"buffer_load_dwordx4 v22, s[72:75], s66 offen lds\n"
"s_add_u32 m0, s84, 31744\n"
"s_xor_b32 s66, s95, 0x2f0000\n"
"buffer_load_dwordx4 v22, s[72:75], s66 offen lds\n"
"s_mov_b64 s[72:73], s[64:65]\n"
"s_mov_b32 s68, s63\n"
"s_waitcnt vmcnt(32)\n"
"v_readfirstlane_b32 s33, v125\n"
"s_cmp_lt_i32 s36, 0\n"
"s_cselect_b32 s34, s34, s33\n"
"s_mul_i32 s33, s34, 0x9e3779b1\n"
"s_add_u32 s33, s33, 0x7f4a7c15\n"
"v_mov_b32 v125, s33\n"
"s_mov_b32 exec_hi, 0\n"
"buffer_store_dword v125, v124, s[40:43], s36 offen sc1\n"
"s_mov_b64 exec, -1\n"
"s_mov_b32 s36, s33\n"
"ds_write_b128 v10, v[12:15] offset:29184\n"
"s_waitcnt vmcnt(17)\n"
"s_barrier\n"
"v_xor_b32 v23, 0, v20\n"
"ds_read_b64_tr_b16 v[24:25], v23 offset:0\n"
"ds_read_b64_tr_b16 v[26:27], v23 offset:2048\n"
"ds_read_b64_tr_b16 v[28:29], v23 offset:512\n"
"ds_read_b64_tr_b16 v[30:31], v23 offset:2560\n"
"ds_read_b64_tr_b16 v[72:73], v23 offset:1024\n"
"ds_read_b64_tr_b16 v[74:75], v23 offset:3072\n"
"ds_read_b64_tr_b16 v[76:77], v23 offset:1536\n"
"ds_read_b64_tr_b16 v[78:79], v23 offset:3584\n"
"s_waitcnt lgkmcnt(4)\n"
"v_xor_b32 v23, 32, v20\n"
"ds_read_b64_tr_b16 v[32:33], v23 offset:0\n"
"ds_read_b64_tr_b16 v[34:35], v23 offset:2048\n"
"ds_read_b64_tr_b16 v[36:37], v23 offset:512\n"
"ds_read_b64_tr_b16 v[38:39], v23 offset:2560\n"
"ds_read_b64_tr_b16 a[96:97], v23 offset:1024\n"
"ds_read_b64_tr_b16 a[98:99], v23 offset:3072\n"
"ds_read_b64_tr_b16 a[100:101], v23 offset:1536\n"
"ds_read_b64_tr_b16 a[102:103], v23 offset:3584\n"
"s_waitcnt lgkmcnt(4)\n"
"v_xor_b32 v23, 32768, v20\n"
"ds_read_b64_tr_b16 a[104:105], v23 offset:0\n"
"ds_read_b64_tr_b16 a[106:107], v23 offset:2048\n"
"ds_read_b64_tr_b16 a[108:109], v23 offset:512\n"
"ds_read_b64_tr_b16 a[110:111], v23 offset:2560\n"
"ds_read_b64_tr_b16 a[120:121], v23 offset:1024\n"
"ds_read_b64_tr_b16 a[122:123], v23 offset:3072\n"
"ds_read_b64_tr_b16 a[124:125], v23 offset:1536\n"
"ds_read_b64_tr_b16 a[126:127], v23 offset:3584\n"
"s_waitcnt lgkmcnt(4)\n"
"v_xor_b32 v23, 32800, v20\n"
"ds_read_b64_tr_b16 a[112:113], v23 offset:0\n"
"ds_read_b64_tr_b16 a[114:115], v23 offset:2048\n"
"ds_read_b64_tr_b16 a[116:117], v23 offset:512\n"
"ds_read_b64_tr_b16 a[118:119], v23 offset:2560\n"
"ds_read_b64_tr_b16 a[128:129], v23 offset:1024\n"
"ds_read_b64_tr_b16 a[130:131], v23 offset:3072\n"
"ds_read_b64_tr_b16 a[132:133], v23 offset:1536\n"
"ds_read_b64_tr_b16 a[134:135], v23 offset:3584\n"
"s_waitcnt lgkmcnt(0)\n"
"s_barrier\n"
"v_xor_b32 v22, 0, v17\n"
"s_add_u32 m0, s84, 0\n"
"s_xor_b32 s66, s95, 0x0\n"
"buffer_load_dwordx4 v22, s[76:79], s66 offen lds\n"
"s_add_u32 m0, s84, 1024\n"
"s_xor_b32 s66, s95, 0x10000\n"
"buffer_load_dwordx4 v22, s[76:79], s66 offen lds\n"
"s_add_u32 m0, s84, 2048\n"
"s_xor_b32 s66, s95, 0x20000\n"
"buffer_load_dwordx4 v22, s[76:79], s66 offen lds\n"
"s_add_u32 m0, s84, 3072\n"
"s_xor_b32 s66, s95, 0x30000\n"
"buffer_load_dwordx4 v22, s[76:79], s66 offen lds\n"
"v_xor_b32 v22, 32, v17\n"
"s_add_u32 m0, s84, 4096\n"
"s_xor_b32 s66, s95, 0x40000\n"
"buffer_load_dwordx4 v22, s[76:79], s66 offen lds\n"
"s_add_u32 m0, s84, 5120\n"
"s_xor_b32 s66, s95, 0x50000\n"
"buffer_load_dwordx4 v22, s[76:79], s66 offen lds\n"
"s_add_u32 m0, s84, 6144\n"
"s_xor_b32 s66, s95, 0x60000\n"
"buffer_load_dwordx4 v22, s[76:79], s66 offen lds\n"
"s_add_u32 m0, s84, 7168\n"
"s_xor_b32 s66, s95, 0x70000\n"
"buffer_load_dwordx4 v22, s[76:79], s66 offen lds\n"
"v_xor_b32 v22, 64, v17\n"
"s_add_u32 m0, s84, 8192\n"
"s_xor_b32 s66, s95, 0x80000\n"
"buffer_load_dwordx4 v22, s[76:79], s66 offen lds\n"
"s_add_u32 m0, s84, 9216\n"
"s_xor_b32 s66, s95, 0x90000\n"
"buffer_load_dwordx4 v22, s[76:79], s66 offen lds\n"
"s_add_u32 m0, s84, 10240\n"
"s_xor_b32 s66, s95, 0xa0000\n"
"buffer_load_dwordx4 v22, s[76:79], s66 offen lds\n"
"s_add_u32 m0, s84, 11264\n"
"s_xor_b32 s66, s95, 0xb0000\n"
"buffer_load_dwordx4 v22, s[76:79], s66 offen lds\n"
"v_xor_b32 v22, 96, v17\n"
"s_add_u32 m0, s84, 12288\n"
"s_xor_b32 s66, s95, 0xc0000\n"
"buffer_load_dwordx4 v22, s[76:79], s66 offen lds\n"
"s_add_u32 m0, s84, 13312\n"
"s_xor_b32 s66, s95, 0xd0000\n"
"buffer_load_dwordx4 v22, s[76:79], s66 offen lds\n"
"s_add_u32 m0, s84, 14336\n"
"s_xor_b32 s66, s95, 0xe0000\n"
"buffer_load_dwordx4 v22, s[76:79], s66 offen lds\n"
"s_add_u32 m0, s84, 15360\n"
"s_xor_b32 s66, s95, 0xf0000\n"
"buffer_load_dwordx4 v22, s[76:79], s66 offen lds\n"
"s_waitcnt vmcnt(17)\n"
"s_barrier\n"
"v_xor_b32 v23, 0, v20\n"
"ds_read_b64_tr_b16 a[136:137], v23 offset:16384\n"
"ds_read_b64_tr_b16 a[138:139], v23 offset:18432\n"
"ds_read_b64_tr_b16 a[140:141], v23 offset:16896\n"
"ds_read_b64_tr_b16 a[142:143], v23 offset:18944\n"
"ds_read_b64_tr_b16 a[152:153], v23 offset:17408\n"
"ds_read_b64_tr_b16 a[154:155], v23 offset:19456\n"
"ds_read_b64_tr_b16 a[156:157], v23 offset:17920\n"
"ds_read_b64_tr_b16 a[158:159], v23 offset:19968\n"
"s_waitcnt lgkmcnt(4)\n"
"v_xor_b32 v23, 32, v20\n"
"ds_read_b64_tr_b16 a[144:145], v23 offset:16384\n"
"ds_read_b64_tr_b16 a[146:147], v23 offset:18432\n"
"ds_read_b64_tr_b16 a[148:149], v23 offset:16896\n"
"ds_read_b64_tr_b16 a[150:151], v23 offset:18944\n"
"ds_read_b64_tr_b16 a[160:161], v23 offset:17408\n"
"ds_read_b64_tr_b16 a[162:163], v23 offset:19456\n"
"ds_read_b64_tr_b16 a[164:165], v23 offset:17920\n"
"ds_read_b64_tr_b16 a[166:167], v23 offset:19968\n"
"s_waitcnt lgkmcnt(4)\n"
"v_xor_b32 v23, 32768, v20\n"
"ds_read_b64_tr_b16 a[168:169], v23 offset:16384\n"
"ds_read_b64_tr_b16 a[170:171], v23 offset:18432\n"
"ds_read_b64_tr_b16 a[172:173], v23 offset:16896\n"
"ds_read_b64_tr_b16 a[174:175], v23 offset:18944\n"
"ds_read_b64_tr_b16 a[184:185], v23 offset:17408\n"
"ds_read_b64_tr_b16 a[186:187], v23 offset:19456\n"
"ds_read_b64_tr_b16 a[188:189], v23 offset:17920\n"
"ds_read_b64_tr_b16 a[190:191], v23 offset:19968\n"
"s_waitcnt lgkmcnt(4)\n"
"v_xor_b32 v23, 32800, v20\n"
"ds_read_b64_tr_b16 a[176:177], v23 offset:16384\n"
"ds_read_b64_tr_b16 a[178:179], v23 offset:18432\n"
"ds_read_b64_tr_b16 a[180:181], v23 offset:16896\n"
"ds_read_b64_tr_b16 a[182:183], v23 offset:18944\n"
"ds_read_b64_tr_b16 a[192:193], v23 offset:17408\n"
"ds_read_b64_tr_b16 a[194:195], v23 offset:19456\n"
"ds_read_b64_tr_b16 a[196:197], v23 offset:17920\n"
"ds_read_b64_tr_b16 a[198:199], v23 offset:19968\n"
"s_waitcnt lgkmcnt(0)\n"
"s_barrier\n"
"v_xor_b32 v22, 0, v17\n"
"s_add_u32 m0, s84, 16384\n"
"s_xor_b32 s66, s95, 0x200000\n"
"buffer_load_dwordx4 v22, s[76:79], s66 offen lds\n"
"s_add_u32 m0, s84, 17408\n"
"s_xor_b32 s66, s95, 0x210000\n"
"buffer_load_dwordx4 v22, s[76:79], s66 offen lds\n"
"s_add_u32 m0, s84, 18432\n"
"s_xor_b32 s66, s95, 0x220000\n"
"buffer_load_dwordx4 v22, s[76:79], s66 offen lds\n"
"s_add_u32 m0, s84, 19456\n"
"s_xor_b32 s66, s95, 0x230000\n"
"buffer_load_dwordx4 v22, s[76:79], s66 offen lds\n"
"v_xor_b32 v22, 32, v17\n"
"s_add_u32 m0, s84, 20480\n"
"s_xor_b32 s66, s95, 0x240000\n"
"buffer_load_dwordx4 v22, s[76:79], s66 offen lds\n"
"s_add_u32 m0, s84, 21504\n"
"s_xor_b32 s66, s95, 0x250000\n"
"buffer_load_dwordx4 v22, s[76:79], s66 offen lds\n"
"s_add_u32 m0, s84, 22528\n"
"s_xor_b32 s66, s95, 0x260000\n"
"buffer_load_dwordx4 v22, s[76:79], s66 offen lds\n"
"s_add_u32 m0, s84, 23552\n"
"s_xor_b32 s66, s95, 0x270000\n"
"buffer_load_dwordx4 v22, s[76:79], s66 offen lds\n"
"v_xor_b32 v22, 64, v17\n"
"s_add_u32 m0, s84, 24576\n"
"s_xor_b32 s66, s95, 0x280000\n"
"buffer_load_dwordx4 v22, s[76:79], s66 offen lds\n"
"s_add_u32 m0, s84, 25600\n"
"s_xor_b32 s66, s95, 0x290000\n"
"buffer_load_dwordx4 v22, s[76:79], s66 offen lds\n"
"s_add_u32 m0, s84, 26624\n"
"s_xor_b32 s66, s95, 0x2a0000\n"
"buffer_load_dwordx4 v22, s[76:79], s66 offen lds\n"
"s_add_u32 m0, s84, 27648\n"
"s_xor_b32 s66, s95, 0x2b0000\n"
"buffer_load_dwordx4 v22, s[76:79], s66 offen lds\n"
"v_xor_b32 v22, 96, v17\n"
"s_add_u32 m0, s84, 28672\n"
"s_xor_b32 s66, s95, 0x2c0000\n"
"buffer_load_dwordx4 v22, s[76:79], s66 offen lds\n"
"s_add_u32 m0, s84, 29696\n"
"s_xor_b32 s66, s95, 0x2d0000\n"
"buffer_load_dwordx4 v22, s[76:79], s66 offen lds\n"
"s_add_u32 m0, s84, 30720\n"
"s_xor_b32 s66, s95, 0x2e0000\n"
"buffer_load_dwordx4 v22, s[76:79], s66 offen lds\n"
"s_add_u32 m0, s84, 31744\n"
"s_xor_b32 s66, s95, 0x2f0000\n"
"buffer_load_dwordx4 v22, s[76:79], s66 offen lds\n"
"s_waitcnt vmcnt(16)\n"
"s_barrier\n"
"v_xor_b32 v23, 0, v20\n"
"ds_read_b64_tr_b16 v[40:41], v23 offset:0\n"
"ds_read_b64_tr_b16 v[42:43], v23 offset:2048\n"
"ds_read_b64_tr_b16 v[44:45], v23 offset:512\n"
"ds_read_b64_tr_b16 v[46:47], v23 offset:2560\n"
"ds_read_b64_tr_b16 a[200:201], v23 offset:1024\n"
"ds_read_b64_tr_b16 a[202:203], v23 offset:3072\n"
"ds_read_b64_tr_b16 a[204:205], v23 offset:1536\n"
"ds_read_b64_tr_b16 a[206:207], v23 offset:3584\n"
"s_waitcnt lgkmcnt(4)\n"
"v_xor_b32 v23, 32, v20\n"
"ds_read_b64_tr_b16 v[48:49], v23 offset:0\n"
"ds_read_b64_tr_b16 v[50:51], v23 offset:2048\n"
"ds_read_b64_tr_b16 v[52:53], v23 offset:512\n"
"ds_read_b64_tr_b16 v[54:55], v23 offset:2560\n"
"ds_read_b64_tr_b16 a[208:209], v23 offset:1024\n"
"ds_read_b64_tr_b16 a[210:211], v23 offset:3072\n"
"ds_read_b64_tr_b16 a[212:213], v23 offset:1536\n"
"ds_read_b64_tr_b16 a[214:215], v23 offset:3584\n"
"s_waitcnt lgkmcnt(4)\n"
"v_xor_b32 v23, 32768, v20\n"
"ds_read_b64_tr_b16 a[216:217], v23 offset:0\n"
"ds_read_b64_tr_b16 a[218:219], v23 offset:2048\n"
"ds_read_b64_tr_b16 a[220:221], v23 offset:512\n"
"ds_read_b64_tr_b16 a[222:223], v23 offset:2560\n"
"ds_read_b64_tr_b16 a[232:233], v23 offset:1024\n"
"ds_read_b64_tr_b16 a[234:235], v23 offset:3072\n"
"ds_read_b64_tr_b16 a[236:237], v23 offset:1536\n"
"ds_read_b64_tr_b16 a[238:239], v23 offset:3584\n"
"s_waitcnt lgkmcnt(4)\n"
"v_xor_b32 v23, 32800, v20\n"
"ds_read_b64_tr_b16 a[224:225], v23 offset:0\n"
"ds_read_b64_tr_b16 a[226:227], v23 offset:2048\n"
"ds_read_b64_tr_b16 a[228:229], v23 offset:512\n"
"ds_read_b64_tr_b16 a[230:231], v23 offset:2560\n"
"ds_read_b64_tr_b16 a[240:241], v23 offset:1024\n"
"ds_read_b64_tr_b16 a[242:243], v23 offset:3072\n"
"ds_read_b64_tr_b16 a[244:245], v23 offset:1536\n"
"ds_read_b64_tr_b16 a[246:247], v23 offset:3584\n"
"s_waitcnt lgkmcnt(0)\n"
"s_barrier\n"
"v_xor_b32 v22, 0, v17\n"
"s_add_u32 m0, s84, 0\n"
"s_xor_b32 s66, s95, 0x0\n"
"buffer_load_dwordx4 v22, s[72:75], s66 offen lds\n"
"s_add_u32 m0, s84, 1024\n"
"s_xor_b32 s66, s95, 0x10000\n"
"buffer_load_dwordx4 v22, s[72:75], s66 offen lds\n"
"s_add_u32 m0, s84, 2048\n"
"s_xor_b32 s66, s95, 0x20000\n"
"buffer_load_dwordx4 v22, s[72:75], s66 offen lds\n"
"s_add_u32 m0, s84, 3072\n"
"s_xor_b32 s66, s95, 0x30000\n"
"buffer_load_dwordx4 v22, s[72:75], s66 offen lds\n"
"v_xor_b32 v22, 32, v17\n"
"s_add_u32 m0, s84, 4096\n"
"s_xor_b32 s66, s95, 0x40000\n"
"buffer_load_dwordx4 v22, s[72:75], s66 offen lds\n"
"s_add_u32 m0, s84, 5120\n"
"s_xor_b32 s66, s95, 0x50000\n"
"buffer_load_dwordx4 v22, s[72:75], s66 offen lds\n"
"s_add_u32 m0, s84, 6144\n"
"s_xor_b32 s66, s95, 0x60000\n"
"buffer_load_dwordx4 v22, s[72:75], s66 offen lds\n"
"s_add_u32 m0, s84, 7168\n"
"s_xor_b32 s66, s95, 0x70000\n"
"buffer_load_dwordx4 v22, s[72:75], s66 offen lds\n"
"v_xor_b32 v22, 64, v17\n"
"s_add_u32 m0, s84, 8192\n"
"s_xor_b32 s66, s95, 0x80000\n"
"buffer_load_dwordx4 v22, s[72:75], s66 offen lds\n"
"s_add_u32 m0, s84, 9216\n"
"s_xor_b32 s66, s95, 0x90000\n"
"buffer_load_dwordx4 v22, s[72:75], s66 offen lds\n"
"s_add_u32 m0, s84, 10240\n"
"s_xor_b32 s66, s95, 0xa0000\n"
"buffer_load_dwordx4 v22, s[72:75], s66 offen lds\n"
"s_add_u32 m0, s84, 11264\n"
"s_xor_b32 s66, s95, 0xb0000\n"
"buffer_load_dwordx4 v22, s[72:75], s66 offen lds\n"
"v_xor_b32 v22, 96, v17\n"
"s_add_u32 m0, s84, 12288\n"
"s_xor_b32 s66, s95, 0xc0000\n"
"buffer_load_dwordx4 v22, s[72:75], s66 offen lds\n"
"s_add_u32 m0, s84, 13312\n"
"s_xor_b32 s66, s95, 0xd0000\n"
"buffer_load_dwordx4 v22, s[72:75], s66 offen lds\n"
"s_add_u32 m0, s84, 14336\n"
"s_xor_b32 s66, s95, 0xe0000\n"
"buffer_load_dwordx4 v22, s[72:75], s66 offen lds\n"
"s_add_u32 m0, s84, 15360\n"
"s_xor_b32 s66, s95, 0xf0000\n"
"buffer_load_dwordx4 v22, s[72:75], s66 offen lds\n"
"s_waitcnt vmcnt(16)\n"
"s_barrier\n"
"v_xor_b32 v23, 0, v20\n"
"ds_read_b64_tr_b16 v[80:81], v23 offset:16384\n"
"ds_read_b64_tr_b16 v[82:83], v23 offset:18432\n"
"ds_read_b64_tr_b16 v[84:85], v23 offset:16896\n"
"ds_read_b64_tr_b16 v[86:87], v23 offset:18944\n"
"ds_read_b64_tr_b16 v[96:97], v23 offset:17408\n"
"ds_read_b64_tr_b16 v[98:99], v23 offset:19456\n"
"ds_read_b64_tr_b16 v[100:101], v23 offset:17920\n"
"ds_read_b64_tr_b16 v[102:103], v23 offset:19968\n"
"s_waitcnt lgkmcnt(4)\n"
"v_xor_b32 v23, 32, v20\n"
"ds_read_b64_tr_b16 v[88:89], v23 offset:16384\n"
"ds_read_b64_tr_b16 v[90:91], v23 offset:18432\n"
"ds_read_b64_tr_b16 v[92:93], v23 offset:16896\n"
"ds_read_b64_tr_b16 v[94:95], v23 offset:18944\n"
"ds_read_b64_tr_b16 v[104:105], v23 offset:17408\n"
"ds_read_b64_tr_b16 v[106:107], v23 offset:19456\n"
"ds_read_b64_tr_b16 v[108:109], v23 offset:17920\n"
"ds_read_b64_tr_b16 v[110:111], v23 offset:19968\n"
"s_waitcnt lgkmcnt(4)\n"
"v_xor_b32 v23, 32768, v20\n"
"ds_read_b64_tr_b16 v[112:113], v23 offset:16384\n"
"ds_read_b64_tr_b16 v[114:115], v23 offset:18432\n"
"ds_read_b64_tr_b16 v[116:117], v23 offset:16896\n"
"ds_read_b64_tr_b16 v[118:119], v23 offset:18944\n"
"ds_read_b64_tr_b16 v[128:129], v23 offset:17408\n"
"ds_read_b64_tr_b16 v[130:131], v23 offset:19456\n"
"ds_read_b64_tr_b16 v[132:133], v23 offset:17920\n"
"ds_read_b64_tr_b16 v[134:135], v23 offset:19968\n"
"s_waitcnt lgkmcnt(4)\n"
"v_xor_b32 v23, 32800, v20\n"
"ds_read_b64_tr_b16 a[248:249], v23 offset:16384\n"
"ds_read_b64_tr_b16 a[250:251], v23 offset:18432\n"
"ds_read_b64_tr_b16 a[252:253], v23 offset:16896\n"
"ds_read_b64_tr_b16 a[254:255], v23 offset:18944\n"
"ds_read_b64_tr_b16 v[136:137], v23 offset:17408\n"
"ds_read_b64_tr_b16 v[138:139], v23 offset:19456\n"
"ds_read_b64_tr_b16 v[140:141], v23 offset:17920\n"
"ds_read_b64_tr_b16 v[142:143], v23 offset:19968\n"
"s_waitcnt lgkmcnt(0)\n"
"s_barrier\n"
"v_xor_b32 v22, 0, v17\n"
"s_add_u32 m0, s84, 16384\n"
"s_xor_b32 s66, s95, 0x200000\n"
"buffer_load_dwordx4 v22, s[72:75], s66 offen lds\n"
"s_add_u32 m0, s84, 17408\n"
"s_xor_b32 s66, s95, 0x210000\n"
"buffer_load_dwordx4 v22, s[72:75], s66 offen lds\n"
"s_add_u32 m0, s84, 18432\n"
"s_xor_b32 s66, s95, 0x220000\n"
"buffer_load_dwordx4 v22, s[72:75], s66 offen lds\n"
"s_add_u32 m0, s84, 19456\n"
"s_xor_b32 s66, s95, 0x230000\n"
"buffer_load_dwordx4 v22, s[72:75], s66 offen lds\n"
"v_xor_b32 v22, 32, v17\n"
"s_add_u32 m0, s84, 20480\n"
"s_xor_b32 s66, s95, 0x240000\n"
"buffer_load_dwordx4 v22, s[72:75], s66 offen lds\n"
"s_add_u32 m0, s84, 21504\n"
"s_xor_b32 s66, s95, 0x250000\n"
"buffer_load_dwordx4 v22, s[72:75], s66 offen lds\n"
"s_add_u32 m0, s84, 22528\n"
"s_xor_b32 s66, s95, 0x260000\n"
"buffer_load_dwordx4 v22, s[72:75], s66 offen lds\n"
"s_add_u32 m0, s84, 23552\n"
"s_xor_b32 s66, s95, 0x270000\n"
"buffer_load_dwordx4 v22, s[72:75], s66 offen lds\n"
"v_xor_b32 v22, 64, v17\n"
"s_add_u32 m0, s84, 24576\n"
"s_xor_b32 s66, s95, 0x280000\n"
"buffer_load_dwordx4 v22, s[72:75], s66 offen lds\n"
"s_add_u32 m0, s84, 25600\n"
"s_xor_b32 s66, s95, 0x290000\n"
"buffer_load_dwordx4 v22, s[72:75], s66 offen lds\n"
"s_add_u32 m0, s84, 26624\n"
"s_xor_b32 s66, s95, 0x2a0000\n"
"buffer_load_dwordx4 v22, s[72:75], s66 offen lds\n"
"s_add_u32 m0, s84, 27648\n"
"s_xor_b32 s66, s95, 0x2b0000\n"
"buffer_load_dwordx4 v22, s[72:75], s66 offen lds\n"
"v_xor_b32 v22, 96, v17\n"
"s_add_u32 m0, s84, 28672\n"
"s_xor_b32 s66, s95, 0x2c0000\n"
"buffer_load_dwordx4 v22, s[72:75], s66 offen lds\n"
"s_add_u32 m0, s84, 29696\n"
"s_xor_b32 s66, s95, 0x2d0000\n"
"buffer_load_dwordx4 v22, s[72:75], s66 offen lds\n"
"s_add_u32 m0, s84, 30720\n"
"s_xor_b32 s66, s95, 0x2e0000\n"
"buffer_load_dwordx4 v22, s[72:75], s66 offen lds\n"
"s_add_u32 m0, s84, 31744\n"
"s_xor_b32 s66, s95, 0x2f0000\n"
"buffer_load_dwordx4 v22, s[72:75], s66 offen lds\n"
"s_waitcnt vmcnt(16)\n"
"s_barrier\n"
"v_xor_b32 v23, 0, v20\n"
"ds_read_b64_tr_b16 v[56:57], v23 offset:0\n"
"ds_read_b64_tr_b16 v[58:59], v23 offset:2048\n"
"ds_read_b64_tr_b16 v[60:61], v23 offset:512\n"
"ds_read_b64_tr_b16 v[62:63], v23 offset:2560\n"
"ds_read_b64_tr_b16 v[144:145], v23 offset:1024\n"
"ds_read_b64_tr_b16 v[146:147], v23 offset:3072\n"
"ds_read_b64_tr_b16 v[148:149], v23 offset:1536\n"
"ds_read_b64_tr_b16 v[150:151], v23 offset:3584\n"
"s_waitcnt lgkmcnt(4)\n"
"v_xor_b32 v23, 32, v20\n"
"ds_read_b64_tr_b16 v[64:65], v23 offset:0\n"
"ds_read_b64_tr_b16 v[66:67], v23 offset:2048\n"
"ds_read_b64_tr_b16 v[68:69], v23 offset:512\n"
"ds_read_b64_tr_b16 v[70:71], v23 offset:2560\n"
"ds_read_b64_tr_b16 v[152:153], v23 offset:1024\n"
"ds_read_b64_tr_b16 v[154:155], v23 offset:3072\n"
"ds_read_b64_tr_b16 v[156:157], v23 offset:1536\n"
"ds_read_b64_tr_b16 v[158:159], v23 offset:3584\n"
"s_waitcnt lgkmcnt(4)\n"
"v_xor_b32 v23, 32768, v20\n"
"ds_read_b64_tr_b16 v[160:161], v23 offset:0\n"
"ds_read_b64_tr_b16 v[162:163], v23 offset:2048\n"
"ds_read_b64_tr_b16 v[164:165], v23 offset:512\n"
"ds_read_b64_tr_b16 v[166:167], v23 offset:2560\n"
"ds_read_b64_tr_b16 v[176:177], v23 offset:1024\n"
"ds_read_b64_tr_b16 v[178:179], v23 offset:3072\n"
"ds_read_b64_tr_b16 v[180:181], v23 offset:1536\n"
"ds_read_b64_tr_b16 v[182:183], v23 offset:3584\n"
"s_waitcnt lgkmcnt(4)\n"
"v_xor_b32 v23, 32800, v20\n"
"ds_read_b64_tr_b16 v[168:169], v23 offset:0\n"
"ds_read_b64_tr_b16 v[170:171], v23 offset:2048\n"
"ds_read_b64_tr_b16 v[172:173], v23 offset:512\n"
"ds_read_b64_tr_b16 v[174:175], v23 offset:2560\n"
"ds_read_b64_tr_b16 v[184:185], v23 offset:1024\n"
"ds_read_b64_tr_b16 v[186:187], v23 offset:3072\n"
"ds_read_b64_tr_b16 v[188:189], v23 offset:1536\n"
"ds_read_b64_tr_b16 v[190:191], v23 offset:3584\n"
"s_waitcnt lgkmcnt(0)\n"
"s_barrier\n"
"buffer_load_dword v125, off, s[40:43], s61 sc1\n"
"v_xor_b32 v22, 0, v18\n"
"s_add_u32 m0, s84, 0\n"
"s_mov_b32 s66, 0x0\n"
"buffer_load_dwordx4 v22, s[80:83], s66 offen lds\n"
"v_xor_b32 v22, 64, v18\n"
"s_add_u32 m0, s84, 1024\n"
"s_mov_b32 s66, 0x1000\n"
"buffer_load_dwordx4 v22, s[80:83], s66 offen lds\n"
"v_xor_b32 v22, 0, v18\n"
"s_add_u32 m0, s84, 2048\n"
"s_mov_b32 s66, 0x2000\n"
"buffer_load_dwordx4 v22, s[80:83], s66 offen lds\n"
"v_xor_b32 v22, 64, v18\n"
"s_add_u32 m0, s84, 3072\n"
"s_mov_b32 s66, 0x3000\n"
"buffer_load_dwordx4 v22, s[80:83], s66 offen lds\n"
"v_xor_b32 v22, 128, v18\n"
"s_add_u32 m0, s84, 4096\n"
"s_mov_b32 s66, 0x4000\n"
"buffer_load_dwordx4 v22, s[80:83], s66 offen lds\n"
"v_xor_b32 v22, 192, v18\n"
"s_add_u32 m0, s84, 5120\n"
"s_mov_b32 s66, 0x5000\n"
"buffer_load_dwordx4 v22, s[80:83], s66 offen lds\n"
"v_xor_b32 v22, 128, v18\n"
"s_add_u32 m0, s84, 6144\n"
"s_mov_b32 s66, 0x6000\n"
"buffer_load_dwordx4 v22, s[80:83], s66 offen lds\n"
"v_xor_b32 v22, 192, v18\n"
"s_add_u32 m0, s84, 7168\n"
"s_mov_b32 s66, 0x7000\n"
"buffer_load_dwordx4 v22, s[80:83], s66 offen lds\n"
"v_xor_b32 v22, 0, v18\n"
"s_add_u32 m0, s84, 8192\n"
"s_mov_b32 s66, 0x8000\n"
"buffer_load_dwordx4 v22, s[80:83], s66 offen lds\n"
"v_xor_b32 v22, 64, v18\n"
"s_add_u32 m0, s84, 9216\n"
"s_mov_b32 s66, 0x9000\n"
"buffer_load_dwordx4 v22, s[80:83], s66 offen lds\n"
"v_xor_b32 v22, 0, v18\n"
"s_add_u32 m0, s84, 10240\n"
"s_mov_b32 s66, 0xa000\n"
"buffer_load_dwordx4 v22, s[80:83], s66 offen lds\n"
"v_xor_b32 v22, 64, v18\n"
"s_add_u32 m0, s84, 11264\n"
"s_mov_b32 s66, 0xb000\n"
"buffer_load_dwordx4 v22, s[80:83], s66 offen lds\n"
"v_xor_b32 v22, 128, v18\n"
"s_add_u32 m0, s84, 12288\n"
"s_mov_b32 s66, 0xc000\n"
"buffer_load_dwordx4 v22, s[80:83], s66 offen lds\n"
"v_xor_b32 v22, 192, v18\n"
"s_add_u32 m0, s84, 13312\n"
"s_mov_b32 s66, 0xd000\n"
"buffer_load_dwordx4 v22, s[80:83], s66 offen lds\n"
"v_xor_b32 v22, 128, v18\n"
"s_add_u32 m0, s84, 14336\n"
"s_mov_b32 s66, 0xe000\n"
"buffer_load_dwordx4 v22, s[80:83], s66 offen lds\n"
"v_xor_b32 v22, 192, v18\n"
"s_add_u32 m0, s84, 15360\n"
"s_mov_b32 s66, 0xf000\n"
"buffer_load_dwordx4 v22, s[80:83], s66 offen lds\n"
"s_waitcnt vmcnt(16)\n"
"s_barrier\n"
"v_xor_b32 v23, 0, v20\n"
"ds_read_b64_tr_b16 v[192:193], v23 offset:16384\n"
"ds_read_b64_tr_b16 v[194:195], v23 offset:18432\n"
"ds_read_b64_tr_b16 v[196:197], v23 offset:16896\n"
"ds_read_b64_tr_b16 v[198:199], v23 offset:18944\n"
"ds_read_b64_tr_b16 v[208:209], v23 offset:17408\n"
"ds_read_b64_tr_b16 v[210:211], v23 offset:19456\n"
"ds_read_b64_tr_b16 v[212:213], v23 offset:17920\n"
"ds_read_b64_tr_b16 v[214:215], v23 offset:19968\n"
"s_waitcnt lgkmcnt(4)\n"
"v_xor_b32 v23, 32, v20\n"
"ds_read_b64_tr_b16 v[200:201], v23 offset:16384\n"
"ds_read_b64_tr_b16 v[202:203], v23 offset:18432\n"
"ds_read_b64_tr_b16 v[204:205], v23 offset:16896\n"
"ds_read_b64_tr_b16 v[206:207], v23 offset:18944\n"
"ds_read_b64_tr_b16 v[216:217], v23 offset:17408\n"
"ds_read_b64_tr_b16 v[218:219], v23 offset:19456\n"
"ds_read_b64_tr_b16 v[220:221], v23 offset:17920\n"
"ds_read_b64_tr_b16 v[222:223], v23 offset:19968\n"
"s_waitcnt lgkmcnt(4)\n"
"v_xor_b32 v23, 32768, v20\n"
"ds_read_b64_tr_b16 v[224:225], v23 offset:16384\n"
"ds_read_b64_tr_b16 v[226:227], v23 offset:18432\n"
"ds_read_b64_tr_b16 v[228:229], v23 offset:16896\n"
"ds_read_b64_tr_b16 v[230:231], v23 offset:18944\n"
"ds_read_b64_tr_b16 v[240:241], v23 offset:17408\n"
"ds_read_b64_tr_b16 v[242:243], v23 offset:19456\n"
"ds_read_b64_tr_b16 v[244:245], v23 offset:17920\n"
"ds_read_b64_tr_b16 v[246:247], v23 offset:19968\n"
"s_waitcnt lgkmcnt(4)\n"
"v_xor_b32 v23, 32800, v20\n"
"ds_read_b64_tr_b16 v[232:233], v23 offset:16384\n"
"ds_read_b64_tr_b16 v[234:235], v23 offset:18432\n"
"ds_read_b64_tr_b16 v[236:237], v23 offset:16896\n"
"ds_read_b64_tr_b16 v[238:239], v23 offset:18944\n"
"ds_read_b64_tr_b16 v[248:249], v23 offset:17408\n"
"ds_read_b64_tr_b16 v[250:251], v23 offset:19456\n"
"ds_read_b64_tr_b16 v[252:253], v23 offset:17920\n"
"ds_read_b64_tr_b16 v[254:255], v23 offset:19968\n"
"s_waitcnt lgkmcnt(0)\n"
"s_barrier\n"
"s_mov_b32 s65, 0\n"
"Lcpara%=:\n"
"v_xor_b32 v120, s36, v125\n"
"v_cmp_lt_u32 vcc, 1, v120\n"
"s_cbranch_vccz Lcparb%=\n"
"buffer_load_dword v125, off, s[40:43], s61 sc1\n"
"s_add_u32 s65, s65, 1\n"
"s_cmp_gt_u32 s65, 131072\n"
"s_cbranch_scc1 Lcend%=\n"
"s_waitcnt vmcnt(0)\n"
"s_branch Lcpara%=\n"
"Lcparb%=:\n"
"v_readfirstlane_b32 s66, v125\n"
"s_and_b32 s66, s66, 1\n"
"s_lshl_b32 s66, s66, 13\n"
"s_add_u32 s46, s46, s66\n"
"s_add_u32 s49, s49, s66\n"
"s_add_u32 s53, s53, s66\n"
"s_add_u32 s58, s58, s66\n"
"s_add_u32 s59, s59, s66\n"
"s_add_u32 s50, s50, s66\n"
"v_add_u32 v7, s66, v7\n"
"v_add_u32 v8, s66, v8\n"
"s_mov_b32 s33, 4917504\n"
"buffer_load_dwordx2 v[120:121], off, s[40:43], s33 sc1\n"
"v_xor_b32 v22, 0, v18\n"
"s_add_u32 m0, s84, 16384\n"
"s_mov_b32 s66, 0x10000\n"
"buffer_load_dwordx4 v22, s[80:83], s66 offen lds\n"
"v_xor_b32 v22, 64, v18\n"
"s_add_u32 m0, s84, 17408\n"
"s_mov_b32 s66, 0x11000\n"
"buffer_load_dwordx4 v22, s[80:83], s66 offen lds\n"
"v_xor_b32 v22, 0, v18\n"
"s_add_u32 m0, s84, 18432\n"
"s_mov_b32 s66, 0x12000\n"
"buffer_load_dwordx4 v22, s[80:83], s66 offen lds\n"
"v_xor_b32 v22, 64, v18\n"
"s_add_u32 m0, s84, 19456\n"
"s_mov_b32 s66, 0x13000\n"
"buffer_load_dwordx4 v22, s[80:83], s66 offen lds\n"
"v_xor_b32 v22, 128, v18\n"
"s_add_u32 m0, s84, 20480\n"
"s_mov_b32 s66, 0x14000\n"
"buffer_load_dwordx4 v22, s[80:83], s66 offen lds\n"
"v_xor_b32 v22, 192, v18\n"
"s_add_u32 m0, s84, 21504\n"
"s_mov_b32 s66, 0x15000\n"
"buffer_load_dwordx4 v22, s[80:83], s66 offen lds\n"
"v_xor_b32 v22, 128, v18\n"
"s_add_u32 m0, s84, 22528\n"
"s_mov_b32 s66, 0x16000\n"
"buffer_load_dwordx4 v22, s[80:83], s66 offen lds\n"
"v_xor_b32 v22, 192, v18\n"
"s_add_u32 m0, s84, 23552\n"
"s_mov_b32 s66, 0x17000\n"
"buffer_load_dwordx4 v22, s[80:83], s66 offen lds\n"
"v_xor_b32 v22, 0, v18\n"
"s_add_u32 m0, s84, 24576\n"
"s_mov_b32 s66, 0x18000\n"
"buffer_load_dwordx4 v22, s[80:83], s66 offen lds\n"
"v_xor_b32 v22, 64, v18\n"
"s_add_u32 m0, s84, 25600\n"
"s_mov_b32 s66, 0x19000\n"
"buffer_load_dwordx4 v22, s[80:83], s66 offen lds\n"
"v_xor_b32 v22, 0, v18\n"
"s_add_u32 m0, s84, 26624\n"
"s_mov_b32 s66, 0x1a000\n"
"buffer_load_dwordx4 v22, s[80:83], s66 offen lds\n"
"v_xor_b32 v22, 64, v18\n"
"s_add_u32 m0, s84, 27648\n"
"s_mov_b32 s66, 0x1b000\n"
"buffer_load_dwordx4 v22, s[80:83], s66 offen lds\n"
"v_xor_b32 v22, 128, v18\n"
"s_add_u32 m0, s84, 28672\n"
"s_mov_b32 s66, 0x1c000\n"
"buffer_load_dwordx4 v22, s[80:83], s66 offen lds\n"
"v_xor_b32 v22, 192, v18\n"
"s_add_u32 m0, s84, 29696\n"
"s_mov_b32 s66, 0x1d000\n"
"buffer_load_dwordx4 v22, s[80:83], s66 offen lds\n"
"v_xor_b32 v22, 128, v18\n"
"s_add_u32 m0, s84, 30720\n"
"s_mov_b32 s66, 0x1e000\n"
"buffer_load_dwordx4 v22, s[80:83], s66 offen lds\n"
"v_xor_b32 v22, 192, v18\n"
"s_add_u32 m0, s84, 31744\n"
"s_mov_b32 s66, 0x1f000\n"
"buffer_load_dwordx4 v22, s[80:83], s66 offen lds\n"
"v_mov_b64 v[12:13], -1\n"
"v_mov_b64 v[14:15], -1\n"
"s_add_u32 s66, s50, 524288\n"
"buffer_store_dwordx4 v[12:15], v9, s[40:43], s66 offen sc1\n"
"buffer_store_dwordx4 v[12:15], v9, s[40:43], s66 offen offset:1024 sc1\n"
"s_add_u32 s66, s50, 1048576\n"
"buffer_store_dwordx4 v[12:15], v9, s[40:43], s66 offen sc1\n"
"buffer_store_dwordx4 v[12:15], v9, s[40:43], s66 offen offset:1024 sc1\n"
"s_add_u32 s66, s50, 1572864\n"
"buffer_store_dwordx4 v[12:15], v9, s[40:43], s66 offen sc1\n"
"buffer_store_dwordx4 v[12:15], v9, s[40:43], s66 offen offset:1024 sc1\n"
"s_waitcnt vmcnt(22)\n"
"v_xor_b32 v23, 0, v21\n"
"ds_read_b64_tr_b16 a[0:1], v23 offset:0\n"
"ds_read_b64_tr_b16 a[2:3], v23 offset:512\n"
"ds_read_b64_tr_b16 a[4:5], v23 offset:8192\n"
"ds_read_b64_tr_b16 a[6:7], v23 offset:8704\n"
"v_xor_b32 v23, 32, v21\n"
"ds_read_b64_tr_b16 a[8:9], v23 offset:0\n"
"ds_read_b64_tr_b16 a[10:11], v23 offset:512\n"
"ds_read_b64_tr_b16 a[12:13], v23 offset:8192\n"
"ds_read_b64_tr_b16 a[14:15], v23 offset:8704\n"
"s_waitcnt lgkmcnt(4)\n"
"v_xor_b32 v23, 64, v21\n"
"ds_read_b64_tr_b16 a[16:17], v23 offset:0\n"
"ds_read_b64_tr_b16 a[18:19], v23 offset:512\n"
"ds_read_b64_tr_b16 a[20:21], v23 offset:8192\n"
"ds_read_b64_tr_b16 a[22:23], v23 offset:8704\n"
"v_xor_b32 v23, 96, v21\n"
"ds_read_b64_tr_b16 a[24:25], v23 offset:0\n"
"ds_read_b64_tr_b16 a[26:27], v23 offset:512\n"
"ds_read_b64_tr_b16 a[28:29], v23 offset:8192\n"
"ds_read_b64_tr_b16 a[30:31], v23 offset:8704\n"
"s_waitcnt lgkmcnt(4)\n"
"v_xor_b32 v23, 128, v21\n"
"ds_read_b64_tr_b16 a[32:33], v23 offset:0\n"
"ds_read_b64_tr_b16 a[34:35], v23 offset:512\n"
"ds_read_b64_tr_b16 a[36:37], v23 offset:8192\n"
"ds_read_b64_tr_b16 a[38:39], v23 offset:8704\n"
"v_xor_b32 v23, 160, v21\n"
"ds_read_b64_tr_b16 a[48:49], v23 offset:0\n"
"ds_read_b64_tr_b16 a[50:51], v23 offset:512\n"
"ds_read_b64_tr_b16 a[52:53], v23 offset:8192\n"
"ds_read_b64_tr_b16 a[54:55], v23 offset:8704\n"
"s_waitcnt lgkmcnt(4)\n"
"v_xor_b32 v23, 192, v21\n"
"ds_read_b64_tr_b16 a[64:65], v23 offset:0\n"
"ds_read_b64_tr_b16 a[66:67], v23 offset:512\n"
"ds_read_b64_tr_b16 a[68:69], v23 offset:8192\n"
"ds_read_b64_tr_b16 a[70:71], v23 offset:8704\n"
"v_xor_b32 v23, 224, v21\n"
"ds_read_b64_tr_b16 a[80:81], v23 offset:0\n"
"ds_read_b64_tr_b16 a[82:83], v23 offset:512\n"
"ds_read_b64_tr_b16 a[84:85], v23 offset:8192\n"
"ds_read_b64_tr_b16 a[86:87], v23 offset:8704\n"
"s_waitcnt lgkmcnt(0)\n"
"ds_write_b128 v6, v[24:27] offset:0\n"
"ds_write_b128 v6, v[28:31] offset:1024\n"
"ds_write_b128 v6, v[32:35] offset:2048\n"
"ds_write_b128 v6, v[36:39] offset:3072\n"
"ds_write_b128 v6, v[40:43] offset:4096\n"
"ds_write_b128 v6, v[44:47] offset:5120\n"
"ds_write_b128 v6, v[48:51] offset:6144\n"
"ds_write_b128 v6, v[52:55] offset:7168\n"
"ds_write_b128 v6, v[56:59] offset:8192\n"
"ds_write_b128 v6, v[60:63] offset:9216\n"
"ds_write_b128 v6, v[64:67] offset:10240\n"
"ds_write_b128 v6, v[68:71] offset:11264\n"
"ds_write_b128 v6, v[72:75] offset:12288\n"
"ds_write_b128 v6, v[76:79] offset:13312\n"
"ds_write_b128 v6, v[80:83] offset:14336\n"
"ds_write_b128 v6, v[84:87] offset:15360\n"
"s_mov_b32 s65, 0\n"
"Lcgoa%=:\n"
"v_cmp_ne_u32 vcc, s36, v120\n"
"s_cbranch_vccz Lcgob%=\n"
"buffer_load_dwordx2 v[120:121], off, s[40:43], s33 sc1\n"
"s_add_u32 s65, s65, 1\n"
"s_cmp_gt_u32 s65, 131072\n"
"s_cbranch_scc1 Lcend%=\n"
"s_waitcnt vmcnt(0)\n"
"s_branch Lcgoa%=\n"
"Lcgob%=:\n"
"v_readfirstlane_b32 s99, v121\n"
"s_lshl_b32 s33, s99, 13\n"
"s_xor_b32 s54, s54, s33\n"
"s_xor_b32 s45, s45, s33\n"
"s_xor_b32 s22, s22, s33\n"
"s_add_u32 s22, s22, 0x1000\n"
"s_xor_b32 s99, s99, s37\n"
"s_waitcnt lgkmcnt(0)\n"
"s_waitcnt vmcnt(6)\n"
"v_lshrrev_b32 v17, 2, v9\n"
"v_and_b32 v17, 12, v17\n"
"v_xor_b32 v23, 0, v21\n"
"ds_read_b64_tr_b16 v[24:25], v23 offset:16384\n"
"ds_read_b64_tr_b16 v[26:27], v23 offset:16896\n"
"ds_read_b64_tr_b16 v[28:29], v23 offset:24576\n"
"ds_read_b64_tr_b16 v[30:31], v23 offset:25088\n"
"v_xor_b32 v23, 32, v21\n"
"ds_read_b64_tr_b16 v[32:33], v23 offset:16384\n"
"ds_read_b64_tr_b16 v[34:35], v23 offset:16896\n"
"ds_read_b64_tr_b16 v[36:37], v23 offset:24576\n"
"ds_read_b64_tr_b16 v[38:39], v23 offset:25088\n"
"s_waitcnt lgkmcnt(4)\n"
"v_xor_b32 v23, 64, v21\n"
"ds_read_b64_tr_b16 v[40:41], v23 offset:16384\n"
"ds_read_b64_tr_b16 v[42:43], v23 offset:16896\n"
"ds_read_b64_tr_b16 v[44:45], v23 offset:24576\n"
"ds_read_b64_tr_b16 v[46:47], v23 offset:25088\n"
"v_xor_b32 v23, 96, v21\n"
"ds_read_b64_tr_b16 v[48:49], v23 offset:16384\n"
"ds_read_b64_tr_b16 v[50:51], v23 offset:16896\n"
"ds_read_b64_tr_b16 v[52:53], v23 offset:24576\n"
"ds_read_b64_tr_b16 v[54:55], v23 offset:25088\n"
"s_waitcnt lgkmcnt(4)\n"
"v_xor_b32 v23, 128, v21\n"
"ds_read_b64_tr_b16 a[40:41], v23 offset:16384\n"
"ds_read_b64_tr_b16 a[42:43], v23 offset:16896\n"
"ds_read_b64_tr_b16 a[44:45], v23 offset:24576\n"
"ds_read_b64_tr_b16 a[46:47], v23 offset:25088\n"
"v_xor_b32 v23, 160, v21\n"
"ds_read_b64_tr_b16 a[56:57], v23 offset:16384\n"
"ds_read_b64_tr_b16 a[58:59], v23 offset:16896\n"
"ds_read_b64_tr_b16 a[60:61], v23 offset:24576\n"
"ds_read_b64_tr_b16 a[62:63], v23 offset:25088\n"
"s_waitcnt lgkmcnt(4)\n"
"v_xor_b32 v23, 192, v21\n"
"ds_read_b64_tr_b16 a[72:73], v23 offset:16384\n"
"ds_read_b64_tr_b16 a[74:75], v23 offset:16896\n"
"ds_read_b64_tr_b16 a[76:77], v23 offset:24576\n"
"ds_read_b64_tr_b16 a[78:79], v23 offset:25088\n"
"v_xor_b32 v23, 224, v21\n"
"ds_read_b64_tr_b16 a[88:89], v23 offset:16384\n"
"ds_read_b64_tr_b16 a[90:91], v23 offset:16896\n"
"ds_read_b64_tr_b16 a[92:93], v23 offset:24576\n"
"ds_read_b64_tr_b16 a[94:95], v23 offset:25088\n"
"s_waitcnt lgkmcnt(0)\n"
"buffer_load_dword v16, v17, s[40:43], s45 offen sc1\n"
"ds_write_b128 v6, v[88:91] offset:16384\n"
"ds_write_b128 v6, v[92:95] offset:17408\n"
"ds_write_b128 v6, v[96:99] offset:18432\n"
"ds_write_b128 v6, v[100:103] offset:19456\n"
"ds_write_b128 v6, v[104:107] offset:20480\n"
"ds_write_b128 v6, v[108:111] offset:21504\n"
"ds_write_b128 v6, v[112:115] offset:22528\n"
"ds_write_b128 v6, v[116:119] offset:23552\n"
"ds_write_b128 v6, v[24:27] offset:24576\n"
"ds_write_b128 v6, v[28:31] offset:25600\n"
"ds_write_b128 v6, v[32:35] offset:26624\n"
"ds_write_b128 v6, v[36:39] offset:27648\n"
"ds_write_b128 v6, v[40:43] offset:28672\n"
"ds_write_b128 v6, v[44:47] offset:29696\n"
"ds_write_b128 v6, v[48:51] offset:30720\n"
"ds_write_b128 v6, v[52:55] offset:31744\n"
"s_mov_b32 s88, 0x3e0293ee\n"
"s_mov_b32 s89, 0x3e0293ee\n"
"s_lshl_b32 s90, s60, 11\n"
"s_lshl_b32 s69, s60, 9\n"
"s_add_u32 s69, s69, 135168\n"
"s_mov_b32 s38, 332\n"
"s_mov_b32 s93, 0\n"
"s_mov_b32 s20, 0\n"
"s_mov_b32 s64, 0x10000\n"
"s_mov_b32 s44, 0\n"
"s_mov_b32 s70, 0\n"
"s_mov_b32 s71, 0\n"
"v_mov_b32 v12, -1\n"
"v_lshrrev_b32 v13, 2, v9\n"
"s_mov_b32 s72, s51\n"
"s_mov_b32 s73, s48\n"
"s_bfe_u32 s51, s72, 0x10015\n"
"s_xor_b32 s51, s51, s99\n"
"s_lshl_b32 s51, s51, 13\n"
"s_add_u32 s51, s51, s72\n"
"s_mov_b32 s48, s17\n"
"s_waitcnt vmcnt(0) lgkmcnt(0)\n"
"ds_read_b128 v[72:75], v6 offset:0\n"
"ds_read_b128 v[76:79], v6 offset:1024\n"
"ds_read_b128 v[80:83], v6 offset:2048\n"
"ds_read_b128 v[84:87], v6 offset:3072\n"
"ds_read_b128 v[88:91], v6 offset:4096\n"
"ds_read_b128 v[92:95], v6 offset:5120\n"
"ds_read_b128 v[96:99], v6 offset:6144\n"
"ds_read_b128 v[100:103], v6 offset:7168\n"
"ds_read_b128 v[104:107], v6 offset:8192\n"
"ds_read_b128 v[108:111], v6 offset:9216\n"
"ds_read_b128 v[112:115], v6 offset:10240\n"
"ds_read_b128 v[116:119], v6 offset:11264\n"
"ds_read_b128 v[120:123], v6 offset:12288\n"
"ds_read_b128 v[124:127], v6 offset:13312\n"
"v_mov_b64 v[48:49], 0\n"
"v_mov_b64 v[50:51], 0\n"
"v_mov_b64 v[52:53], 0\n"
"v_mov_b64 v[54:55], 0\n"
"v_mov_b64 v[56:57], 0\n"
"v_mov_b64 v[58:59], 0\n"
"v_mov_b64 v[60:61], 0\n"
"v_mov_b64 v[62:63], 0\n"
"v_mov_b64 v[64:65], 0\n"
"v_mov_b64 v[66:67], 0\n"
"v_mov_b64 v[68:69], 0\n"
"v_mov_b64 v[70:71], 0\n"
"s_mov_b32 s65, 0\n"
"Lckva%=:\n"
"v_cmp_gt_i32 vcc, 0, v16\n"
"s_cbranch_vccz Lckvb%=\n"
"s_sleep 1\n"
"buffer_load_dword v16, v17, s[40:43], s45 offen sc1\n"
"s_add_u32 s65, s65, 1\n"
"s_cmp_gt_u32 s65, 131072\n"
"s_cbranch_scc1 Lcend%=\n"
"s_waitcnt vmcnt(0)\n"
"s_branch Lckva%=\n"
"Lckvb%=:\n"
"s_mov_b32 s65, 0\n"
"s_add_u32 s67, s46, s93\n"
"s_branch Lcloop%=\n"
".p2align 7\n"
"s_nop 0\n"
"s_nop 1\n"
"s_nop 1\n"
"s_nop 1\n"
"s_nop 1\n"
"s_nop 1\n"
"s_nop 1\n"
"s_nop 1\n"
"s_nop 1\n"
"s_nop 1\n"
"s_nop 1\n"
"s_nop 1\n"
"s_nop 1\n"
"s_nop 1\n"
"s_nop 1\n"
"s_nop 1\n"
"s_nop 1\n"
"s_nop 1\n"
"s_nop 1\n"
"s_nop 1\n"
"s_nop 1\n"
"s_nop 1\n"
"s_nop 1\n"
"s_nop 1\n"
"s_nop 1\n"
"s_nop 1\n"
"s_nop 1\n"
"s_nop 1\n"
"s_nop 1\n"
"s_nop 1\n"
"s_nop 1\n"
"s_nop 1\n"
"s_nop 1\n"
"s_nop 1\n"
"s_nop 1\n"
"s_nop 1\n"
"s_nop 1\n"
"s_nop 1\n"
"s_nop 1\n"
"s_nop 1\n"
"s_nop 1\n"
"s_nop 1\n"
"s_nop 1\n"
"s_nop 1\n"
"s_nop 1\n"
"s_nop 1\n"
"s_nop 1\n"
"s_nop 1\n"
"s_nop 1\n"
"s_nop 1\n"
"s_nop 1\n"
"s_nop 1\n"
"s_nop 1\n"
"s_nop 1\n"
"s_nop 1\n"
"s_nop 1\n"
"s_nop 1\n"
"s_nop 1\n"
"s_nop 1\n"
"s_nop 1\n"
"s_nop 1\n"
"s_nop 1\n"
"s_nop 1\n"
"s_nop 1\n"
"s_nop 1\n"
"Lcloop%=:\n"
"s_barrier\n"
"buffer_load_dwordx4 v[16:19], v1, s[40:43], s67 offen sc1\n"
"buffer_load_dwordx4 v[20:23], v1, s[40:43], s67 offen offset:128 sc1\n"
"v_add_u32 v24, s90, v10\n"
"s_add_u32 s94, s93, 0x30000\n"
"s_and_b32 s94, s94, 0x3ffff\n"
"s_add_u32 s94, s94, s49\n"
"s_add_u32 s92, s58, s93\n"
"s_add_u32 s95, s59, s93\n"
"s_sub_u32 s66, s44, 1\n"
"v_mov_b32 v14, s66\n"
"s_bitcmp1_b32 s60, 0\n"
"s_cbranch_scc0 Lnf%=\n"
"s_waitcnt vmcnt(2)\n"
"Lnf%=:\n"
"buffer_store_dword v14, v13, s[40:43], s54 offen sc1\n"
"s_sleep 2\n"
"s_mov_b32 m0, s52\n"
"s_add_u32 s91, s51, 33554432\n"
"buffer_load_dwordx4 v9, s[40:43], s51 offen sc1 lds\n"
"buffer_load_dwordx4 v9, s[40:43], s51 offen offset:1024 sc1 lds\n"
"s_sleep 2\n"
"s_add_u32 m0, s52, 8192\n"
"s_nop 0\n"
"buffer_load_dwordx4 v9, s[40:43], s91 offen sc1 lds\n"
"buffer_load_dwordx4 v9, s[40:43], s91 offen offset:1024 sc1 lds\n"
"s_add_u32 s23, s51, s16\n"
"s_add_u32 s72, s72, 0x80000\n"
"s_lshr_b32 s21, s44, 2\n"
"s_and_b32 s50, s21, 1\n"
"s_lshl_b32 s50, s50, 16\n"
"s_xor_b32 s47, s50, 0x10000\n"
"s_and_b32 s20, s44, 3\n"
"s_lshl_b32 s20, s20, 19\n"
"s_and_b32 s66, s44, 0\n"
"s_lshl_b32 s66, s66, 20\n"
"s_add_u32 s20, s20, s66\n"
"s_add_u32 s20, s20, 8388608\n"
"s_add_u32 s64, s20, 0x10000\n"
"s_bfe_u32 s51, s72, 0x10015\n"
"s_xor_b32 s51, s51, s99\n"
"s_lshl_b32 s51, s51, 13\n"
"s_add_u32 s51, s51, s72\n"
"s_bfe_u32 s48, s73, 0x10015\n"
"s_xor_b32 s48, s48, s99\n"
"s_lshl_b32 s48, s48, 13\n"
"s_add_u32 s48, s48, s73\n"
"s_xor_b32 s48, s48, 8192\n"
"s_add_u32 s48, s23, 0\n"
"v_xor_b32 v31, 0x800, v24\n"
"v_xor_b32 v39, 0x1000, v24\n"
"v_xor_b32 v47, 0x1800, v24\n"
"s_waitcnt vmcnt(5) lgkmcnt(0)\n"
"Lp2%=:\n"
"ds_write_b128 v24, v[16:19]\n"
"ds_write_b128 v24, v[20:23] offset:1024\n"
"v_smfmac_f32_16x16x64_bf16 v[48:51], v[16:19], v[72:79], v0\n"
"v_max3_u32 v25, v16, v17, v18\n"
"v_smfmac_f32_16x16x64_bf16 v[52:55], v[16:19], v[80:87], v0\n"
"v_max3_u32 v25, v25, v19, v20\n"
"v_smfmac_f32_16x16x64_bf16 v[56:59], v[16:19], v[88:95], v0\n"
"v_max3_u32 v25, v25, v21, v22\n"
"v_smfmac_f32_16x16x64_bf16 v[60:63], v[16:19], v[96:103], v0\n"
"v_max_u32 v25, v25, v23\n"
"v_smfmac_f32_16x16x64_bf16 v[64:67], v[16:19], v[104:111], v0\n"
"v_cmp_eq_u32 vcc, -1, v25\n"
"v_smfmac_f32_16x16x64_bf16 v[68:71], v[16:19], v[112:119], v0\n"
"s_cbranch_vccnz Lfail2%=\n"
"v_smfmac_f32_16x16x64_bf16 v[48:51], v[20:23], v[120:127], v0\n"
"s_waitcnt lgkmcnt(0)\n"
"s_barrier\n"
"ds_read_b128 v[24:27], v31\n"
"v_smfmac_f32_16x16x64_bf16 v[52:55], v[20:23], a[96:103], v0\n"
"s_memrealtime s[74:75]\n"
"ds_read_b128 v[28:31], v31 offset:1024\n"
"v_smfmac_f32_16x16x64_bf16 v[56:59], v[20:23], a[200:207], v0\n"
"ds_read_b128 v[32:35], v39\n"
"s_nop 0\n"
"s_memrealtime s[32:33]\n"
"v_smfmac_f32_16x16x64_bf16 v[60:63], v[20:23], a[208:215], v0\n"
"buffer_load_dword v15, v11, s[40:43], s22 offen sc1\n"
"ds_read_b128 v[36:39], v39 offset:1024\n"
"v_smfmac_f32_16x16x64_bf16 v[64:67], v[20:23], v[144:151], v0\n"
"s_memrealtime s[36:37]\n"
"ds_read_b128 v[40:43], v47\n"
"v_smfmac_f32_16x16x64_bf16 v[68:71], v[20:23], v[152:159], v0\n"
"s_waitcnt lgkmcnt(4)\n"
"ds_read_b128 v[44:47], v47 offset:1024\n"
"s_memrealtime s[62:63]\n"
"v_smfmac_f32_16x16x64_bf16 v[48:51], v[24:27], a[104:111], v0\n"
"buffer_store_dword v12, v13, s[40:43], s94 offen sc1\n"
"v_cmp_gt_u32 vcc, 32, v9\n"
"v_smfmac_f32_16x16x64_bf16 v[52:55], v[24:27], a[112:119], v0\n"
"s_waitcnt lgkmcnt(4)\n"
"v_lshlrev_b32 v81, 10, v9\n"
"v_smfmac_f32_16x16x64_bf16 v[48:51], v[28:31], a[120:127], v0\n"
"v_cndmask_b32 v81, 0x40000000, v81, vcc\n"
"v_smfmac_f32_16x16x64_bf16 v[52:55], v[28:31], a[128:135], v0\n"
"s_waitcnt lgkmcnt(0)\n"
"s_add_u32 s39, s74, s32\n"
"ds_read_b128 v[72:75], v6 offset:14336\n"
"v_smfmac_f32_16x16x64_bf16 v[48:51], v[32:35], a[136:143], v0\n"
"s_add_u32 s36, s36, s62\n"
"s_add_u32 s39, s39, s36\n"
"ds_read_b128 v[76:79], v6 offset:15360\n"
"v_smfmac_f32_16x16x64_bf16 v[52:55], v[32:35], a[144:151], v0\n"
"s_add_u32 s96, s39, s38\n"
"s_lshl_b32 s97, s96, 8\n"
"ds_read_b128 v[96:99], v6 offset:16384\n"
"v_smfmac_f32_16x16x64_bf16 v[48:51], v[36:39], a[152:159], v0\n"
"s_or_b32 s97, s97, s44\n"
"ds_read_b128 v[100:103], v6 offset:17408\n"
"v_mov_b32 v80, s97\n"
"v_smfmac_f32_16x16x64_bf16 v[52:55], v[36:39], a[160:167], v0\n"
"buffer_store_dword v80, v81, s[40:43], s92 offen sc1\n"
"ds_read_b128 v[88:91], v6 offset:18432\n"
"v_smfmac_f32_16x16x64_bf16 v[48:51], v[40:43], a[168:175], v0\n"
"ds_read_b128 v[92:95], v6 offset:19456\n"
"v_smfmac_f32_16x16x64_bf16 v[52:55], v[40:43], a[176:183], v0\n"
"ds_read_b128 v[112:115], v6 offset:20480\n"
"v_smfmac_f32_16x16x64_bf16 v[48:51], v[44:47], a[184:191], v0\n"
"ds_read_b128 v[116:119], v6 offset:21504\n"
"v_smfmac_f32_16x16x64_bf16 v[52:55], v[44:47], a[192:199], v0\n"
"ds_read_b128 v[120:123], v6 offset:22528\n"
"v_smfmac_f32_16x16x64_bf16 v[56:59], v[24:27], a[216:223], v0\n"
"ds_read_b128 v[124:127], v6 offset:23552\n"
"v_add_f32 v48, v48, v50\n"
"v_smfmac_f32_16x16x64_bf16 v[60:63], v[24:27], a[224:231], v0\n"
"v_add_f32 v49, v49, v51\n"
"v_smfmac_f32_16x16x64_bf16 v[56:59], v[28:31], a[232:239], v0\n"
"v_add_f32 v52, v52, v54\n"
"v_smfmac_f32_16x16x64_bf16 v[60:63], v[28:31], a[240:247], v0\n"
"s_waitcnt lgkmcnt(8)\n"
"v_add_f32 v53, v53, v55\n"
"v_smfmac_f32_16x16x64_bf16 v[56:59], v[32:35], v[72:79], v0\n"
"s_waitcnt lgkmcnt(6)\n"
"v_cvt_pk_bf16_f32 v48, v48, v52\n"
"v_smfmac_f32_16x16x64_bf16 v[60:63], v[32:35], v[96:103], v0\n"
"s_waitcnt lgkmcnt(4)\n"
"v_cvt_pk_bf16_f32 v49, v49, v53\n"
"v_smfmac_f32_16x16x64_bf16 v[56:59], v[36:39], v[88:95], v0\n"
"s_waitcnt lgkmcnt(2)\n"
"ds_write2_b32 v3, v48, v49 offset0:0 offset1:136\n"
"v_smfmac_f32_16x16x64_bf16 v[60:63], v[36:39], v[112:119], v0\n"
"s_waitcnt lgkmcnt(1)\n"
"v_smfmac_f32_16x16x64_bf16 v[56:59], v[40:43], v[120:127], v0\n"
"v_smfmac_f32_16x16x64_bf16 v[60:63], v[40:43], a[248:255], v0\n"
"v_smfmac_f32_16x16x64_bf16 v[56:59], v[44:47], v[128:135], v0\n"
"s_waitcnt vmcnt(5) lgkmcnt(0)\n"
"s_barrier\n"
"ds_read_b128 v[72:75], v2 offset:0\n"
"v_smfmac_f32_16x16x64_bf16 v[60:63], v[44:47], v[136:143], v0\n"
"ds_read_b128 v[96:99], v10 offset:8192\n"
"v_add_u32 v48, s90, v10\n"
"v_smfmac_f32_16x16x64_bf16 v[64:67], v[32:35], v[192:199], v0\n"
"ds_read_b128 v[76:79], v2 offset:64\n"
"v_add_f32 v56, v56, v58\n"
"v_smfmac_f32_16x16x64_bf16 v[68:71], v[32:35], v[200:207], v0\n"
"ds_read_b128 v[100:103], v10 offset:9216\n"
"v_add_f32 v57, v57, v59\n"
"v_smfmac_f32_16x16x64_bf16 v[64:67], v[36:39], v[208:215], v0\n"
"ds_read_b128 v[80:83], v2 offset:128\n"
"v_add_f32 v60, v60, v62\n"
"v_smfmac_f32_16x16x64_bf16 v[68:71], v[36:39], v[216:223], v0\n"
"ds_read_b128 v[104:107], v10 offset:10240\n"
"v_add_f32 v61, v61, v63\n"
"v_smfmac_f32_16x16x64_bf16 v[64:67], v[40:43], v[224:231], v0\n"
"s_waitcnt lgkmcnt(4)\n"
"ds_read_b128 v[84:87], v2 offset:192\n"
"v_cvt_pk_bf16_f32 v56, v56, v60\n"
"v_mfma_f32_16x16x32_bf16 v[88:91], v[96:99], v[72:75], 0\n"
"ds_read_b128 v[108:111], v10 offset:11264\n"
"v_cvt_pk_bf16_f32 v57, v57, v61\n"
"v_smfmac_f32_16x16x64_bf16 v[68:71], v[40:43], v[232:239], v0\n"
"s_waitcnt lgkmcnt(4)\n"
"ds_write2_b32 v3, v56, v57 offset0:68 offset1:204\n"
"v_lshl_add_u32 v49, v13, 1, s69\n"
"v_mfma_f32_16x16x32_bf16 v[88:91], v[100:103], v[76:79], v[88:91]\n"
"ds_read_b128 v[112:115], v10 offset:12288\n"
"v_mov_b32 v56, 0x3f803f80\n"
"v_smfmac_f32_16x16x64_bf16 v[64:67], v[44:47], v[240:247], v0\n"
"s_waitcnt lgkmcnt(4)\n"
"ds_read_b128 v[116:119], v10 offset:13312\n"
"v_mov_b32 v57, v56\n"
"v_mfma_f32_16x16x32_bf16 v[88:91], v[104:107], v[80:83], v[88:91]\n"
"ds_read_b128 v[120:123], v10 offset:14336\n"
"v_mov_b64 v[58:59], v[56:57]\n"
"v_smfmac_f32_16x16x64_bf16 v[68:71], v[44:47], v[248:255], v0\n"
"s_waitcnt lgkmcnt(4)\n"
"ds_read_b128 v[124:127], v10 offset:15360\n"
"v_lshlrev_b32 v14, 3, v9\n"
"v_mfma_f32_16x16x32_bf16 v[88:91], v[108:111], v[84:87], v[88:91]\n"
"s_waitcnt vmcnt(3)\n"
"s_waitcnt lgkmcnt(3)\n"
"v_and_b32 v14, 0xfff, v14\n"
"v_mfma_f32_16x16x32_bf16 v[92:95], v[112:115], v[72:75], 0\n"
"s_barrier\n"
"s_waitcnt lgkmcnt(2)\n"
"ds_read_b128 v[96:99], v2 offset:272\n"
"v_mfma_f32_16x16x32_bf16 v[92:95], v[116:119], v[76:79], v[92:95]\n"
"s_waitcnt lgkmcnt(2)\n"
"ds_read_b128 v[100:103], v2 offset:336\n"
"v_max3_f32 v50, v88, v89, v90\n"
"v_mfma_f32_16x16x32_bf16 v[92:95], v[120:123], v[80:83], v[92:95]\n"
"s_waitcnt lgkmcnt(2)\n"
"ds_read_b128 v[104:107], v2 offset:400\n"
"v_mfma_f32_16x16x32_bf16 v[92:95], v[124:127], v[84:87], v[92:95]\n"
"buffer_load_dword v14, v14, s[40:43], s95 offen sc1\n"
"ds_read_b128 v[108:111], v2 offset:464\n"
"v_smfmac_f32_16x16x64_bf16 v[64:67], v[24:27], v[160:167], v0\n"
"s_waitcnt vmcnt(3)\n"
"ds_read_b128 v[16:19], v48 offset:16384\n"
"v_cmp_gt_i32 vcc, s44, v15\n"
"v_smfmac_f32_16x16x64_bf16 v[68:71], v[24:27], v[168:175], v0\n"
"s_and_b32 s66, vcc_lo, 0xf0\n"
"s_waitcnt lgkmcnt(4)\n"
"ds_read_b128 v[20:23], v48 offset:17408\n"
"v_max3_f32 v50, v50, v91, v92\n"
"v_mfma_f32_16x16x32_bf16 v[52:55], v[96:99], v[72:75], 0\n"
"s_waitcnt lgkmcnt(4)\n"
"ds_read_b128 v[112:115], v6 offset:28672\n"
"v_max3_f32 v50, v50, v93, v94\n"
"v_mfma_f32_16x16x32_bf16 v[52:55], v[100:103], v[76:79], v[52:55]\n"
"s_waitcnt lgkmcnt(4)\n"
"ds_read_b128 v[116:119], v6 offset:29696\n"
"v_max_f32 v50, v50, v95\n"
"v_mfma_f32_16x16x32_bf16 v[52:55], v[104:107], v[80:83], v[52:55]\n"
"s_cselect_b32 s66, s17, 0\n"
"s_waitcnt lgkmcnt(4)\n"
"ds_read_b128 v[120:123], v6 offset:30720\n"
"v_cndmask_b32 v88, v92, v88, s[56:57]\n"
"v_mfma_f32_16x16x32_bf16 v[52:55], v[108:111], v[84:87], v[52:55]\n"
"s_or_b32 s23, s23, s66\n"
"ds_read_b128 v[124:127], v6 offset:31744\n"
"v_cndmask_b32 v89, v93, v89, s[56:57]\n"
"v_smfmac_f32_16x16x64_bf16 v[64:67], v[28:31], v[176:183], v0\n"
"ds_read_b128 v[96:99], v6 offset:24576\n"
"v_cndmask_b32 v90, v94, v90, s[56:57]\n"
"v_smfmac_f32_16x16x64_bf16 v[68:71], v[28:31], v[184:191], v0\n"
"ds_read_b128 v[100:103], v6 offset:25600\n"
"v_max3_f32 v51, v50, v52, v53\n"
"v_max3_f32 v50, v50, v52, v53\n"
"v_cndmask_b32 v54, v53, v52, s[56:57]\n"
"v_cndmask_b32 v91, v95, v91, s[56:57]\n"
"v_permlane16_swap_b32 v50, v51\n"
"v_max_f32 v50, v50, v51\n"
"v_mov_b32 v51, v50\n"
"v_add_f32 v64, v64, v66\n"
"v_add_f32 v65, v65, v67\n"
"v_permlane32_swap_b32 v50, v51\n"
"v_max_f32 v50, v50, v51\n"
"v_mul_f32 v50, s88, v50\n"
"v_fma_f32 v54, v54, s88, -v50\n"
"v_exp_f32 v54, v54\n"
"v_pk_fma_f32 v[88:89], v[88:89], s[88:89], v[50:51] op_sel_hi:[1,1,0] neg_lo:[0,0,1] neg_hi:[0,0,1]\n"
"v_add_f32 v68, v68, v70\n"
"v_pk_fma_f32 v[90:91], v[90:91], s[88:89], v[50:51] op_sel_hi:[1,1,0] neg_lo:[0,0,1] neg_hi:[0,0,1]\n"
"v_add_f32 v69, v69, v71\n"
"v_mov_b32_dpp v55, v54 quad_perm:[2,3,2,3] row_mask:0xf bank_mask:0xf\n"
"v_mov_b32 v53, 0\n"
"v_cvt_pk_bf16_f32 v52, v54, v55\n"
"v_cvt_pk_bf16_f32 v66, v64, v65\n"
"v_cvt_pk_bf16_f32 v67, v68, v69\n"
"v_mfma_f32_16x16x16_bf16 v[60:63], v[56:57], v[52:53], 0\n"
"v_exp_f32 v88, v88\n"
"v_mfma_f32_16x16x16_bf16 v[72:75], v[66:67], v[52:53], 0\n"
"v_cvt_pk_bf16_f32 v70, v68, v69\n"
"v_mov_b32 v71, 0\n"
"v_exp_f32 v89, v89\n"
"v_exp_f32 v90, v90\n"
"v_mfma_f32_16x16x16_bf16 v[76:79], v[70:71], v[52:53], 0\n"
"s_waitcnt lgkmcnt(6)\n"
"v_exp_f32 v91, v91\n"
"v_cvt_pk_bf16_f32 v92, v88, v89\n"
"v_cvt_pk_bf16_f32 v93, v90, v91\n"
"v_mov_b64 v[84:85], 0\n"
"v_mov_b32_dpp v94, v92 quad_perm:[2,3,2,3] row_mask:0xf bank_mask:0xf\n"
"v_mov_b32_dpp v95, v93 quad_perm:[2,3,2,3] row_mask:0xf bank_mask:0xf\n"
"v_mov_b64 v[86:87], 0\n"
"s_nop 0\n"
"v_mfma_f32_16x16x32_bf16 v[60:63], v[56:59], v[92:95], v[60:63]\n"
"ds_read_b128 v[104:107], v6 offset:26624\n"
"ds_read_b128 v[108:111], v6 offset:27648\n"
"v_mfma_f32_16x16x32_bf16 v[72:75], v[16:19], v[92:95], v[72:75]\n"
"ds_write_b64 v49, v[66:67]\n"
"v_mov_b64 v[88:89], 0\n"
"v_mov_b32 v68, 0xfffe0000\n"
"v_mfma_f32_16x16x32_bf16 v[76:79], v[20:23], v[92:95], v[76:79]\n"
"s_bitcmp1_b32 s60, 0\n"
"v_rcp_f32 v50, v60\n"
"v_mov_b64 v[90:91], 0\n"
"v_mov_b64 v[92:93], 0\n"
"s_mov_b64 exec, s[56:57]\n"
"v_pk_mul_f32 v[72:73], v[72:73], v[50:51] op_sel_hi:[1,0]\n"
"v_cvt_pk_bf16_f32 v80, v72, v73\n"
"v_pk_mul_f32 v[74:75], v[74:75], v[50:51] op_sel_hi:[1,0]\n"
"v_cvt_pk_bf16_f32 v81, v74, v75\n"
"v_pk_mul_f32 v[76:77], v[76:77], v[50:51] op_sel_hi:[1,0]\n"
"v_cvt_pk_bf16_f32 v82, v76, v77\n"
"v_pk_mul_f32 v[78:79], v[78:79], v[50:51] op_sel_hi:[1,0]\n"
"v_cvt_pk_bf16_f32 v83, v78, v79\n"
"v_mfma_f32_32x32x16_bf16 v[48:63], v[84:87], v[84:87], 0\n"
"ds_write_b128 v4, v[80:83]\n"
"s_mov_b64 exec, -1\n"
"s_cbranch_scc0 Lnq%=\n"
"buffer_load_dwordx4 v[32:35], v9, s[40:43], s23 offen offset:0 sc1\n"
"buffer_load_dwordx4 v[36:39], v9, s[40:43], s23 offen offset:1024 sc1\n"
"buffer_load_dwordx4 v[40:43], v9, s[40:43], s23 offen offset:2048 sc1\n"
"buffer_load_dwordx4 v[44:47], v9, s[40:43], s23 offen offset:3072 sc1\n"
"Lnq%=:\n"
"s_waitcnt lgkmcnt(0)\n"
"s_barrier\n"
"ds_read_b128 v[72:75], v5\n"
"ds_read_b128 v[76:79], v5 offset:1024\n"
"v_mov_b64 v[94:95], 0\n"
"v_mov_b64 v[64:65], 0\n"
"v_mov_b64 v[66:67], 0\n"
"s_cmp_eq_u32 s44, 63\n"
"s_cbranch_scc1 Lclast%=\n"
"s_add_u32 s73, s73, 0x80000\n"
"s_add_u32 s93, s93, 0x10000\n"
"s_and_b32 s93, s93, 0x3ffff\n"
"s_add_u32 s67, s93, s53\n"
"s_bitcmp1_b32 s60, 0\n"
"s_waitcnt lgkmcnt(1)\n"
"v_smfmac_f32_16x16x64_bf16 v[48:51], v[72:75], a[0:7], v0\n"
"v_smfmac_f32_16x16x64_bf16 v[52:55], v[72:75], a[8:15], v0\n"
"s_waitcnt lgkmcnt(0)\n"
"ds_read_b128 v[16:19], v2 offset:272\n"
"v_smfmac_f32_16x16x64_bf16 v[48:51], v[76:79], v[96:103], v0\n"
"ds_read_b128 v[20:23], v2 offset:336\n"
"v_smfmac_f32_16x16x64_bf16 v[52:55], v[76:79], v[104:111], v0\n"
"ds_read_b128 v[24:27], v2 offset:400\n"
"v_smfmac_f32_16x16x64_bf16 v[56:59], v[72:75], a[16:23], v0\n"
"ds_read_b128 v[28:31], v2 offset:464\n"
"v_add_f32 v48, v48, v50\n"
"v_add_f32 v49, v49, v51\n"
"v_smfmac_f32_16x16x64_bf16 v[60:63], v[72:75], a[24:31], v0\n"
"ds_read_b128 v[96:99], v10 offset:8192\n"
"v_add_u32 v48, s47, v48\n"
"v_bfi_b32 v48, v68, v48, s50\n"
"v_smfmac_f32_16x16x64_bf16 v[56:59], v[76:79], v[112:119], v0\n"
"ds_read_b128 v[100:103], v10 offset:9216\n"
"v_add_f32 v52, v52, v54\n"
"v_add_f32 v53, v53, v55\n"
"v_smfmac_f32_16x16x64_bf16 v[60:63], v[76:79], v[120:127], v0\n"
"ds_read_b128 v[104:107], v10 offset:10240\n"
"v_cvt_pk_bf16_f32 v80, v48, v52\n"
"v_cvt_pk_bf16_f32 v82, v49, v53\n"
"v_smfmac_f32_16x16x64_bf16 v[84:87], v[72:75], a[32:39], v0\n"
"ds_read_b128 v[108:111], v10 offset:11264\n"
"v_add_f32 v56, v56, v58\n"
"v_add_f32 v57, v57, v59\n"
"v_smfmac_f32_16x16x64_bf16 v[88:91], v[72:75], a[48:55], v0\n"
"ds_read_b128 v[112:115], v10 offset:12288\n"
"v_add_f32 v60, v60, v62\n"
"v_add_f32 v61, v61, v63\n"
"v_smfmac_f32_16x16x64_bf16 v[84:87], v[76:79], a[40:47], v0\n"
"ds_read_b128 v[116:119], v10 offset:13312\n"
"v_cvt_pk_bf16_f32 v81, v56, v60\n"
"v_cvt_pk_bf16_f32 v83, v57, v61\n"
"v_smfmac_f32_16x16x64_bf16 v[88:91], v[76:79], a[56:63], v0\n"
"buffer_store_dwordx4 v[80:83], v7, s[40:43], s20 offen sc1\n"
"v_smfmac_f32_16x16x64_bf16 v[92:95], v[72:75], a[64:71], v0\n"
"ds_read_b128 v[120:123], v10 offset:14336\n"
"v_add_f32 v84, v84, v86\n"
"v_smfmac_f32_16x16x64_bf16 v[64:67], v[72:75], a[80:87], v0\n"
"ds_read_b128 v[124:127], v10 offset:15360\n"
"v_add_f32 v85, v85, v87\n"
"v_smfmac_f32_16x16x64_bf16 v[92:95], v[76:79], a[72:79], v0\n"
"v_add_u32 v84, s47, v84\n"
"v_smfmac_f32_16x16x64_bf16 v[64:67], v[76:79], a[88:95], v0\n"
"v_bfi_b32 v84, v68, v84, s50\n"
"v_add_f32 v88, v88, v90\n"
"v_add_f32 v89, v89, v91\n"
"v_cvt_pk_bf16_f32 v48, v84, v88\n"
"v_cvt_pk_bf16_f32 v50, v85, v89\n"
"v_pk_add_f32 v[92:93], v[92:93], v[94:95]\n"
"v_pk_add_f32 v[64:65], v[64:65], v[66:67]\n"
"v_cvt_pk_bf16_f32 v49, v92, v64\n"
"v_cvt_pk_bf16_f32 v51, v93, v65\n"
"s_waitcnt lgkmcnt(0)\n"
"buffer_store_dwordx4 v[48:51], v7, s[40:43], s64 offen sc1\n"
"s_cbranch_scc1 Lhouse%=\n"
"s_waitcnt vmcnt(2)\n"
"Lan%=:\n"
"s_memrealtime s[74:75]\n"
"v_and_b32 v69, 0xff, v14\n"
"v_subrev_u32 v14, s97, v14\n"
"v_cmp_ne_u32 vcc, s44, v69\n"
"s_cbranch_vccnz Lanfail%=\n"
"v_max_i32_dpp v14, v14, v14 row_shr:1 row_mask:0xf bank_mask:0xf\n"
"s_memrealtime s[32:33]\n"
"s_nop 0\n"
"v_max_i32_dpp v14, v14, v14 row_shr:2 row_mask:0xf bank_mask:0xf\n"
"s_nop 1\n"
"v_max_i32_dpp v14, v14, v14 row_shr:4 row_mask:0xf bank_mask:0xf\n"
"s_memrealtime s[36:37]\n"
"s_nop 0\n"
"v_max_i32_dpp v14, v14, v14 row_shr:8 row_mask:0xf bank_mask:0xf\n"
"s_nop 1\n"
"v_max_i32_dpp v14, v14, v14 row_bcast:15 row_mask:0xf bank_mask:0xf\n"
"s_memrealtime s[62:63]\n"
"v_readlane_b32 s98, v14, 31\n"
"s_add_u32 s19, s96, s18\n"
"s_ashr_i32 s98, s98, 8\n"
"s_min_i32 s98, s98, 0x10000\n"
"s_add_u32 s19, s19, s98\n"
"s_waitcnt lgkmcnt(0)\n"
"s_add_u32 s66, s74, s32\n"
"s_add_u32 s36, s36, s62\n"
"s_add_u32 s66, s66, s36\n"
"s_sub_u32 s98, s66, s96\n"
"Lsl2%=:\n"
"s_sub_u32 s96, s66, s19\n"
"s_getpc_b64 s[62:63]\n"
"Ljb%=:\n"
"s_min_i32 s66, s96, 0\n"
"s_mul_i32 s66, s66, 11\n"
"s_ashr_i32 s66, s66, 4\n"
"s_add_u32 s66, s66, 96\n"
"s_cmp_gt_u32 s66, 96\n"
"s_cbranch_scc1 Lslow%=\n"
"s_lshl2_add_u32 s91, s66, Lp1%=-Ljb%=-384\n"
"s_add_u32 s62, s62, s91\n"
"s_addc_u32 s63, s63, 0\n"
"s_setpc_b64 s[62:63]\n"
"s_nop 1\n"
"s_nop 1\n"
"s_nop 1\n"
"s_nop 1\n"
"s_nop 1\n"
"s_nop 1\n"
"s_nop 1\n"
"s_nop 1\n"
"s_nop 1\n"
"s_nop 1\n"
"s_nop 1\n"
"s_nop 1\n"
"s_nop 1\n"
"s_nop 1\n"
"s_nop 1\n"
"s_nop 1\n"
"s_nop 1\n"
"s_nop 1\n"
"s_nop 1\n"
"s_nop 1\n"
"s_nop 1\n"
"s_nop 1\n"
"s_nop 1\n"
"s_nop 1\n"
"s_nop 1\n"
"s_nop 1\n"
"s_nop 1\n"
"s_nop 1\n"
"s_nop 1\n"
"s_nop 1\n"
"s_nop 1\n"
"s_nop 1\n"
"s_nop 1\n"
"s_nop 1\n"
"s_nop 1\n"
"s_nop 1\n"
"s_nop 1\n"
"s_nop 1\n"
"s_nop 1\n"
"s_nop 1\n"
"s_nop 1\n"
"s_nop 1\n"
"s_nop 1\n"
"s_nop 1\n"
"s_nop 1\n"
"s_nop 1\n"
"s_nop 1\n"
"s_nop 1\n"
"s_nop 1\n"
"s_nop 1\n"
"s_nop 1\n"
"s_nop 1\n"
"s_nop 1\n"
"s_nop 1\n"
"s_nop 1\n"
"s_nop 1\n"
"s_nop 1\n"
"s_nop 1\n"
"s_nop 1\n"
"s_nop 1\n"
"s_nop 1\n"
"s_nop 1\n"
"s_nop 1\n"
"s_nop 1\n"
"s_nop 1\n"
"s_nop 1\n"
"s_nop 1\n"
"s_nop 1\n"
"s_nop 1\n"
"s_nop 1\n"
"s_nop 1\n"
"s_nop 1\n"
"s_nop 1\n"
"s_nop 1\n"
"s_nop 1\n"
"s_nop 1\n"
"s_nop 1\n"
"s_nop 1\n"
"s_nop 1\n"
"s_nop 1\n"
"s_nop 1\n"
"s_nop 1\n"
"s_nop 1\n"
"s_nop 1\n"
"s_nop 1\n"
"s_nop 1\n"
"s_nop 1\n"
"s_nop 1\n"
"s_nop 1\n"
"s_nop 1\n"
"s_nop 1\n"
"s_nop 1\n"
"s_nop 1\n"
"s_nop 1\n"
"s_nop 1\n"
"s_nop 1\n"
"Lp1%=:\n"
"buffer_load_dwordx4 v[16:19], v8, s[40:43], s20 offen offset:0 sc1\n"
"buffer_load_dwordx4 v[20:23], v8, s[40:43], s20 offen offset:1024 sc1\n"
"buffer_load_dwordx4 v[24:27], v8, s[40:43], s20 offen offset:2048 sc1\n"
"buffer_load_dwordx4 v[28:31], v8, s[40:43], s20 offen offset:3072 sc1\n"
"s_barrier\n"
"ds_read_b128 v[56:59], v10 offset:29184\n"
"ds_read_b128 v[72:75], v6 offset:0\n"
"ds_read_b128 v[76:79], v6 offset:1024\n"
"ds_read_b128 v[80:83], v6 offset:2048\n"
"ds_read_b128 v[84:87], v6 offset:3072\n"
"ds_read_b128 v[88:91], v6 offset:4096\n"
"ds_read_b128 v[92:95], v6 offset:5120\n"
"ds_read_b128 v[96:99], v6 offset:6144\n"
"ds_read_b128 v[100:103], v6 offset:7168\n"
"ds_read_b128 v[104:107], v6 offset:8192\n"
"ds_read_b128 v[108:111], v6 offset:9216\n"
"ds_read_b128 v[112:115], v6 offset:10240\n"
"ds_read_b128 v[116:119], v6 offset:11264\n"
"ds_read_b128 v[120:123], v6 offset:12288\n"
"ds_read_b128 v[124:127], v6 offset:13312\n"
"s_min_i32 s98, s98, 12\n"
"s_max_i32 s98, s98, -24\n"
"s_add_u32 s98, s98, 2\n"
"s_ashr_i32 s98, s98, 2\n"
"s_add_u32 s38, s38, s98\n"
"s_max_i32 s96, s96, 0\n"
"s_add_u32 s96, s96, s19\n"
"s_sub_u32 s96, s96, s18\n"
"s_add_u32 s96, s96, 248\n"
"s_add_u32 s66, s67, 0x8000\n"
"s_mov_b32 s65, 0\n"
"s_waitcnt lgkmcnt(0)\n"
"s_waitcnt vmcnt(3)\n"
"v_mfma_f32_16x16x32_bf16 v[48:51], v[16:19], v[56:59], 0\n"
"v_xor_b32 v52, s21, v16\n"
"s_waitcnt vmcnt(2)\n"
"v_mfma_f32_16x16x32_bf16 v[48:51], v[20:23], v[56:59], v[48:51]\n"
"v_bitop3_b32 v52, v52, v20, s21 bitop3:0xf6\n"
"s_waitcnt vmcnt(1)\n"
"v_mfma_f32_16x16x32_bf16 v[48:51], v[24:27], v[56:59], v[48:51]\n"
"v_bitop3_b32 v52, v52, v24, s21 bitop3:0xf6\n"
"s_waitcnt vmcnt(0)\n"
"v_mfma_f32_16x16x32_bf16 v[48:51], v[28:31], v[56:59], v[48:51]\n"
"v_bitop3_b32 v52, v52, v28, s21 bitop3:0xf6\n"
"v_and_b32 v52, 1, v52\n"
"v_cmp_ne_u32 vcc, 0, v52\n"
"s_cbranch_vccnz Lp1r%=\n"
"Lpub%=:\n"
"s_nop 3\n"
"v_permlane32_swap_b32 v48, v50\n"
"v_permlane32_swap_b32 v49, v51\n"
"v_pk_add_f32 v[48:49], v[48:49], v[50:51]\n"
"v_cvt_pk_bf16_f32 v48, v48, v49\n"
"buffer_store_dword v48, v11, s[40:43], s67 offen sc1\n"
"s_add_u32 s44, s44, 1\n"
"s_add_u32 s67, s46, s93\n"
"s_mov_b32 s65, 0\n"
"Lw2%=:\n"
"s_memrealtime s[74:75]\n"
"v_mov_b64 v[64:65], 0\n"
"v_mov_b64 v[66:67], 0\n"
"s_nop 1\n"
"s_memrealtime s[32:33]\n"
"v_mov_b64 v[68:69], 0\n"
"v_mov_b64 v[70:71], 0\n"
"s_nop 1\n"
"s_memrealtime s[36:37]\n"
"v_mfma_f32_32x32x16_bf16 v[48:63], v[64:67], v[64:67], 0\n"
"s_nop 3\n"
"s_memrealtime s[62:63]\n"
"s_waitcnt lgkmcnt(0)\n"
"s_add_u32 s66, s74, s32\n"
"s_add_u32 s36, s36, s62\n"
"s_add_u32 s66, s66, s36\n"
"s_sub_u32 s66, s66, s96\n"
"s_min_i32 s66, s66, 0\n"
"s_mul_i32 s66, s66, 11\n"
"s_ashr_i32 s66, s66, 4\n"
"s_add_u32 s66, s66, 64\n"
"s_cmp_gt_u32 s66, 64\n"
"s_cbranch_scc1 Lw2%=\n"
"s_lshl2_add_u32 s66, s66, Lcloop%=-Ljc%=-256\n"
"s_getpc_b64 s[62:63]\n"
"Ljc%=:\n"
"s_add_u32 s62, s62, s66\n"
"s_addc_u32 s63, s63, -1\n"
"s_setpc_b64 s[62:63]\n"
"Lhouse%=:\n"
"s_cmp_lt_i32 s23, 0\n"
"v_mov_b32 v92, 0xf149f2ca\n"
"s_cbranch_scc1 Lqslow%=\n"
"Lqok%=:\n"
"s_waitcnt vmcnt(2)\n"
"v_mfma_f32_16x16x32_bf16 v[88:91], v[16:19], v[32:35], 0\n"
"s_mov_b32 s66, 135168\n"
"v_cndmask_b32 v80, 0, v92, s[0:1]\n"
"v_cndmask_b32 v81, 0, v92, s[2:3]\n"
"v_mfma_f32_16x16x32_bf16 v[88:91], v[20:23], v[36:39], v[88:91]\n"
"v_cndmask_b32 v82, 0, v92, s[4:5]\n"
"v_cndmask_b32 v83, 0, v92, s[6:7]\n"
"v_mfma_f32_16x16x32_bf16 v[88:91], v[24:27], v[40:43], v[88:91]\n"
"ds_read_b128 v[64:67], v10 offset:16384\n"
"v_cndmask_b32 v84, 0, v92, s[8:9]\n"
"v_mfma_f32_16x16x32_bf16 v[88:91], v[28:31], v[44:47], v[88:91]\n"
"ds_read_b128 v[68:71], v10 offset:17408\n"
"v_cndmask_b32 v85, 0, v92, s[10:11]\n"
"v_mfma_f32_16x16x32_bf16 v[80:83], v[96:99], v[32:35], v[80:83]\n"
"ds_read_b128 v[72:75], v10 offset:18432\n"
"v_cndmask_b32 v86, 0, v92, s[12:13]\n"
"v_mfma_f32_16x16x32_bf16 v[80:83], v[100:103], v[36:39], v[80:83]\n"
"ds_read_b128 v[76:79], v10 offset:19456\n"
"v_lshl_add_u32 v93, v13, 1, s66\n"
"v_mfma_f32_16x16x32_bf16 v[80:83], v[104:107], v[40:43], v[80:83]\n"
"ds_read_b128 v[48:51], v10 offset:20480\n"
"v_cndmask_b32 v87, 0, v92, s[14:15]\n"
"v_mfma_f32_16x16x32_bf16 v[80:83], v[108:111], v[44:47], v[80:83]\n"
"ds_read2st64_b64 v[16:19], v93 offset0:0 offset1:1\n"
"v_mov_b32 v24, 0x3f803f80\n"
"v_mfma_f32_16x16x32_bf16 v[84:87], v[112:115], v[32:35], v[84:87]\n"
"ds_read2st64_b64 v[20:23], v93 offset0:2 offset1:3\n"
"v_cmp_ge_i32 vcc, s44, v15\n"
"v_mfma_f32_16x16x32_bf16 v[84:87], v[116:119], v[36:39], v[84:87]\n"
"ds_read_b128 v[52:55], v10 offset:21504\n"
"v_max3_f32 v94, v90, v91, v80\n"
"v_mfma_f32_16x16x32_bf16 v[84:87], v[120:123], v[40:43], v[84:87]\n"
"ds_read_b128 v[56:59], v10 offset:22528\n"
"v_max3_f32 v94, v94, v81, v82\n"
"v_mfma_f32_16x16x32_bf16 v[84:87], v[124:127], v[44:47], v[84:87]\n"
"ds_read_b128 v[60:63], v10 offset:23552\n"
"v_mov_b32 v25, v24\n"
"v_mov_b64 v[26:27], v[24:25]\n"
"v_mov_b32 v37, 0\n"
"v_mov_b32 v38, 0\n"
"s_nop 1\n"
"v_max_f32 v94, v94, v83\n"
"v_max3_f32 v94, v94, v84, v85\n"
"v_max3_f32 v95, v94, v86, v87\n"
"v_max3_f32 v94, v94, v86, v87\n"
"s_nop 1\n"
"v_permlane16_swap_b32 v94, v95\n"
"v_max_f32 v94, v94, v95\n"
"v_mov_b32 v95, v94\n"
"s_nop 1\n"
"v_permlane32_swap_b32 v94, v95\n"
"v_max_f32 v94, v94, v95\n"
"v_mul_f32 v94, s88, v94\n"
"v_pk_fma_f32 v[90:91], v[90:91], s[88:89], v[94:95] op_sel_hi:[1,1,0] neg_lo:[0,0,1] neg_hi:[0,0,1]\n"
"v_exp_f32 v90, v90\n"
"v_pk_fma_f32 v[80:81], v[80:81], s[88:89], v[94:95] op_sel_hi:[1,1,0] neg_lo:[0,0,1] neg_hi:[0,0,1]\n"
"v_exp_f32 v91, v91\n"
"v_pk_fma_f32 v[82:83], v[82:83], s[88:89], v[94:95] op_sel_hi:[1,1,0] neg_lo:[0,0,1] neg_hi:[0,0,1]\n"
"v_cvt_pk_bf16_f32 v36, v90, v91\n"
"v_pk_fma_f32 v[84:85], v[84:85], s[88:89], v[94:95] op_sel_hi:[1,1,0] neg_lo:[0,0,1] neg_hi:[0,0,1]\n"
"v_cvt_pk_bf16_f32 v39, v90, v91\n"
"v_pk_fma_f32 v[86:87], v[86:87], s[88:89], v[94:95] op_sel_hi:[1,1,0] neg_lo:[0,0,1] neg_hi:[0,0,1]\n"
"s_waitcnt lgkmcnt(3)\n"
"v_mfma_f32_16x16x16_bf16 v[28:31], v[24:25], v[36:37], 0\n"
"v_exp_f32 v80, v80\n"
"v_mfma_f32_16x16x16_bf16 v[96:99], v[16:17], v[36:37], 0\n"
"v_exp_f32 v81, v81\n"
"v_mfma_f32_16x16x16_bf16 v[100:103], v[16:17], v[38:39], 0\n"
"v_exp_f32 v82, v82\n"
"v_mfma_f32_16x16x16_bf16 v[104:107], v[18:19], v[36:37], 0\n"
"v_exp_f32 v83, v83\n"
"v_mfma_f32_16x16x16_bf16 v[108:111], v[18:19], v[38:39], 0\n"
"v_exp_f32 v84, v84\n"
"v_mfma_f32_16x16x16_bf16 v[112:115], v[20:21], v[36:37], 0\n"
"v_exp_f32 v85, v85\n"
"v_mfma_f32_16x16x16_bf16 v[116:119], v[20:21], v[38:39], 0\n"
"v_exp_f32 v86, v86\n"
"v_mfma_f32_16x16x16_bf16 v[120:123], v[22:23], v[36:37], 0\n"
"v_exp_f32 v87, v87\n"
"v_mfma_f32_16x16x16_bf16 v[124:127], v[22:23], v[38:39], 0\n"
"v_cvt_pk_bf16_f32 v32, v80, v81\n"
"v_cvt_pk_bf16_f32 v33, v82, v83\n"
"v_cvt_pk_bf16_f32 v34, v84, v85\n"
"v_cvt_pk_bf16_f32 v35, v86, v87\n"
"s_waitcnt lgkmcnt(0)\n"
"s_cmp_eq_u32 s44, 63\n"
"v_mfma_f32_16x16x32_bf16 v[28:31], v[24:27], v[32:35], v[28:31]\n"
"v_mfma_f32_16x16x32_bf16 v[96:99], v[64:67], v[32:35], v[96:99]\n"
"v_mfma_f32_16x16x32_bf16 v[100:103], v[68:71], v[32:35], v[100:103]\n"
"v_rcp_f32 v16, v28\n"
"v_mfma_f32_16x16x32_bf16 v[104:107], v[72:75], v[32:35], v[104:107]\n"
"v_mul_f32 v96, v16, v96\n"
"v_mul_f32 v97, v16, v97\n"
"v_mfma_f32_16x16x32_bf16 v[108:111], v[76:79], v[32:35], v[108:111]\n"
"v_mul_f32 v98, v16, v98\n"
"v_mul_f32 v99, v16, v99\n"
"v_mfma_f32_16x16x32_bf16 v[112:115], v[48:51], v[32:35], v[112:115]\n"
"v_mul_f32 v100, v16, v100\n"
"v_mul_f32 v101, v16, v101\n"
"v_mfma_f32_16x16x32_bf16 v[116:119], v[52:55], v[32:35], v[116:119]\n"
"v_mul_f32 v102, v16, v102\n"
"v_mul_f32 v103, v16, v103\n"
"v_mfma_f32_16x16x32_bf16 v[120:123], v[56:59], v[32:35], v[120:123]\n"
"v_cvt_pk_bf16_f32 v44, v96, v97\n"
"v_mov_b64 v[64:65], 0\n"
"v_mfma_f32_16x16x32_bf16 v[124:127], v[60:63], v[32:35], v[124:127]\n"
"s_cbranch_scc1 Lnb%=\n"
"s_barrier\n"
"Lnb%=:\n"
"s_and_b32 s66, vcc_lo, 15\n"
"s_cbranch_scc1 Lkvp%=\n"
"Lkvq%=:\n"
"v_cvt_pk_bf16_f32 v45, v98, v99\n"
"v_mov_b64 v[66:67], 0\n"
"v_cvt_pk_bf16_f32 v46, v100, v101\n"
"v_mov_b64 v[68:69], 0\n"
"v_cvt_pk_bf16_f32 v47, v102, v103\n"
"v_mov_b64 v[70:71], 0\n"
"v_pk_mul_f32 v[104:105], v[104:105], v[16:17] op_sel_hi:[1,0]\n"
"v_cvt_pk_bf16_f32 v40, v104, v105\n"
"v_pk_mul_f32 v[106:107], v[106:107], v[16:17] op_sel_hi:[1,0]\n"
"v_cvt_pk_bf16_f32 v41, v106, v107\n"
"v_pk_mul_f32 v[108:109], v[108:109], v[16:17] op_sel_hi:[1,0]\n"
"v_cvt_pk_bf16_f32 v42, v108, v109\n"
"v_pk_mul_f32 v[110:111], v[110:111], v[16:17] op_sel_hi:[1,0]\n"
"v_cvt_pk_bf16_f32 v43, v110, v111\n"
"buffer_store_dwordx4 v[44:47], v9, s[40:43], s48 offen offset:0 sc1\n"
"buffer_store_dwordx4 v[40:43], v9, s[40:43], s48 offen offset:1024 sc1\n"
"v_pk_mul_f32 v[112:113], v[112:113], v[16:17] op_sel_hi:[1,0]\n"
"v_cvt_pk_bf16_f32 v36, v112, v113\n"
"v_pk_mul_f32 v[114:115], v[114:115], v[16:17] op_sel_hi:[1,0]\n"
"v_cvt_pk_bf16_f32 v37, v114, v115\n"
"v_pk_mul_f32 v[116:117], v[116:117], v[16:17] op_sel_hi:[1,0]\n"
"v_cvt_pk_bf16_f32 v38, v116, v117\n"
"v_pk_mul_f32 v[118:119], v[118:119], v[16:17] op_sel_hi:[1,0]\n"
"v_cvt_pk_bf16_f32 v39, v118, v119\n"
"v_pk_mul_f32 v[120:121], v[120:121], v[16:17] op_sel_hi:[1,0]\n"
"v_cvt_pk_bf16_f32 v32, v120, v121\n"
"v_pk_mul_f32 v[122:123], v[122:123], v[16:17] op_sel_hi:[1,0]\n"
"v_cvt_pk_bf16_f32 v33, v122, v123\n"
"v_pk_mul_f32 v[124:125], v[124:125], v[16:17] op_sel_hi:[1,0]\n"
"v_cvt_pk_bf16_f32 v34, v124, v125\n"
"v_pk_mul_f32 v[126:127], v[126:127], v[16:17] op_sel_hi:[1,0]\n"
"v_cvt_pk_bf16_f32 v35, v126, v127\n"
"v_mfma_f32_32x32x16_bf16 v[48:63], v[64:67], v[64:67], 0\n"
"buffer_store_dwordx4 v[36:39], v9, s[40:43], s48 offen offset:2048 sc1\n"
"buffer_store_dwordx4 v[32:35], v9, s[40:43], s48 offen offset:3072 sc1\n"
"s_cmp_eq_u32 s44, 63\n"
"s_cbranch_scc1 Lcfin%=\n"
"ds_read_b128 v[72:75], v6 offset:0\n"
"ds_read_b128 v[76:79], v6 offset:1024\n"
"ds_read_b128 v[80:83], v6 offset:2048\n"
"ds_read_b128 v[84:87], v6 offset:3072\n"
"ds_read_b128 v[88:91], v6 offset:4096\n"
"ds_read_b128 v[92:95], v6 offset:5120\n"
"ds_read_b128 v[96:99], v6 offset:6144\n"
"ds_read_b128 v[100:103], v6 offset:7168\n"
"ds_read_b128 v[104:107], v6 offset:8192\n"
"ds_read_b128 v[108:111], v6 offset:9216\n"
"ds_read_b128 v[112:115], v6 offset:10240\n"
"ds_read_b128 v[116:119], v6 offset:11264\n"
"ds_read_b128 v[120:123], v6 offset:12288\n"
"ds_read_b128 v[124:127], v6 offset:13312\n"
"s_waitcnt vmcnt(4)\n"
"s_add_u32 s44, s44, 1\n"
"s_mov_b32 s45, s22\n"
"s_and_b32 s66, s22, 0x1000\n"
"s_lshl1_add_u32 s22, s66, s22\n"
"s_add_u32 s22, s22, 0x1000\n"
"s_add_u32 s67, s46, s93\n"
"v_cmp_gt_i32 vcc, s44, v15\n"
"s_mov_b32 s65, 0\n"
"s_and_b32 s66, vcc_lo, 15\n"
"s_cbranch_scc0 Lcloop%=\n"
"v_and_b32 v17, 12, v13\n"
"s_sub_u32 s66, s44, 1\n"
"buffer_load_dword v16, v17, s[40:43], s45 offen sc1\n"
"v_mov_b32 v26, s66\n"
"s_waitcnt vmcnt(1)\n"
"buffer_store_dword v26, v13, s[40:43], s54 offen sc1\n"
"s_waitcnt vmcnt(1)\n"
"v_cmp_gt_i32 vcc, s44, v16\n"
"s_cbranch_vccz Lcloop%=\n"
"s_mov_b32 s65, 0\n"
"v_and_b32 v17, 12, v13\n"
"Lck2p%=:\n"
"buffer_load_dword v16, v17, s[40:43], s45 offen sc1\n"
"s_add_u32 s65, s65, 1\n"
"s_cmp_gt_u32 s65, 131072\n"
"s_cbranch_scc1 Lcend%=\n"
"s_waitcnt vmcnt(0)\n"
"v_cmp_gt_i32 vcc, s44, v16\n"
"s_cbranch_vccz Lck2r%=\n"
"s_sleep 1\n"
"s_branch Lck2p%=\n"
"Lck2r%=:\n"
"s_mov_b32 s65, 0\n"
"s_branch Lcloop%=\n"
"Lanfail%=:\n"
"s_add_u32 s65, s65, 1\n"
"s_cmp_gt_u32 s65, 131072\n"
"s_cbranch_scc1 Lcend%=\n"
"v_lshlrev_b32 v14, 3, v9\n"
"v_and_b32 v14, 0xfff, v14\n"
"buffer_load_dword v14, v14, s[40:43], s95 offen sc1\n"
"s_waitcnt vmcnt(0) lgkmcnt(0)\n"
"s_branch Lan%=\n"
"Lslow%=:\n"
"s_memrealtime s[74:75]\n"
"s_nop 4\n"
"s_memrealtime s[32:33]\n"
"s_nop 4\n"
"s_memrealtime s[36:37]\n"
"s_nop 4\n"
"s_memrealtime s[62:63]\n"
"s_nop 4\n"
"s_waitcnt lgkmcnt(0)\n"
"s_add_u32 s66, s74, s32\n"
"s_add_u32 s36, s36, s62\n"
"s_add_u32 s66, s66, s36\n"
"s_add_u32 s66, s66, 4\n"
"s_branch Lsl2%=\n"
"Lp1r%=:\n"
"buffer_load_dwordx4 v[16:19], v8, s[40:43], s20 offen offset:0 sc1\n"
"buffer_load_dwordx4 v[20:23], v8, s[40:43], s20 offen offset:1024 sc1\n"
"buffer_load_dwordx4 v[24:27], v8, s[40:43], s20 offen offset:2048 sc1\n"
"buffer_load_dwordx4 v[28:31], v8, s[40:43], s20 offen offset:3072 sc1\n"
"s_memrealtime s[74:75]\n"
"s_add_u32 s65, s65, 1\n"
"s_cmp_gt_u32 s65, 131072\n"
"s_cbranch_scc1 Lcend%=\n"
"s_waitcnt lgkmcnt(0)\n"
"s_lshl2_add_u32 s91, s74, 168\n"
"s_sub_u32 s91, s91, s96\n"
"s_max_i32 s91, s91, 0\n"
"s_add_u32 s96, s96, s91\n"
"Lp1s%=:\n"
"s_add_u32 s70, s70, 1\n"
"s_nop 4\n"
"s_waitcnt vmcnt(3)\n"
"v_mfma_f32_16x16x32_bf16 v[48:51], v[16:19], v[56:59], 0\n"
"v_xor_b32 v52, s21, v16\n"
"s_waitcnt vmcnt(2)\n"
"v_mfma_f32_16x16x32_bf16 v[48:51], v[20:23], v[56:59], v[48:51]\n"
"v_bitop3_b32 v52, v52, v20, s21 bitop3:0xf6\n"
"s_waitcnt vmcnt(1)\n"
"v_mfma_f32_16x16x32_bf16 v[48:51], v[24:27], v[56:59], v[48:51]\n"
"v_bitop3_b32 v52, v52, v24, s21 bitop3:0xf6\n"
"s_waitcnt vmcnt(0)\n"
"v_mfma_f32_16x16x32_bf16 v[48:51], v[28:31], v[56:59], v[48:51]\n"
"v_bitop3_b32 v52, v52, v28, s21 bitop3:0xf6\n"
"v_and_b32 v52, 1, v52\n"
"v_cmp_ne_u32 vcc, 0, v52\n"
"s_cbranch_vccz Lpub%=\n"
"s_branch Lp1r%=\n"
"Lfail2%=:\n"
"s_add_u32 s65, s65, 1\n"
"s_cmp_gt_u32 s65, 131072\n"
"s_cbranch_scc1 Lcend%=\n"
"s_add_u32 s71, s71, 1\n"
"buffer_load_dwordx4 v[16:19], v1, s[40:43], s67 offen sc1\n"
"buffer_load_dwordx4 v[20:23], v1, s[40:43], s67 offen offset:128 sc1\n"
"v_mov_b64 v[48:49], 0\n"
"v_mov_b64 v[50:51], 0\n"
"v_mov_b64 v[52:53], 0\n"
"v_mov_b64 v[54:55], 0\n"
"v_mov_b64 v[56:57], 0\n"
"v_mov_b64 v[58:59], 0\n"
"v_mov_b64 v[60:61], 0\n"
"v_mov_b64 v[62:63], 0\n"
"v_mov_b64 v[64:65], 0\n"
"v_mov_b64 v[66:67], 0\n"
"v_mov_b64 v[68:69], 0\n"
"v_mov_b64 v[70:71], 0\n"
"s_waitcnt vmcnt(0)\n"
"s_branch Lp2%=\n"
"Lkvp%=:\n"
"buffer_load_dword v15, v11, s[40:43], s22 offen sc1\n"
"s_branch Lkvq%=\n"
"Lqslow%=:\n"
"s_mov_b32 s65, 0\n"
"Lqp%=:\n"
"buffer_load_dword v15, v11, s[40:43], s22 offen sc1\n"
"s_add_u32 s65, s65, 1\n"
"s_cmp_gt_u32 s65, 131072\n"
"s_cbranch_scc1 Lcend%=\n"
"s_waitcnt vmcnt(0)\n"
"v_cmp_gt_i32 vcc, s44, v15\n"
"s_bitset0_b32 s23, 31\n"
"s_and_b32 s66, vcc_lo, 0xf0\n"
"s_cbranch_scc1 Lqp%=\n"
"buffer_load_dwordx4 v[32:35], v9, s[40:43], s23 offen offset:0 sc1\n"
"buffer_load_dwordx4 v[36:39], v9, s[40:43], s23 offen offset:1024 sc1\n"
"buffer_load_dwordx4 v[40:43], v9, s[40:43], s23 offen offset:2048 sc1\n"
"buffer_load_dwordx4 v[44:47], v9, s[40:43], s23 offen offset:3072 sc1\n"
"s_waitcnt vmcnt(0)\n"
"s_branch Lqok%=\n"
"Lclast%=:\n"
"v_mov_b32 v48, s34\n"
"s_cmp_eq_u32 s60, 0\n"
"s_cselect_b64 exec, 1, 0\n"
"buffer_store_dword v48, v9, s[40:43], s61 offen sc1\n"
"s_mov_b64 exec, -1\n"
"s_bitcmp1_b32 s60, 0\n"
"s_cbranch_scc0 Lcend%=\n"
"s_waitcnt lgkmcnt(0)\n"
"ds_read_b128 v[16:19], v2 offset:272\n"
"ds_read_b128 v[20:23], v2 offset:336\n"
"ds_read_b128 v[24:27], v2 offset:400\n"
"ds_read_b128 v[28:31], v2 offset:464\n"
"ds_read_b128 v[96:99], v10 offset:8192\n"
"ds_read_b128 v[100:103], v10 offset:9216\n"
"ds_read_b128 v[104:107], v10 offset:10240\n"
"ds_read_b128 v[108:111], v10 offset:11264\n"
"ds_read_b128 v[112:115], v10 offset:12288\n"
"ds_read_b128 v[116:119], v10 offset:13312\n"
"ds_read_b128 v[120:123], v10 offset:14336\n"
"ds_read_b128 v[124:127], v10 offset:15360\n"
"s_waitcnt vmcnt(1) lgkmcnt(0)\n"
"s_branch Lhouse%=\n"
"Lcfin%=:\n"
"s_waitcnt vmcnt(0)\n"
"v_mov_b32 v16, s44\n"
"buffer_store_dword v16, v13, s[40:43], s54 offen sc1\n"
"Lcend%=:\n"
"s_waitcnt vmcnt(0) lgkmcnt(0)\n"
"LKCH:\n"
    OPS);
  } else if(role==6){
    const int h=j;
    const int th=16*((i32>>2)&1)+8*(i32>>4)+4*((i32>>3)&1)+(i32&3);
    VC[0]=lane*16; VC[1]=i32*1040+hh*16;
    VC[2]=(((th>>2)&1)*4+w)*1024+(hh*32+4*(th>>3)+(th&3))*16; VC[3]=(2*w+(i32>>4))*1024+(hh*32+c)*16; VC[4]=((th>>4)*4+w)*1024+(hh*32+(th&15))*16;
    VC[5]=0x9400+w*3072+lane*16; VC[6]=(lane<32)? (w+4*((lane>>1)&1))*256+(lane>>2)*32+(lane&1)*16 : 0x7f000000; VC[7]=33280+((th>>2)&3)*1088+(th>>4)*32+hh*256+(th&3)*8; VC[8]=0xC800+w*128+i32*4; VC[9]=0xC800+w*128+hh*16; VC[10]=(lane&(p?15:3))*FS;
    VC[11]=(4*w+g)*8192+h*256+((c^(g<<2))*16);
    VC[15]=(4*w+g)*8192+h*256+((c^((g&1)|((g&2)<<2)))*16);
    VC[12]=(8*(g>>1)+q4)*256+((128*(w>>1)+64*(g&1)+32*(p4>>1)+16*(w&1)+8*(p4&1))^(((q4&1)|((q4&2)<<2))<<4)); VC[13]=(8*(g>>1)+q4)*256+((64*w+32*(g&1)+8*p4)^(q4<<6));
    SA[5]=p? 99 : 10; SA[7]=OFF_FLAG+(p? F_X2+SD2*8192+w*16*FS : F_X0+w*4*FS); SA[15]=p? 0x2000 : 0; SA[9]=8*w*1040; SA[10]=OFF_K+h*16384; SA[11]=OFF_FLAG+F_KV+SDK*8192+h*FS+p*32+w*4; SA[12]=w*1024; SA[14]=33280+w*1088;
    SB[6]=p? OFF_V+(4+(w>>1))*16384+(w&1)*4096 : 4*w*1024; SB[7]=p? OFF_V+9*16384 : OFF_SA; SB[8]=p? 0x80000 : 0x8000; SB[9]=p? 0x80000 : 2048; SB[10]=p? 32768 : 16384; SB[11]=0x2000;
    if(p) setrs(SB,0,ws,WS_USED); else setrs(SB,0,x,2048*512*2);
    SB[4]=0; SB[5]=~0u;
    setrs(SC,0,Wk,512*4096*2); setrs(SC,4,Wv,512*4096*2); setrs(SC,8,Wq,512*4096*2);
    asm volatile(
"s_waitcnt lgkmcnt(0)\n"
"s_nop 4\n"
"v_mov_b32 v112, 0x3f803f80\n"
"v_mov_b32 v113, 0x3f803f80\n"
"v_mov_b32 v114, 0x3f803f80\n"
"v_mov_b32 v115, 0x3f803f80\n"
"s_lshr_b32 s89, s62, 8\n"
"s_xor_b32 s89, s89, s67\n"
"s_and_b32 s89, s89, s55\n"
"s_add_u32 s48, s62, s89\n"
"s_add_u32 s53, s63, s89\n"
"s_add_u32 s68, s48, s66\n"
"s_mov_b32 s33, 4917504\n"
"buffer_load_dword v125, off, s[40:43], s33 sc1\n"
"v_mbcnt_lo_u32_b32 v126, -1, 0\n"
"v_lshlrev_b32 v126, 2, v126\n"
"s_add_u32 m0, s52, 49152\n"
"s_mov_b32 s89, 0x0\n"
"buffer_load_dwordx4 v15, s[80:83], s89 offen lds\n"
"s_add_u32 m0, s52, 53248\n"
"s_mov_b32 s89, 0x20000\n"
"buffer_load_dwordx4 v15, s[80:83], s89 offen lds\n"
"s_add_u32 m0, s52, 57344\n"
"s_mov_b32 s89, 0x40000\n"
"buffer_load_dwordx4 v15, s[80:83], s89 offen lds\n"
"s_add_u32 m0, s52, 61440\n"
"s_mov_b32 s89, 0x60000\n"
"buffer_load_dwordx4 v15, s[80:83], s89 offen lds\n"
"s_add_u32 m0, s52, 65536\n"
"s_mov_b32 s89, 0x80000\n"
"buffer_load_dwordx4 v15, s[80:83], s89 offen lds\n"
"s_add_u32 m0, s52, 69632\n"
"s_mov_b32 s89, 0xa0000\n"
"buffer_load_dwordx4 v15, s[80:83], s89 offen lds\n"
"s_add_u32 m0, s52, 73728\n"
"s_mov_b32 s89, 0xc0000\n"
"buffer_load_dwordx4 v15, s[80:83], s89 offen lds\n"
"s_add_u32 m0, s52, 77824\n"
"s_mov_b32 s89, 0xe0000\n"
"buffer_load_dwordx4 v15, s[80:83], s89 offen lds\n"
"s_add_u32 m0, s52, 81920\n"
"s_mov_b32 s89, 0x100000\n"
"buffer_load_dwordx4 v15, s[80:83], s89 offen lds\n"
"s_add_u32 m0, s52, 86016\n"
"s_mov_b32 s89, 0x120000\n"
"buffer_load_dwordx4 v15, s[80:83], s89 offen lds\n"
"s_add_u32 m0, s52, 90112\n"
"s_mov_b32 s89, 0x140000\n"
"buffer_load_dwordx4 v15, s[80:83], s89 offen lds\n"
"s_add_u32 m0, s52, 94208\n"
"s_mov_b32 s89, 0x160000\n"
"buffer_load_dwordx4 v15, s[80:83], s89 offen lds\n"
"s_waitcnt vmcnt(12)\n"
"v_readfirstlane_b32 s33, v125\n"
"s_cmp_lt_i32 s36, 0\n"
"s_cselect_b32 s34, s34, s33\n"
"s_mul_i32 s33, s34, 0x9e3779b1\n"
"s_add_u32 s33, s33, 0x7f4a7c15\n"
"v_mov_b32 v125, s33\n"
"s_mov_b32 exec_hi, 0\n"
"buffer_store_dword v125, v126, s[40:43], s36 offen sc1\n"
"s_mov_b64 exec, -1\n"
"s_mov_b32 s36, s33\n"
"s_add_u32 m0, s52, 98304\n"
"s_mov_b32 s89, 0x180000\n"
"buffer_load_dwordx4 v15, s[80:83], s89 offen lds\n"
"s_add_u32 m0, s52, 102400\n"
"s_mov_b32 s89, 0x1a0000\n"
"buffer_load_dwordx4 v15, s[80:83], s89 offen lds\n"
"s_add_u32 m0, s52, 106496\n"
"s_mov_b32 s89, 0x1c0000\n"
"buffer_load_dwordx4 v15, s[80:83], s89 offen lds\n"
"s_add_u32 m0, s52, 110592\n"
"s_mov_b32 s89, 0x1e0000\n"
"buffer_load_dwordx4 v15, s[80:83], s89 offen lds\n"
"s_add_u32 m0, s52, 114688\n"
"s_mov_b32 s89, 0x200000\n"
"buffer_load_dwordx4 v15, s[80:83], s89 offen lds\n"
"s_add_u32 m0, s52, 118784\n"
"s_mov_b32 s89, 0x220000\n"
"buffer_load_dwordx4 v15, s[80:83], s89 offen lds\n"
"s_add_u32 m0, s52, 122880\n"
"s_mov_b32 s89, 0x240000\n"
"buffer_load_dwordx4 v15, s[80:83], s89 offen lds\n"
"s_add_u32 m0, s52, 126976\n"
"s_mov_b32 s89, 0x260000\n"
"buffer_load_dwordx4 v15, s[80:83], s89 offen lds\n"
"s_add_u32 m0, s52, 131072\n"
"s_mov_b32 s89, 0x280000\n"
"buffer_load_dwordx4 v15, s[80:83], s89 offen lds\n"
"s_add_u32 m0, s52, 135168\n"
"s_mov_b32 s89, 0x2a0000\n"
"buffer_load_dwordx4 v15, s[80:83], s89 offen lds\n"
"s_add_u32 m0, s52, 139264\n"
"s_mov_b32 s89, 0x2c0000\n"
"buffer_load_dwordx4 v15, s[80:83], s89 offen lds\n"
"s_add_u32 m0, s52, 143360\n"
"s_mov_b32 s89, 0x2e0000\n"
"buffer_load_dwordx4 v15, s[80:83], s89 offen lds\n"
"s_add_u32 m0, s52, 0\n"
"s_mov_b32 s89, 0x300000\n"
"buffer_load_dwordx4 v15, s[80:83], s89 offen lds\n"
"s_add_u32 m0, s52, 4096\n"
"s_mov_b32 s89, 0x320000\n"
"buffer_load_dwordx4 v15, s[80:83], s89 offen lds\n"
"s_add_u32 m0, s52, 8192\n"
"s_mov_b32 s89, 0x340000\n"
"buffer_load_dwordx4 v15, s[80:83], s89 offen lds\n"
"s_add_u32 m0, s52, 12288\n"
"s_mov_b32 s89, 0x360000\n"
"buffer_load_dwordx4 v15, s[80:83], s89 offen lds\n"
"s_add_u32 m0, s52, 16384\n"
"s_mov_b32 s89, 0x380000\n"
"buffer_load_dwordx4 v15, s[80:83], s89 offen lds\n"
"s_add_u32 m0, s52, 20480\n"
"s_mov_b32 s89, 0x3a0000\n"
"buffer_load_dwordx4 v15, s[80:83], s89 offen lds\n"
"s_add_u32 m0, s52, 24576\n"
"s_mov_b32 s89, 0x3c0000\n"
"buffer_load_dwordx4 v15, s[80:83], s89 offen lds\n"
"s_add_u32 m0, s52, 28672\n"
"s_mov_b32 s89, 0x3e0000\n"
"buffer_load_dwordx4 v15, s[80:83], s89 offen lds\n"
"s_add_u32 m0, s52, 32768\n"
"s_mov_b32 s89, 0x0\n"
"buffer_load_dwordx4 v15, s[72:75], s89 offen lds\n"
"s_add_u32 m0, s52, 36864\n"
"s_mov_b32 s89, 0x20000\n"
"buffer_load_dwordx4 v15, s[72:75], s89 offen lds\n"
"s_add_u32 m0, s52, 40960\n"
"s_mov_b32 s89, 0x40000\n"
"buffer_load_dwordx4 v15, s[72:75], s89 offen lds\n"
"s_add_u32 m0, s52, 45056\n"
"s_mov_b32 s89, 0x60000\n"
"buffer_load_dwordx4 v15, s[72:75], s89 offen lds\n"
"s_waitcnt vmcnt(25)\n"
"s_barrier\n"
"v_add_u32 v124, 0xc000, v12\n"
"ds_read_b64_tr_b16 v[128:129], v124 offset:0\n"
"ds_read_b64_tr_b16 v[130:131], v124 offset:1024\n"
"ds_read_b64_tr_b16 v[132:133], v124 offset:4096\n"
"ds_read_b64_tr_b16 v[134:135], v124 offset:5120\n"
"ds_read_b64_tr_b16 v[136:137], v124 offset:8192\n"
"ds_read_b64_tr_b16 v[138:139], v124 offset:9216\n"
"ds_read_b64_tr_b16 v[140:141], v124 offset:12288\n"
"ds_read_b64_tr_b16 v[142:143], v124 offset:13312\n"
"s_waitcnt lgkmcnt(6)\n"
"v_add_u32 v124, 0x10000, v12\n"
"ds_read_b64_tr_b16 v[144:145], v124 offset:0\n"
"ds_read_b64_tr_b16 v[146:147], v124 offset:1024\n"
"ds_read_b64_tr_b16 v[148:149], v124 offset:4096\n"
"ds_read_b64_tr_b16 v[150:151], v124 offset:5120\n"
"ds_read_b64_tr_b16 v[152:153], v124 offset:8192\n"
"ds_read_b64_tr_b16 v[154:155], v124 offset:9216\n"
"ds_read_b64_tr_b16 v[156:157], v124 offset:12288\n"
"ds_read_b64_tr_b16 v[158:159], v124 offset:13312\n"
"s_waitcnt lgkmcnt(6)\n"
"v_add_u32 v124, 0x14000, v12\n"
"ds_read_b64_tr_b16 v[160:161], v124 offset:0\n"
"ds_read_b64_tr_b16 v[162:163], v124 offset:1024\n"
"ds_read_b64_tr_b16 v[164:165], v124 offset:4096\n"
"ds_read_b64_tr_b16 v[166:167], v124 offset:5120\n"
"ds_read_b64_tr_b16 v[168:169], v124 offset:8192\n"
"ds_read_b64_tr_b16 v[170:171], v124 offset:9216\n"
"ds_read_b64_tr_b16 v[172:173], v124 offset:12288\n"
"ds_read_b64_tr_b16 v[174:175], v124 offset:13312\n"
"s_waitcnt lgkmcnt(0)\n"
"s_barrier\n"
"s_add_u32 m0, s52, 49152\n"
"s_mov_b32 s89, 0x80000\n"
"buffer_load_dwordx4 v15, s[72:75], s89 offen lds\n"
"s_add_u32 m0, s52, 53248\n"
"s_mov_b32 s89, 0xa0000\n"
"buffer_load_dwordx4 v15, s[72:75], s89 offen lds\n"
"s_add_u32 m0, s52, 57344\n"
"s_mov_b32 s89, 0xc0000\n"
"buffer_load_dwordx4 v15, s[72:75], s89 offen lds\n"
"s_add_u32 m0, s52, 61440\n"
"s_mov_b32 s89, 0xe0000\n"
"buffer_load_dwordx4 v15, s[72:75], s89 offen lds\n"
"s_add_u32 m0, s52, 65536\n"
"s_mov_b32 s89, 0x100000\n"
"buffer_load_dwordx4 v15, s[72:75], s89 offen lds\n"
"s_add_u32 m0, s52, 69632\n"
"s_mov_b32 s89, 0x120000\n"
"buffer_load_dwordx4 v15, s[72:75], s89 offen lds\n"
"s_add_u32 m0, s52, 73728\n"
"s_mov_b32 s89, 0x140000\n"
"buffer_load_dwordx4 v15, s[72:75], s89 offen lds\n"
"s_add_u32 m0, s52, 77824\n"
"s_mov_b32 s89, 0x160000\n"
"buffer_load_dwordx4 v15, s[72:75], s89 offen lds\n"
"s_add_u32 m0, s52, 81920\n"
"s_mov_b32 s89, 0x180000\n"
"buffer_load_dwordx4 v15, s[72:75], s89 offen lds\n"
"s_add_u32 m0, s52, 86016\n"
"s_mov_b32 s89, 0x1a0000\n"
"buffer_load_dwordx4 v15, s[72:75], s89 offen lds\n"
"s_add_u32 m0, s52, 90112\n"
"s_mov_b32 s89, 0x1c0000\n"
"buffer_load_dwordx4 v15, s[72:75], s89 offen lds\n"
"s_add_u32 m0, s52, 94208\n"
"s_mov_b32 s89, 0x1e0000\n"
"buffer_load_dwordx4 v15, s[72:75], s89 offen lds\n"
"v_mfma_f32_32x32x16_bf16 v[48:63], v[112:115], v[128:131], 0\n"
"v_mfma_f32_32x32x16_bf16 v[48:63], v[112:115], v[132:135], v[48:63]\n"
"v_mfma_f32_32x32x16_bf16 v[48:63], v[112:115], v[136:139], v[48:63]\n"
"v_mfma_f32_32x32x16_bf16 v[48:63], v[112:115], v[140:143], v[48:63]\n"
"v_mfma_f32_32x32x16_bf16 v[48:63], v[112:115], v[144:147], v[48:63]\n"
"v_mfma_f32_32x32x16_bf16 v[48:63], v[112:115], v[148:151], v[48:63]\n"
"v_mfma_f32_32x32x16_bf16 v[48:63], v[112:115], v[152:155], v[48:63]\n"
"v_mfma_f32_32x32x16_bf16 v[48:63], v[112:115], v[156:159], v[48:63]\n"
"v_mfma_f32_32x32x16_bf16 v[48:63], v[112:115], v[160:163], v[48:63]\n"
"v_mfma_f32_32x32x16_bf16 v[48:63], v[112:115], v[164:167], v[48:63]\n"
"v_mfma_f32_32x32x16_bf16 v[48:63], v[112:115], v[168:171], v[48:63]\n"
"v_mfma_f32_32x32x16_bf16 v[48:63], v[112:115], v[172:175], v[48:63]\n"
"s_waitcnt vmcnt(24)\n"
"s_barrier\n"
"v_add_u32 v124, 0x18000, v12\n"
"ds_read_b64_tr_b16 v[176:177], v124 offset:0\n"
"ds_read_b64_tr_b16 v[178:179], v124 offset:1024\n"
"ds_read_b64_tr_b16 v[180:181], v124 offset:4096\n"
"ds_read_b64_tr_b16 v[182:183], v124 offset:5120\n"
"ds_read_b64_tr_b16 v[184:185], v124 offset:8192\n"
"ds_read_b64_tr_b16 v[186:187], v124 offset:9216\n"
"ds_read_b64_tr_b16 v[188:189], v124 offset:12288\n"
"ds_read_b64_tr_b16 v[190:191], v124 offset:13312\n"
"s_waitcnt lgkmcnt(6)\n"
"v_add_u32 v124, 0x1c000, v12\n"
"ds_read_b64_tr_b16 v[192:193], v124 offset:0\n"
"ds_read_b64_tr_b16 v[194:195], v124 offset:1024\n"
"ds_read_b64_tr_b16 v[196:197], v124 offset:4096\n"
"ds_read_b64_tr_b16 v[198:199], v124 offset:5120\n"
"ds_read_b64_tr_b16 v[200:201], v124 offset:8192\n"
"ds_read_b64_tr_b16 v[202:203], v124 offset:9216\n"
"ds_read_b64_tr_b16 v[204:205], v124 offset:12288\n"
"ds_read_b64_tr_b16 v[206:207], v124 offset:13312\n"
"s_waitcnt lgkmcnt(6)\n"
"v_add_u32 v124, 0x20000, v12\n"
"ds_read_b64_tr_b16 v[208:209], v124 offset:0\n"
"ds_read_b64_tr_b16 v[210:211], v124 offset:1024\n"
"ds_read_b64_tr_b16 v[212:213], v124 offset:4096\n"
"ds_read_b64_tr_b16 v[214:215], v124 offset:5120\n"
"ds_read_b64_tr_b16 v[216:217], v124 offset:8192\n"
"ds_read_b64_tr_b16 v[218:219], v124 offset:9216\n"
"ds_read_b64_tr_b16 v[220:221], v124 offset:12288\n"
"ds_read_b64_tr_b16 v[222:223], v124 offset:13312\n"
"s_waitcnt lgkmcnt(0)\n"
"s_barrier\n"
"s_add_u32 m0, s52, 98304\n"
"s_mov_b32 s89, 0x200000\n"
"buffer_load_dwordx4 v15, s[72:75], s89 offen lds\n"
"s_add_u32 m0, s52, 102400\n"
"s_mov_b32 s89, 0x220000\n"
"buffer_load_dwordx4 v15, s[72:75], s89 offen lds\n"
"s_add_u32 m0, s52, 106496\n"
"s_mov_b32 s89, 0x240000\n"
"buffer_load_dwordx4 v15, s[72:75], s89 offen lds\n"
"s_add_u32 m0, s52, 110592\n"
"s_mov_b32 s89, 0x260000\n"
"buffer_load_dwordx4 v15, s[72:75], s89 offen lds\n"
"s_add_u32 m0, s52, 114688\n"
"s_mov_b32 s89, 0x280000\n"
"buffer_load_dwordx4 v15, s[72:75], s89 offen lds\n"
"s_add_u32 m0, s52, 118784\n"
"s_mov_b32 s89, 0x2a0000\n"
"buffer_load_dwordx4 v15, s[72:75], s89 offen lds\n"
"s_add_u32 m0, s52, 122880\n"
"s_mov_b32 s89, 0x2c0000\n"
"buffer_load_dwordx4 v15, s[72:75], s89 offen lds\n"
"s_add_u32 m0, s52, 126976\n"
"s_mov_b32 s89, 0x2e0000\n"
"buffer_load_dwordx4 v15, s[72:75], s89 offen lds\n"
"s_add_u32 m0, s52, 131072\n"
"s_mov_b32 s89, 0x300000\n"
"buffer_load_dwordx4 v15, s[72:75], s89 offen lds\n"
"s_add_u32 m0, s52, 135168\n"
"s_mov_b32 s89, 0x320000\n"
"buffer_load_dwordx4 v15, s[72:75], s89 offen lds\n"
"s_add_u32 m0, s52, 139264\n"
"s_mov_b32 s89, 0x340000\n"
"buffer_load_dwordx4 v15, s[72:75], s89 offen lds\n"
"s_add_u32 m0, s52, 143360\n"
"s_mov_b32 s89, 0x360000\n"
"buffer_load_dwordx4 v15, s[72:75], s89 offen lds\n"
"v_mfma_f32_32x32x16_bf16 v[48:63], v[112:115], v[176:179], v[48:63]\n"
"v_mfma_f32_32x32x16_bf16 v[48:63], v[112:115], v[180:183], v[48:63]\n"
"v_mfma_f32_32x32x16_bf16 v[48:63], v[112:115], v[184:187], v[48:63]\n"
"v_mfma_f32_32x32x16_bf16 v[48:63], v[112:115], v[188:191], v[48:63]\n"
"v_mfma_f32_32x32x16_bf16 v[48:63], v[112:115], v[192:195], v[48:63]\n"
"v_mfma_f32_32x32x16_bf16 v[48:63], v[112:115], v[196:199], v[48:63]\n"
"v_mfma_f32_32x32x16_bf16 v[48:63], v[112:115], v[200:203], v[48:63]\n"
"v_mfma_f32_32x32x16_bf16 v[48:63], v[112:115], v[204:207], v[48:63]\n"
"v_mfma_f32_32x32x16_bf16 v[48:63], v[112:115], v[208:211], v[48:63]\n"
"v_mfma_f32_32x32x16_bf16 v[48:63], v[112:115], v[212:215], v[48:63]\n"
"v_mfma_f32_32x32x16_bf16 v[48:63], v[112:115], v[216:219], v[48:63]\n"
"v_mfma_f32_32x32x16_bf16 v[48:63], v[112:115], v[220:223], v[48:63]\n"
"s_waitcnt vmcnt(24)\n"
"s_barrier\n"
"v_add_u32 v124, 0x0, v12\n"
"ds_read_b64_tr_b16 v[224:225], v124 offset:0\n"
"ds_read_b64_tr_b16 v[226:227], v124 offset:1024\n"
"ds_read_b64_tr_b16 v[228:229], v124 offset:4096\n"
"ds_read_b64_tr_b16 v[230:231], v124 offset:5120\n"
"ds_read_b64_tr_b16 v[232:233], v124 offset:8192\n"
"ds_read_b64_tr_b16 v[234:235], v124 offset:9216\n"
"ds_read_b64_tr_b16 v[236:237], v124 offset:12288\n"
"ds_read_b64_tr_b16 v[238:239], v124 offset:13312\n"
"s_waitcnt lgkmcnt(6)\n"
"v_add_u32 v124, 0x4000, v12\n"
"ds_read_b64_tr_b16 v[240:241], v124 offset:0\n"
"ds_read_b64_tr_b16 v[242:243], v124 offset:1024\n"
"ds_read_b64_tr_b16 v[244:245], v124 offset:4096\n"
"ds_read_b64_tr_b16 v[246:247], v124 offset:5120\n"
"ds_read_b64_tr_b16 v[248:249], v124 offset:8192\n"
"ds_read_b64_tr_b16 v[250:251], v124 offset:9216\n"
"ds_read_b64_tr_b16 v[252:253], v124 offset:12288\n"
"ds_read_b64_tr_b16 v[254:255], v124 offset:13312\n"
"s_waitcnt lgkmcnt(6)\n"
"v_add_u32 v124, 0x8000, v12\n"
"ds_read_b64_tr_b16 a[0:1], v124 offset:0\n"
"ds_read_b64_tr_b16 a[2:3], v124 offset:1024\n"
"ds_read_b64_tr_b16 a[4:5], v124 offset:4096\n"
"ds_read_b64_tr_b16 a[6:7], v124 offset:5120\n"
"ds_read_b64_tr_b16 a[8:9], v124 offset:8192\n"
"ds_read_b64_tr_b16 a[10:11], v124 offset:9216\n"
"ds_read_b64_tr_b16 a[12:13], v124 offset:12288\n"
"ds_read_b64_tr_b16 a[14:15], v124 offset:13312\n"
"s_waitcnt lgkmcnt(0)\n"
"s_barrier\n"
"s_add_u32 m0, s52, 0\n"
"s_mov_b32 s89, 0x380000\n"
"buffer_load_dwordx4 v15, s[72:75], s89 offen lds\n"
"s_add_u32 m0, s52, 4096\n"
"s_mov_b32 s89, 0x3a0000\n"
"buffer_load_dwordx4 v15, s[72:75], s89 offen lds\n"
"s_add_u32 m0, s52, 8192\n"
"s_mov_b32 s89, 0x3c0000\n"
"buffer_load_dwordx4 v15, s[72:75], s89 offen lds\n"
"s_add_u32 m0, s52, 12288\n"
"s_mov_b32 s89, 0x3e0000\n"
"buffer_load_dwordx4 v15, s[72:75], s89 offen lds\n"
"s_add_u32 m0, s52, 16384\n"
"s_mov_b32 s89, 0x0\n"
"buffer_load_dwordx4 v11, s[76:79], s89 offen lds\n"
"s_add_u32 m0, s52, 20480\n"
"s_mov_b32 s89, 0x20000\n"
"buffer_load_dwordx4 v11, s[76:79], s89 offen lds\n"
"s_add_u32 m0, s52, 24576\n"
"s_mov_b32 s89, 0x40000\n"
"buffer_load_dwordx4 v11, s[76:79], s89 offen lds\n"
"s_add_u32 m0, s52, 28672\n"
"s_mov_b32 s89, 0x60000\n"
"buffer_load_dwordx4 v11, s[76:79], s89 offen lds\n"
"s_add_u32 m0, s52, 32768\n"
"s_mov_b32 s89, 0x80000\n"
"buffer_load_dwordx4 v11, s[76:79], s89 offen lds\n"
"s_add_u32 m0, s52, 36864\n"
"s_mov_b32 s89, 0xa0000\n"
"buffer_load_dwordx4 v11, s[76:79], s89 offen lds\n"
"s_add_u32 m0, s52, 40960\n"
"s_mov_b32 s89, 0xc0000\n"
"buffer_load_dwordx4 v11, s[76:79], s89 offen lds\n"
"s_add_u32 m0, s52, 45056\n"
"s_mov_b32 s89, 0xe0000\n"
"buffer_load_dwordx4 v11, s[76:79], s89 offen lds\n"
"v_mfma_f32_32x32x16_bf16 v[48:63], v[112:115], v[224:227], v[48:63]\n"
"v_mfma_f32_32x32x16_bf16 v[48:63], v[112:115], v[228:231], v[48:63]\n"
"v_mfma_f32_32x32x16_bf16 v[48:63], v[112:115], v[232:235], v[48:63]\n"
"v_mfma_f32_32x32x16_bf16 v[48:63], v[112:115], v[236:239], v[48:63]\n"
"v_mfma_f32_32x32x16_bf16 v[48:63], v[112:115], v[240:243], v[48:63]\n"
"v_mfma_f32_32x32x16_bf16 v[48:63], v[112:115], v[244:247], v[48:63]\n"
"v_mfma_f32_32x32x16_bf16 v[48:63], v[112:115], v[248:251], v[48:63]\n"
"v_mfma_f32_32x32x16_bf16 v[48:63], v[112:115], v[252:255], v[48:63]\n"
"v_mfma_f32_32x32x16_bf16 v[16:31], v[112:115], a[0:3], 0\n"
"v_mfma_f32_32x32x16_bf16 v[16:31], v[112:115], a[4:7], v[16:31]\n"
"v_mfma_f32_32x32x16_bf16 v[16:31], v[112:115], a[8:11], v[16:31]\n"
"v_mfma_f32_32x32x16_bf16 v[16:31], v[112:115], a[12:15], v[16:31]\n"
"s_waitcnt vmcnt(24)\n"
"s_barrier\n"
"v_add_u32 v124, 0xc000, v12\n"
"ds_read_b64_tr_b16 a[16:17], v124 offset:0\n"
"ds_read_b64_tr_b16 a[18:19], v124 offset:1024\n"
"ds_read_b64_tr_b16 a[20:21], v124 offset:4096\n"
"ds_read_b64_tr_b16 a[22:23], v124 offset:5120\n"
"ds_read_b64_tr_b16 a[24:25], v124 offset:8192\n"
"ds_read_b64_tr_b16 a[26:27], v124 offset:9216\n"
"ds_read_b64_tr_b16 a[28:29], v124 offset:12288\n"
"ds_read_b64_tr_b16 a[30:31], v124 offset:13312\n"
"s_waitcnt lgkmcnt(6)\n"
"v_add_u32 v124, 0x10000, v12\n"
"ds_read_b64_tr_b16 a[32:33], v124 offset:0\n"
"ds_read_b64_tr_b16 a[34:35], v124 offset:1024\n"
"ds_read_b64_tr_b16 a[36:37], v124 offset:4096\n"
"ds_read_b64_tr_b16 a[38:39], v124 offset:5120\n"
"ds_read_b64_tr_b16 a[40:41], v124 offset:8192\n"
"ds_read_b64_tr_b16 a[42:43], v124 offset:9216\n"
"ds_read_b64_tr_b16 a[44:45], v124 offset:12288\n"
"ds_read_b64_tr_b16 a[46:47], v124 offset:13312\n"
"s_waitcnt lgkmcnt(6)\n"
"v_add_u32 v124, 0x14000, v12\n"
"ds_read_b64_tr_b16 a[48:49], v124 offset:0\n"
"ds_read_b64_tr_b16 a[50:51], v124 offset:1024\n"
"ds_read_b64_tr_b16 a[52:53], v124 offset:4096\n"
"ds_read_b64_tr_b16 a[54:55], v124 offset:5120\n"
"ds_read_b64_tr_b16 a[56:57], v124 offset:8192\n"
"ds_read_b64_tr_b16 a[58:59], v124 offset:9216\n"
"ds_read_b64_tr_b16 a[60:61], v124 offset:12288\n"
"ds_read_b64_tr_b16 a[62:63], v124 offset:13312\n"
"s_waitcnt lgkmcnt(0)\n"
"s_barrier\n"
"buffer_load_dwordx2 v[88:89], v10, s[40:43], s47 offen sc1\n"
"s_add_u32 m0, s52, 49152\n"
"s_mov_b32 s89, 0x100000\n"
"buffer_load_dwordx4 v11, s[76:79], s89 offen lds\n"
"s_add_u32 m0, s52, 53248\n"
"s_mov_b32 s89, 0x120000\n"
"buffer_load_dwordx4 v11, s[76:79], s89 offen lds\n"
"s_add_u32 m0, s52, 57344\n"
"s_mov_b32 s89, 0x140000\n"
"buffer_load_dwordx4 v11, s[76:79], s89 offen lds\n"
"s_add_u32 m0, s52, 61440\n"
"s_mov_b32 s89, 0x160000\n"
"buffer_load_dwordx4 v11, s[76:79], s89 offen lds\n"
"s_add_u32 m0, s52, 65536\n"
"s_mov_b32 s89, 0x180000\n"
"buffer_load_dwordx4 v11, s[76:79], s89 offen lds\n"
"s_add_u32 m0, s52, 69632\n"
"s_mov_b32 s89, 0x1a0000\n"
"buffer_load_dwordx4 v11, s[76:79], s89 offen lds\n"
"s_add_u32 m0, s52, 73728\n"
"s_mov_b32 s89, 0x1c0000\n"
"buffer_load_dwordx4 v11, s[76:79], s89 offen lds\n"
"s_add_u32 m0, s52, 77824\n"
"s_mov_b32 s89, 0x1e0000\n"
"buffer_load_dwordx4 v11, s[76:79], s89 offen lds\n"
"s_add_u32 m0, s52, 81920\n"
"s_mov_b32 s89, 0x200000\n"
"buffer_load_dwordx4 v11, s[76:79], s89 offen lds\n"
"s_add_u32 m0, s52, 86016\n"
"s_mov_b32 s89, 0x220000\n"
"buffer_load_dwordx4 v11, s[76:79], s89 offen lds\n"
"s_add_u32 m0, s52, 90112\n"
"s_mov_b32 s89, 0x240000\n"
"buffer_load_dwordx4 v11, s[76:79], s89 offen lds\n"
"s_add_u32 m0, s52, 94208\n"
"s_mov_b32 s89, 0x260000\n"
"buffer_load_dwordx4 v11, s[76:79], s89 offen lds\n"
"v_mfma_f32_32x32x16_bf16 v[16:31], v[112:115], a[16:19], v[16:31]\n"
"v_mfma_f32_32x32x16_bf16 v[16:31], v[112:115], a[20:23], v[16:31]\n"
"v_mfma_f32_32x32x16_bf16 v[16:31], v[112:115], a[24:27], v[16:31]\n"
"v_mfma_f32_32x32x16_bf16 v[16:31], v[112:115], a[28:31], v[16:31]\n"
"v_mfma_f32_32x32x16_bf16 v[16:31], v[112:115], a[32:35], v[16:31]\n"
"v_mfma_f32_32x32x16_bf16 v[16:31], v[112:115], a[36:39], v[16:31]\n"
"v_mfma_f32_32x32x16_bf16 v[16:31], v[112:115], a[40:43], v[16:31]\n"
"v_mfma_f32_32x32x16_bf16 v[16:31], v[112:115], a[44:47], v[16:31]\n"
"v_mfma_f32_32x32x16_bf16 v[16:31], v[112:115], a[48:51], v[16:31]\n"
"v_mfma_f32_32x32x16_bf16 v[16:31], v[112:115], a[52:55], v[16:31]\n"
"v_mfma_f32_32x32x16_bf16 v[16:31], v[112:115], a[56:59], v[16:31]\n"
"v_mfma_f32_32x32x16_bf16 v[16:31], v[112:115], a[60:63], v[16:31]\n"
"s_waitcnt vmcnt(25)\n"
"s_barrier\n"
"v_add_u32 v124, 0x18000, v12\n"
"ds_read_b64_tr_b16 a[64:65], v124 offset:0\n"
"ds_read_b64_tr_b16 a[66:67], v124 offset:1024\n"
"ds_read_b64_tr_b16 a[68:69], v124 offset:4096\n"
"ds_read_b64_tr_b16 a[70:71], v124 offset:5120\n"
"ds_read_b64_tr_b16 a[72:73], v124 offset:8192\n"
"ds_read_b64_tr_b16 a[74:75], v124 offset:9216\n"
"ds_read_b64_tr_b16 a[76:77], v124 offset:12288\n"
"ds_read_b64_tr_b16 a[78:79], v124 offset:13312\n"
"s_waitcnt lgkmcnt(6)\n"
"v_add_u32 v124, 0x1c000, v12\n"
"ds_read_b64_tr_b16 a[80:81], v124 offset:0\n"
"ds_read_b64_tr_b16 a[82:83], v124 offset:1024\n"
"ds_read_b64_tr_b16 a[84:85], v124 offset:4096\n"
"ds_read_b64_tr_b16 a[86:87], v124 offset:5120\n"
"ds_read_b64_tr_b16 a[88:89], v124 offset:8192\n"
"ds_read_b64_tr_b16 a[90:91], v124 offset:9216\n"
"ds_read_b64_tr_b16 a[92:93], v124 offset:12288\n"
"ds_read_b64_tr_b16 a[94:95], v124 offset:13312\n"
"s_waitcnt lgkmcnt(6)\n"
"v_add_u32 v124, 0x20000, v12\n"
"ds_read_b64_tr_b16 a[96:97], v124 offset:0\n"
"ds_read_b64_tr_b16 a[98:99], v124 offset:1024\n"
"ds_read_b64_tr_b16 a[100:101], v124 offset:4096\n"
"ds_read_b64_tr_b16 a[102:103], v124 offset:5120\n"
"ds_read_b64_tr_b16 a[104:105], v124 offset:8192\n"
"ds_read_b64_tr_b16 a[106:107], v124 offset:9216\n"
"ds_read_b64_tr_b16 a[108:109], v124 offset:12288\n"
"ds_read_b64_tr_b16 a[110:111], v124 offset:13312\n"
"s_waitcnt lgkmcnt(0)\n"
"s_barrier\n"
"s_add_u32 m0, s52, 98304\n"
"s_mov_b32 s89, 0x280000\n"
"buffer_load_dwordx4 v11, s[76:79], s89 offen lds\n"
"s_add_u32 m0, s52, 102400\n"
"s_mov_b32 s89, 0x2a0000\n"
"buffer_load_dwordx4 v11, s[76:79], s89 offen lds\n"
"s_add_u32 m0, s52, 106496\n"
"s_mov_b32 s89, 0x2c0000\n"
"buffer_load_dwordx4 v11, s[76:79], s89 offen lds\n"
"s_add_u32 m0, s52, 110592\n"
"s_mov_b32 s89, 0x2e0000\n"
"buffer_load_dwordx4 v11, s[76:79], s89 offen lds\n"
"s_add_u32 m0, s52, 114688\n"
"s_mov_b32 s89, 0x300000\n"
"buffer_load_dwordx4 v11, s[76:79], s89 offen lds\n"
"s_add_u32 m0, s52, 118784\n"
"s_mov_b32 s89, 0x320000\n"
"buffer_load_dwordx4 v11, s[76:79], s89 offen lds\n"
"s_add_u32 m0, s52, 122880\n"
"s_mov_b32 s89, 0x340000\n"
"buffer_load_dwordx4 v11, s[76:79], s89 offen lds\n"
"s_add_u32 m0, s52, 126976\n"
"s_mov_b32 s89, 0x360000\n"
"buffer_load_dwordx4 v11, s[76:79], s89 offen lds\n"
"s_add_u32 m0, s52, 131072\n"
"s_mov_b32 s89, 0x380000\n"
"buffer_load_dwordx4 v11, s[76:79], s89 offen lds\n"
"s_add_u32 m0, s52, 135168\n"
"s_mov_b32 s89, 0x3a0000\n"
"buffer_load_dwordx4 v11, s[76:79], s89 offen lds\n"
"s_add_u32 m0, s52, 139264\n"
"s_mov_b32 s89, 0x3c0000\n"
"buffer_load_dwordx4 v11, s[76:79], s89 offen lds\n"
"s_add_u32 m0, s52, 143360\n"
"s_mov_b32 s89, 0x3e0000\n"
"buffer_load_dwordx4 v11, s[76:79], s89 offen lds\n"
"v_mfma_f32_32x32x16_bf16 v[16:31], v[112:115], a[64:67], v[16:31]\n"
"v_mfma_f32_32x32x16_bf16 v[16:31], v[112:115], a[68:71], v[16:31]\n"
"v_mfma_f32_32x32x16_bf16 v[16:31], v[112:115], a[72:75], v[16:31]\n"
"v_mfma_f32_32x32x16_bf16 v[16:31], v[112:115], a[76:79], v[16:31]\n"
"v_mfma_f32_32x32x16_bf16 v[16:31], v[112:115], a[80:83], v[16:31]\n"
"v_mfma_f32_32x32x16_bf16 v[16:31], v[112:115], a[84:87], v[16:31]\n"
"v_mfma_f32_32x32x16_bf16 v[16:31], v[112:115], a[88:91], v[16:31]\n"
"v_mfma_f32_32x32x16_bf16 v[16:31], v[112:115], a[92:95], v[16:31]\n"
"v_mfma_f32_32x32x16_bf16 v[16:31], v[112:115], a[96:99], v[16:31]\n"
"v_mfma_f32_32x32x16_bf16 v[16:31], v[112:115], a[100:103], v[16:31]\n"
"v_mfma_f32_32x32x16_bf16 v[16:31], v[112:115], a[104:107], v[16:31]\n"
"v_mfma_f32_32x32x16_bf16 v[16:31], v[112:115], a[108:111], v[16:31]\n"
"s_waitcnt vmcnt(24)\n"
"s_barrier\n"
"v_add_u32 v124, 0x0, v12\n"
"ds_read_b64_tr_b16 a[112:113], v124 offset:0\n"
"ds_read_b64_tr_b16 a[114:115], v124 offset:1024\n"
"ds_read_b64_tr_b16 a[116:117], v124 offset:4096\n"
"ds_read_b64_tr_b16 a[118:119], v124 offset:5120\n"
"ds_read_b64_tr_b16 a[120:121], v124 offset:8192\n"
"ds_read_b64_tr_b16 a[122:123], v124 offset:9216\n"
"ds_read_b64_tr_b16 a[124:125], v124 offset:12288\n"
"ds_read_b64_tr_b16 a[126:127], v124 offset:13312\n"
"s_waitcnt lgkmcnt(6)\n"
"v_add_u32 v124, 0x4000, v13\n"
"ds_read_b64_tr_b16 a[128:129], v124 offset:0\n"
"ds_read_b64_tr_b16 a[130:131], v124 offset:1024\n"
"ds_read_b64_tr_b16 a[132:133], v124 offset:4096\n"
"ds_read_b64_tr_b16 a[134:135], v124 offset:5120\n"
"ds_read_b64_tr_b16 a[136:137], v124 offset:8192\n"
"ds_read_b64_tr_b16 a[138:139], v124 offset:9216\n"
"ds_read_b64_tr_b16 a[140:141], v124 offset:12288\n"
"ds_read_b64_tr_b16 a[142:143], v124 offset:13312\n"
"s_waitcnt lgkmcnt(6)\n"
"v_add_u32 v124, 0x8000, v13\n"
"ds_read_b64_tr_b16 a[144:145], v124 offset:0\n"
"ds_read_b64_tr_b16 a[146:147], v124 offset:1024\n"
"ds_read_b64_tr_b16 a[148:149], v124 offset:4096\n"
"ds_read_b64_tr_b16 a[150:151], v124 offset:5120\n"
"ds_read_b64_tr_b16 a[152:153], v124 offset:8192\n"
"ds_read_b64_tr_b16 a[154:155], v124 offset:9216\n"
"ds_read_b64_tr_b16 a[156:157], v124 offset:12288\n"
"ds_read_b64_tr_b16 a[158:159], v124 offset:13312\n"
"s_waitcnt lgkmcnt(0)\n"
"s_barrier\n"
"s_mov_b32 s46, 0\n"
"v_cmp_ne_u32 vcc, s36, v89\n"
"s_bitcmp1_b32 vcc_lo, 0\n"
"s_cbranch_scc1 Lqne%=\n"
"s_mov_b32 s46, 1\n"
"s_add_u32 m0, s49, 0\n"
"s_add_u32 s89, s48, 0\n"
"buffer_load_dwordx4 v0, s[56:59], s89 offen sc1 lds\n"
"s_add_u32 m0, s49, 1040\n"
"s_add_u32 s89, s48, 1024\n"
"buffer_load_dwordx4 v0, s[56:59], s89 offen sc1 lds\n"
"s_add_u32 m0, s49, 2080\n"
"s_add_u32 s89, s48, 2048\n"
"buffer_load_dwordx4 v0, s[56:59], s89 offen sc1 lds\n"
"s_add_u32 m0, s49, 3120\n"
"s_add_u32 s89, s48, 3072\n"
"buffer_load_dwordx4 v0, s[56:59], s89 offen sc1 lds\n"
"s_add_u32 m0, s49, 4160\n"
"s_add_u32 s89, s68, 0\n"
"buffer_load_dwordx4 v0, s[56:59], s89 offen sc1 lds\n"
"s_add_u32 m0, s49, 5200\n"
"s_add_u32 s89, s68, 1024\n"
"buffer_load_dwordx4 v0, s[56:59], s89 offen sc1 lds\n"
"s_add_u32 m0, s49, 6240\n"
"s_add_u32 s89, s68, 2048\n"
"buffer_load_dwordx4 v0, s[56:59], s89 offen sc1 lds\n"
"s_add_u32 m0, s49, 7280\n"
"s_add_u32 s89, s68, 3072\n"
"buffer_load_dwordx4 v0, s[56:59], s89 offen sc1 lds\n"
"s_mov_b32 m0, s54\n"
"s_xor_b32 s54, s54, 0x10000\n"
"buffer_load_dwordx4 v6, s[40:43], s53 offen sc1 lds\n"
"s_add_u32 s62, s62, s64\n"
"s_add_u32 s63, s63, s65\n"
"s_xor_b32 s49, s49, 0x10000\n"
"s_lshr_b32 s89, s62, 8\n"
"s_xor_b32 s89, s89, s67\n"
"s_and_b32 s89, s89, s55\n"
"s_add_u32 s48, s62, s89\n"
"s_add_u32 s53, s63, s89\n"
"s_add_u32 s68, s48, s66\n"
"Lqne%=:\n"
"s_mov_b32 s33, 4917504\n"
"buffer_load_dwordx2 v[88:89], off, s[40:43], s33 sc1\n"
"v_mfma_f32_32x32x16_bf16 v[16:31], v[112:115], a[112:115], v[16:31]\n"
"v_mfma_f32_32x32x16_bf16 v[16:31], v[112:115], a[116:119], v[16:31]\n"
"v_mfma_f32_32x32x16_bf16 v[16:31], v[112:115], a[120:123], v[16:31]\n"
"v_mfma_f32_32x32x16_bf16 v[16:31], v[112:115], a[124:127], v[16:31]\n"
"v_mfma_f32_32x32x16_bf16 v[32:47], v[112:115], a[128:131], 0\n"
"v_mfma_f32_32x32x16_bf16 v[32:47], v[112:115], a[132:135], v[32:47]\n"
"v_mfma_f32_32x32x16_bf16 v[32:47], v[112:115], a[136:139], v[32:47]\n"
"v_mfma_f32_32x32x16_bf16 v[32:47], v[112:115], a[140:143], v[32:47]\n"
"v_mfma_f32_32x32x16_bf16 v[32:47], v[112:115], a[144:147], v[32:47]\n"
"v_mfma_f32_32x32x16_bf16 v[32:47], v[112:115], a[148:151], v[32:47]\n"
"v_mfma_f32_32x32x16_bf16 v[32:47], v[112:115], a[152:155], v[32:47]\n"
"v_mfma_f32_32x32x16_bf16 v[32:47], v[112:115], a[156:159], v[32:47]\n"
"s_waitcnt vmcnt(12)\n"
"s_barrier\n"
"v_add_u32 v124, 0xc000, v13\n"
"ds_read_b64_tr_b16 a[160:161], v124 offset:0\n"
"ds_read_b64_tr_b16 a[162:163], v124 offset:1024\n"
"ds_read_b64_tr_b16 a[164:165], v124 offset:4096\n"
"ds_read_b64_tr_b16 a[166:167], v124 offset:5120\n"
"ds_read_b64_tr_b16 a[168:169], v124 offset:8192\n"
"ds_read_b64_tr_b16 a[170:171], v124 offset:9216\n"
"ds_read_b64_tr_b16 a[172:173], v124 offset:12288\n"
"ds_read_b64_tr_b16 a[174:175], v124 offset:13312\n"
"s_waitcnt lgkmcnt(6)\n"
"v_add_u32 v124, 0x10000, v13\n"
"ds_read_b64_tr_b16 a[176:177], v124 offset:0\n"
"ds_read_b64_tr_b16 a[178:179], v124 offset:1024\n"
"ds_read_b64_tr_b16 a[180:181], v124 offset:4096\n"
"ds_read_b64_tr_b16 a[182:183], v124 offset:5120\n"
"ds_read_b64_tr_b16 a[184:185], v124 offset:8192\n"
"ds_read_b64_tr_b16 a[186:187], v124 offset:9216\n"
"ds_read_b64_tr_b16 a[188:189], v124 offset:12288\n"
"ds_read_b64_tr_b16 a[190:191], v124 offset:13312\n"
"s_waitcnt lgkmcnt(6)\n"
"v_add_u32 v124, 0x14000, v13\n"
"ds_read_b64_tr_b16 a[192:193], v124 offset:0\n"
"ds_read_b64_tr_b16 a[194:195], v124 offset:1024\n"
"ds_read_b64_tr_b16 a[196:197], v124 offset:4096\n"
"ds_read_b64_tr_b16 a[198:199], v124 offset:5120\n"
"ds_read_b64_tr_b16 a[200:201], v124 offset:8192\n"
"ds_read_b64_tr_b16 a[202:203], v124 offset:9216\n"
"ds_read_b64_tr_b16 a[204:205], v124 offset:12288\n"
"ds_read_b64_tr_b16 a[206:207], v124 offset:13312\n"
"s_waitcnt lgkmcnt(0)\n"
"s_barrier\n"
"v_mfma_f32_32x32x16_bf16 v[32:47], v[112:115], a[160:163], v[32:47]\n"
"v_mfma_f32_32x32x16_bf16 v[32:47], v[112:115], a[164:167], v[32:47]\n"
"v_mfma_f32_32x32x16_bf16 v[32:47], v[112:115], a[168:171], v[32:47]\n"
"v_mfma_f32_32x32x16_bf16 v[32:47], v[112:115], a[172:175], v[32:47]\n"
"v_mfma_f32_32x32x16_bf16 v[32:47], v[112:115], a[176:179], v[32:47]\n"
"v_mfma_f32_32x32x16_bf16 v[32:47], v[112:115], a[180:183], v[32:47]\n"
"v_mfma_f32_32x32x16_bf16 v[32:47], v[112:115], a[184:187], v[32:47]\n"
"v_mfma_f32_32x32x16_bf16 v[32:47], v[112:115], a[188:191], v[32:47]\n"
"v_mfma_f32_32x32x16_bf16 v[32:47], v[112:115], a[192:195], v[32:47]\n"
"v_mfma_f32_32x32x16_bf16 v[32:47], v[112:115], a[196:199], v[32:47]\n"
"v_mfma_f32_32x32x16_bf16 v[32:47], v[112:115], a[200:203], v[32:47]\n"
"v_mfma_f32_32x32x16_bf16 v[32:47], v[112:115], a[204:207], v[32:47]\n"
"s_waitcnt vmcnt(0)\n"
"s_barrier\n"
"v_add_u32 v124, 0x18000, v13\n"
"ds_read_b64_tr_b16 a[208:209], v124 offset:0\n"
"ds_read_b64_tr_b16 a[210:211], v124 offset:1024\n"
"ds_read_b64_tr_b16 a[212:213], v124 offset:4096\n"
"ds_read_b64_tr_b16 a[214:215], v124 offset:5120\n"
"ds_read_b64_tr_b16 a[216:217], v124 offset:8192\n"
"ds_read_b64_tr_b16 a[218:219], v124 offset:9216\n"
"ds_read_b64_tr_b16 a[220:221], v124 offset:12288\n"
"ds_read_b64_tr_b16 a[222:223], v124 offset:13312\n"
"s_waitcnt lgkmcnt(6)\n"
"v_add_u32 v124, 0x1c000, v13\n"
"ds_read_b64_tr_b16 a[224:225], v124 offset:0\n"
"ds_read_b64_tr_b16 a[226:227], v124 offset:1024\n"
"ds_read_b64_tr_b16 a[228:229], v124 offset:4096\n"
"ds_read_b64_tr_b16 a[230:231], v124 offset:5120\n"
"ds_read_b64_tr_b16 a[232:233], v124 offset:8192\n"
"ds_read_b64_tr_b16 a[234:235], v124 offset:9216\n"
"ds_read_b64_tr_b16 a[236:237], v124 offset:12288\n"
"ds_read_b64_tr_b16 a[238:239], v124 offset:13312\n"
"s_waitcnt lgkmcnt(6)\n"
"v_add_u32 v124, 0x20000, v13\n"
"ds_read_b64_tr_b16 a[240:241], v124 offset:0\n"
"ds_read_b64_tr_b16 a[242:243], v124 offset:1024\n"
"ds_read_b64_tr_b16 a[244:245], v124 offset:4096\n"
"ds_read_b64_tr_b16 a[246:247], v124 offset:5120\n"
"ds_read_b64_tr_b16 a[248:249], v124 offset:8192\n"
"ds_read_b64_tr_b16 a[250:251], v124 offset:9216\n"
"ds_read_b64_tr_b16 a[252:253], v124 offset:12288\n"
"ds_read_b64_tr_b16 a[254:255], v124 offset:13312\n"
"s_waitcnt lgkmcnt(0)\n"
"s_barrier\n"
"v_mfma_f32_32x32x16_bf16 v[32:47], v[112:115], a[208:211], v[32:47]\n"
"v_mfma_f32_32x32x16_bf16 v[32:47], v[112:115], a[212:215], v[32:47]\n"
"v_mfma_f32_32x32x16_bf16 v[32:47], v[112:115], a[216:219], v[32:47]\n"
"v_mfma_f32_32x32x16_bf16 v[32:47], v[112:115], a[220:223], v[32:47]\n"
"v_mfma_f32_32x32x16_bf16 v[32:47], v[112:115], a[224:227], v[32:47]\n"
"v_mfma_f32_32x32x16_bf16 v[32:47], v[112:115], a[228:231], v[32:47]\n"
"v_mfma_f32_32x32x16_bf16 v[32:47], v[112:115], a[232:235], v[32:47]\n"
"v_mfma_f32_32x32x16_bf16 v[32:47], v[112:115], a[236:239], v[32:47]\n"
"v_mfma_f32_32x32x16_bf16 v[32:47], v[112:115], a[240:243], v[32:47]\n"
"v_mfma_f32_32x32x16_bf16 v[32:47], v[112:115], a[244:247], v[32:47]\n"
"v_mfma_f32_32x32x16_bf16 v[32:47], v[112:115], a[248:251], v[32:47]\n"
"v_mfma_f32_32x32x16_bf16 v[32:47], v[112:115], a[252:255], v[32:47]\n"
"s_nop 7\n"
"s_nop 3\n"
"v_cvt_pk_bf16_f32 v20, v16, v16\n"
"v_and_b32 v21, 0xffff0000, v20\n"
"v_sub_f32 v21, v16, v21\n"
"v_cvt_pk_bf16_f32 v20, v16, v21\n"
"v_cndmask_b32 v20, v20, 0, s[60:61]\n"
"v_mov_b32 v21, v20\n"
"v_mov_b64 v[22:23], 0\n"
"v_cvt_pk_bf16_f32 v36, v32, v32\n"
"v_and_b32 v37, 0xffff0000, v36\n"
"v_sub_f32 v37, v32, v37\n"
"v_cvt_pk_bf16_f32 v36, v32, v37\n"
"v_cndmask_b32 v36, v36, 0, s[60:61]\n"
"v_mov_b32 v37, v36\n"
"v_mov_b64 v[38:39], 0\n"
"v_cvt_pk_bf16_f32 v52, v48, v48\n"
"v_and_b32 v53, 0xffff0000, v52\n"
"v_sub_f32 v53, v48, v53\n"
"v_cvt_pk_bf16_f32 v52, v48, v53\n"
"v_cndmask_b32 v52, v52, 0, s[60:61]\n"
"v_mov_b32 v53, v52\n"
"v_mov_b64 v[54:55], 0\n"
"ds_write_b128 v5, v[20:23] offset:0\n"
"ds_write_b128 v5, v[36:39] offset:1024\n"
"ds_write_b128 v5, v[52:55] offset:2048\n"
"v_lshrrev_b32 v11, 2, v0\n"
"v_and_b32 v11, 0x7f, v11\n"
"v_and_b32 v13, 12, v11\n"
"v_add_u32 v13, 0xcc00, v13\n"
"s_lshr_b32 s89, s52, 8\n"
"s_add_u32 s89, s89, 0xcc00\n"
"v_mov_b32 v12, s89\n"
"v_mov_b32 v14, -2\n"
"ds_write_b32 v12, v14\n"
"s_waitcnt lgkmcnt(0)\n"
"v_min_u32 v11, 1, v0\n"
"v_lshlrev_b32 v11, 30, v11\n"
"s_mov_b32 s33, 4917504\n"
"s_mov_b32 s88, 0\n"
"s_branch Lqgp%=\n"
"Lqgo%=:\n"
"buffer_load_dwordx2 v[88:89], off, s[40:43], s33 sc1\n"
"s_add_u32 s88, s88, 1\n"
"s_cmp_gt_u32 s88, 131072\n"
"s_cbranch_scc1 Lqend%=\n"
"Lqgp%=:\n"
"s_waitcnt vmcnt(0)\n"
"v_cmp_ne_u32 vcc, s36, v88\n"
"s_cbranch_vccnz Lqgo%=\n"
"v_readfirstlane_b32 s99, v89\n"
"s_lshl_b32 s33, s99, 13\n"
"s_xor_b32 s99, s99, s37\n"
"s_and_b32 s33, s33, s55\n"
"s_xor_b32 s47, s47, s33\n"
"s_xor_b32 s67, s67, s33\n"
"s_lshr_b32 s89, s62, 8\n"
"s_xor_b32 s89, s89, s67\n"
"s_and_b32 s89, s89, s55\n"
"s_add_u32 s48, s62, s89\n"
"s_add_u32 s53, s63, s89\n"
"s_add_u32 s68, s48, s66\n"
"s_mov_b32 s92, s50\n"
"s_mov_b32 s91, 0x80000000\n"
"s_lshl_b32 s93, s99, 13\n"
"s_xor_b32 s93, s93, s51\n"
"v_mov_b32 v127, -1\n"
"s_bfe_u32 s50, s92, 0x10015\n"
"s_xor_b32 s50, s50, s99\n"
"s_lshl_b32 s50, s50, 13\n"
"s_add_u32 s50, s50, s92\n"
"s_barrier\n"
"s_mov_b32 s44, -1\n"
"s_cmp_eq_u32 s46, 1\n"
"s_mov_b32 s46, 0x80000000\n"
"s_cbranch_scc0 Lqnext%=\n"
"s_branch Lqland%=\n"
"Lqloop%=:\n"
"v_mfma_f32_32x32x16_bf16 v[16:31], a[0:3], v[64:67], 0\n"
"ds_read_b128 v[84:87], v1 offset:160\n"
"buffer_load_dword v14, v10, s[40:43], s47 offen sc1\n"
"v_mfma_f32_32x32x16_bf16 v[32:47], v[64:67], a[128:131], 0\n"
"s_waitcnt lgkmcnt(4)\n"
"ds_read2_b64 v[88:91], v7 offset0:0 offset1:8\n"
"v_mul_f32 v48, v15, v48\n"
"v_mul_f32 v52, v15, v52\n"
"v_cvt_pk_bf16_f32 v116, v48, v52\n"
"v_mfma_f32_32x32x16_bf16 v[16:31], a[4:7], v[68:71], v[16:31]\n"
"ds_read_b128 v[64:67], v1 offset:192\n"
"v_mul_f32 v56, v15, v56\n"
"v_mul_f32 v60, v15, v60\n"
"v_cvt_pk_bf16_f32 v117, v56, v60\n"
"v_mul_f32 v49, v15, v49\n"
"v_mfma_f32_32x32x16_bf16 v[32:47], v[68:71], a[132:135], v[32:47]\n"
"s_waitcnt lgkmcnt(5)\n"
"ds_read2_b64 v[92:95], v7 offset0:16 offset1:24\n"
"v_mul_f32 v53, v15, v53\n"
"v_cvt_pk_bf16_f32 v118, v49, v53\n"
"v_mul_f32 v57, v15, v57\n"
"v_mfma_f32_32x32x16_bf16 v[16:31], a[8:11], v[72:75], v[16:31]\n"
"ds_read_b128 v[68:71], v1 offset:224\n"
"v_mul_f32 v61, v15, v61\n"
"v_cvt_pk_bf16_f32 v119, v57, v61\n"
"v_mul_f32 v50, v15, v50\n"
"v_mul_f32 v54, v15, v54\n"
"v_mfma_f32_32x32x16_bf16 v[32:47], v[72:75], a[136:139], v[32:47]\n"
"s_waitcnt lgkmcnt(6)\n"
"buffer_store_dwordx4 v[116:119], v4, s[40:43], s91 offen offset:0 sc1\n"
"v_cvt_pk_bf16_f32 v120, v50, v54\n"
"v_mul_f32 v58, v15, v58\n"
"v_mul_f32 v62, v15, v62\n"
"v_mfma_f32_32x32x16_bf16 v[16:31], a[12:15], v[76:79], v[16:31]\n"
"ds_read_b128 v[72:75], v1 offset:256\n"
"v_cvt_pk_bf16_f32 v121, v58, v62\n"
"v_mul_f32 v51, v15, v51\n"
"v_mul_f32 v55, v15, v55\n"
"v_cvt_pk_bf16_f32 v122, v51, v55\n"
"v_mfma_f32_32x32x16_bf16 v[32:47], v[76:79], a[140:143], v[32:47]\n"
"s_waitcnt lgkmcnt(6)\n"
"v_mul_f32 v59, v15, v59\n"
"v_mul_f32 v63, v15, v63\n"
"v_cvt_pk_bf16_f32 v123, v59, v63\n"
"v_mfma_f32_32x32x16_bf16 v[16:31], a[16:19], v[80:83], v[16:31]\n"
"ds_read_b128 v[76:79], v1 offset:288\n"
"buffer_store_dwordx4 v[120:123], v4, s[40:43], s91 offen offset:256 sc1\n"
"v_mfma_f32_32x32x16_bf16 v[32:47], v[80:83], a[144:147], v[32:47]\n"
"s_waitcnt lgkmcnt(6)\n"
"s_waitcnt lgkmcnt(3)\n"
"v_add_f32 v88, v88, v92\n"
"v_add_f32 v89, v89, v93\n"
"v_add_f32 v90, v90, v94\n"
"v_mfma_f32_32x32x16_bf16 v[16:31], a[20:23], v[84:87], v[16:31]\n"
"ds_read_b128 v[80:83], v1 offset:320\n"
"v_add_f32 v91, v91, v95\n"
"v_add_f32 v88, v88, v90\n"
"v_add_f32 v89, v89, v91\n"
"v_mov_b32 v124, v88\n"
"v_mfma_f32_32x32x16_bf16 v[32:47], v[84:87], a[148:151], v[32:47]\n"
"v_mov_b32 v125, v89\n"
"s_nop 0\n"
"v_permlane32_swap_b32 v88, v124\n"
"v_permlane32_swap_b32 v89, v125\n"
"v_mfma_f32_32x32x16_bf16 v[16:31], a[24:27], v[64:67], v[16:31]\n"
"ds_read_b128 v[84:87], v1 offset:352\n"
"v_add_f32 v88, v88, v124\n"
"v_add_f32 v89, v89, v125\n"
"v_mul_f32 v124, 0xbb000000, v88\n"
"v_mul_f32 v125, 0x3b000000, v89\n"
"v_mfma_f32_32x32x16_bf16 v[32:47], v[64:67], a[152:155], v[32:47]\n"
"s_waitcnt lgkmcnt(4)\n"
"v_fma_f32 v125, -v124, v124, v125\n"
"v_max_f32 v125, 0, v125\n"
"v_add_f32 v125, 0x3727c5ac, v125\n"
"v_rsq_f32 v15, v125\n"
"v_mfma_f32_32x32x16_bf16 v[16:31], a[28:31], v[68:71], v[16:31]\n"
"ds_read_b128 v[64:67], v1 offset:384\n"
"v_cvt_pk_bf16_f32 v112, v124, v124\n"
"v_and_b32 v126, 0xffff0000, v112\n"
"v_sub_f32 v126, v124, v126\n"
"v_cvt_pk_bf16_f32 v113, v126, v126\n"
"v_mfma_f32_32x32x16_bf16 v[32:47], v[68:71], a[156:159], v[32:47]\n"
"s_waitcnt lgkmcnt(4)\n"
"v_mov_b64 v[114:115], 0\n"
"v_mfma_f32_32x32x16_bf16 v[16:31], a[32:35], v[72:75], v[16:31]\n"
"ds_read_b128 v[68:71], v1 offset:416\n"
"ds_write_b32 v8, v15\n"
"v_mfma_f32_32x32x16_bf16 v[32:47], v[72:75], a[160:163], v[32:47]\n"
"s_waitcnt lgkmcnt(5)\n"
"ds_read_b128 v[96:99], v9 offset:0\n"
"v_mfma_f32_32x32x16_bf16 v[16:31], a[36:39], v[76:79], v[16:31]\n"
"ds_read_b128 v[72:75], v1 offset:448\n"
"ds_read_b128 v[100:103], v9 offset:32\n"
"v_mfma_f32_32x32x16_bf16 v[32:47], v[76:79], a[164:167], v[32:47]\n"
"s_waitcnt lgkmcnt(7)\n"
"ds_read_b128 v[104:107], v9 offset:64\n"
"v_mfma_f32_32x32x16_bf16 v[16:31], a[40:43], v[80:83], v[16:31]\n"
"ds_read_b128 v[76:79], v1 offset:480\n"
"ds_read_b128 v[108:111], v9 offset:96\n"
"v_mfma_f32_32x32x16_bf16 v[32:47], v[80:83], a[168:171], v[32:47]\n"
"s_waitcnt lgkmcnt(9)\n"
"v_mfma_f32_32x32x16_bf16 v[16:31], a[44:47], v[84:87], v[16:31]\n"
"ds_read_b128 v[80:83], v1 offset:512\n"
"v_mfma_f32_32x32x16_bf16 v[32:47], v[84:87], a[172:175], v[32:47]\n"
"s_waitcnt lgkmcnt(9)\n"
"v_mfma_f32_32x32x16_bf16 v[16:31], a[48:51], v[64:67], v[16:31]\n"
"ds_read_b128 v[84:87], v1 offset:544\n"
"v_mfma_f32_32x32x16_bf16 v[32:47], v[64:67], a[176:179], v[32:47]\n"
"s_waitcnt lgkmcnt(9)\n"
"v_mfma_f32_32x32x16_bf16 v[16:31], a[52:55], v[68:71], v[16:31]\n"
"ds_read_b128 v[64:67], v1 offset:576\n"
"v_mfma_f32_32x32x16_bf16 v[32:47], v[68:71], a[180:183], v[32:47]\n"
"s_waitcnt lgkmcnt(7)\n"
"v_mfma_f32_32x32x16_bf16 v[16:31], a[56:59], v[72:75], v[16:31]\n"
"ds_read_b128 v[68:71], v1 offset:608\n"
"v_mfma_f32_32x32x16_bf16 v[32:47], v[72:75], a[184:187], v[32:47]\n"
"s_barrier\n"
"s_waitcnt lgkmcnt(5)\n"
"v_mfma_f32_32x32x16_bf16 v[16:31], a[60:63], v[76:79], v[16:31]\n"
"ds_read_b128 v[72:75], v1 offset:640\n"
"s_waitcnt vmcnt(2)\n"
"v_cmp_ge_i32 vcc, s44, v14\n"
"s_cbranch_vccnz Lqn1%=\n"
"v_mfma_f32_32x32x16_bf16 v[32:47], v[76:79], a[188:191], v[32:47]\n"
"s_add_u32 m0, s49, 0\n"
"s_add_u32 s89, s48, 0\n"
"buffer_load_dwordx4 v0, s[56:59], s89 offen sc1 lds\n"
"s_waitcnt lgkmcnt(4)\n"
"v_mfma_f32_32x32x16_bf16 v[16:31], a[64:67], v[80:83], v[16:31]\n"
"s_add_u32 m0, s49, 1040\n"
"s_add_u32 s89, s48, 1024\n"
"buffer_load_dwordx4 v0, s[56:59], s89 offen sc1 lds\n"
"ds_read_b128 v[76:79], v1 offset:672\n"
"v_mfma_f32_32x32x16_bf16 v[32:47], v[80:83], a[192:195], v[32:47]\n"
"s_add_u32 m0, s49, 2080\n"
"s_add_u32 s89, s48, 2048\n"
"buffer_load_dwordx4 v0, s[56:59], s89 offen sc1 lds\n"
"s_waitcnt lgkmcnt(4)\n"
"v_mfma_f32_32x32x16_bf16 v[16:31], a[68:71], v[84:87], v[16:31]\n"
"s_add_u32 m0, s49, 3120\n"
"s_add_u32 s89, s48, 3072\n"
"buffer_load_dwordx4 v0, s[56:59], s89 offen sc1 lds\n"
"ds_read_b128 v[80:83], v1 offset:704\n"
"v_mfma_f32_32x32x16_bf16 v[32:47], v[84:87], a[196:199], v[32:47]\n"
"s_add_u32 m0, s49, 4160\n"
"s_add_u32 s89, s68, 0\n"
"buffer_load_dwordx4 v0, s[56:59], s89 offen sc1 lds\n"
"s_waitcnt lgkmcnt(4)\n"
"v_mfma_f32_32x32x16_bf16 v[16:31], a[72:75], v[64:67], v[16:31]\n"
"s_add_u32 m0, s49, 5200\n"
"s_add_u32 s89, s68, 1024\n"
"buffer_load_dwordx4 v0, s[56:59], s89 offen sc1 lds\n"
"ds_read_b128 v[84:87], v1 offset:736\n"
"v_mfma_f32_32x32x16_bf16 v[32:47], v[64:67], a[200:203], v[32:47]\n"
"s_add_u32 m0, s49, 6240\n"
"s_add_u32 s89, s68, 2048\n"
"buffer_load_dwordx4 v0, s[56:59], s89 offen sc1 lds\n"
"s_waitcnt lgkmcnt(4)\n"
"v_mfma_f32_32x32x16_bf16 v[16:31], a[76:79], v[68:71], v[16:31]\n"
"s_add_u32 m0, s49, 7280\n"
"s_add_u32 s89, s68, 3072\n"
"buffer_load_dwordx4 v0, s[56:59], s89 offen sc1 lds\n"
"ds_read_b128 v[64:67], v1 offset:768\n"
"v_mfma_f32_32x32x16_bf16 v[32:47], v[68:71], a[204:207], v[32:47]\n"
"s_mov_b32 m0, s54\n"
"s_xor_b32 s54, s54, 0x10000\n"
"buffer_load_dwordx4 v6, s[40:43], s53 offen sc1 lds\n"
"s_waitcnt lgkmcnt(4)\n"
"v_mfma_f32_32x32x16_bf16 v[16:31], a[80:83], v[72:75], v[16:31]\n"
"s_add_u32 s62, s62, s64\n"
"s_add_u32 s63, s63, s65\n"
"s_xor_b32 s49, s49, 0x10000\n"
"s_lshr_b32 s89, s62, 8\n"
"ds_read_b128 v[68:71], v1 offset:800\n"
"v_mfma_f32_32x32x16_bf16 v[32:47], v[72:75], a[208:211], v[32:47]\n"
"s_xor_b32 s89, s89, s67\n"
"s_and_b32 s89, s89, s55\n"
"s_add_u32 s48, s62, s89\n"
"s_add_u32 s53, s63, s89\n"
"s_add_u32 s68, s48, s66\n"
"s_waitcnt lgkmcnt(4)\n"
"v_mfma_f32_32x32x16_bf16 v[16:31], a[84:87], v[76:79], v[16:31]\n"
"ds_read_b128 v[72:75], v1 offset:832\n"
"v_mfma_f32_32x32x16_bf16 v[32:47], v[76:79], a[212:215], v[32:47]\n"
"s_waitcnt lgkmcnt(4)\n"
"v_mfma_f32_32x32x16_bf16 v[16:31], a[88:91], v[80:83], v[16:31]\n"
"s_waitcnt vmcnt(9)\n"
"buffer_store_dword v127, v11, s[40:43], s46 offen offset:16 sc1\n"
"s_mov_b32 s46, 0x80000000\n"
"ds_read_b128 v[76:79], v1 offset:864\n"
"v_mfma_f32_32x32x16_bf16 v[32:47], v[80:83], a[216:219], v[32:47]\n"
"s_waitcnt lgkmcnt(4)\n"
"v_mfma_f32_32x32x16_bf16 v[16:31], a[92:95], v[84:87], v[16:31]\n"
"ds_read_b128 v[80:83], v1 offset:896\n"
"v_mfma_f32_32x32x16_bf16 v[32:47], v[84:87], a[220:223], v[32:47]\n"
"s_waitcnt lgkmcnt(4)\n"
"v_mfma_f32_32x32x16_bf16 v[16:31], a[96:99], v[64:67], v[16:31]\n"
"ds_read_b128 v[84:87], v1 offset:928\n"
"v_mfma_f32_32x32x16_bf16 v[32:47], v[64:67], a[224:227], v[32:47]\n"
"s_waitcnt lgkmcnt(4)\n"
"v_mfma_f32_32x32x16_bf16 v[16:31], a[100:103], v[68:71], v[16:31]\n"
"ds_read_b128 v[64:67], v1 offset:960\n"
"v_mfma_f32_32x32x16_bf16 v[32:47], v[68:71], a[228:231], v[32:47]\n"
"s_waitcnt lgkmcnt(4)\n"
"v_mfma_f32_32x32x16_bf16 v[16:31], a[104:107], v[72:75], v[16:31]\n"
"ds_read_b128 v[68:71], v1 offset:992\n"
"v_mfma_f32_32x32x16_bf16 v[32:47], v[72:75], a[232:235], v[32:47]\n"
"s_waitcnt lgkmcnt(4)\n"
"v_mfma_f32_32x32x16_bf16 v[16:31], a[108:111], v[76:79], v[16:31]\n"
"ds_read_b128 v[72:75], v5 offset:0\n"
"v_mfma_f32_32x32x16_bf16 v[32:47], v[76:79], a[236:239], v[32:47]\n"
"s_waitcnt lgkmcnt(4)\n"
"v_mfma_f32_32x32x16_bf16 v[16:31], a[112:115], v[80:83], v[16:31]\n"
"ds_read_b128 v[76:79], v5 offset:1024\n"
"v_mfma_f32_32x32x16_bf16 v[32:47], v[80:83], a[240:243], v[32:47]\n"
"s_waitcnt lgkmcnt(4)\n"
"v_mfma_f32_32x32x16_bf16 v[16:31], a[116:119], v[84:87], v[16:31]\n"
"ds_read_b128 v[80:83], v1 offset:0\n"
"v_mfma_f32_32x32x16_bf16 v[32:47], v[84:87], a[244:247], v[32:47]\n"
"s_waitcnt lgkmcnt(4)\n"
"v_mfma_f32_32x32x16_bf16 v[16:31], a[120:123], v[64:67], v[16:31]\n"
"ds_read_b128 v[84:87], v1 offset:32\n"
"v_mfma_f32_32x32x16_bf16 v[32:47], v[64:67], a[248:251], v[32:47]\n"
"s_waitcnt lgkmcnt(4)\n"
"v_mfma_f32_32x32x16_bf16 v[16:31], a[124:127], v[68:71], v[16:31]\n"
"ds_read_b128 v[64:67], v1 offset:64\n"
"v_mfma_f32_32x32x16_bf16 v[32:47], v[68:71], a[252:255], v[32:47]\n"
"s_waitcnt lgkmcnt(4)\n"
"v_mfma_f32_32x32x16_bf16 v[16:31], v[72:75], v[112:115], v[16:31]\n"
"s_waitcnt lgkmcnt(3)\n"
"ds_read_b128 v[68:71], v1 offset:96\n"
"v_mfma_f32_32x32x16_bf16 v[32:47], v[112:115], v[76:79], v[32:47]\n"
"s_waitcnt lgkmcnt(2)\n"
"ds_read_b128 v[72:75], v1 offset:128\n"
"s_add_u32 s90, s50, 33554432\n"
"Lqp2%=:\n"
"v_mfma_f32_32x32x16_bf16 v[48:63], v[128:131], v[80:83], 0\n"
"ds_read_b128 v[76:79], v1 offset:160\n"
"v_mul_f32 v16, v15, v16\n"
"v_mul_f32 v20, v15, v20\n"
"v_cvt_pk_bf16_f32 v116, v16, v20\n"
"v_mul_f32 v24, v15, v24\n"
"v_mul_f32 v28, v15, v28\n"
"v_mfma_f32_32x32x16_bf16 v[48:63], v[132:135], v[84:87], v[48:63]\n"
"s_waitcnt lgkmcnt(2)\n"
"ds_read_b128 v[80:83], v1 offset:192\n"
"v_cvt_pk_bf16_f32 v117, v24, v28\n"
"v_mul_f32 v17, v15, v17\n"
"v_mul_f32 v21, v15, v21\n"
"v_cvt_pk_bf16_f32 v118, v17, v21\n"
"v_mfma_f32_32x32x16_bf16 v[48:63], v[136:139], v[64:67], v[48:63]\n"
"ds_read_b128 v[84:87], v1 offset:224\n"
"v_mul_f32 v25, v15, v25\n"
"v_mul_f32 v29, v15, v29\n"
"v_cvt_pk_bf16_f32 v119, v25, v29\n"
"v_mul_f32 v18, v15, v18\n"
"v_mul_f32 v22, v15, v22\n"
"v_mfma_f32_32x32x16_bf16 v[48:63], v[140:143], v[68:71], v[48:63]\n"
"s_waitcnt lgkmcnt(2)\n"
"ds_read_b128 v[64:67], v1 offset:256\n"
"buffer_store_dwordx4 v[116:119], v2, s[40:43], s50 offen offset:0 sc1\n"
"v_cvt_pk_bf16_f32 v120, v18, v22\n"
"v_mul_f32 v26, v15, v26\n"
"v_mul_f32 v30, v15, v30\n"
"v_mfma_f32_32x32x16_bf16 v[48:63], v[144:147], v[72:75], v[48:63]\n"
"ds_read_b128 v[68:71], v1 offset:288\n"
"v_cvt_pk_bf16_f32 v121, v26, v30\n"
"v_mul_f32 v19, v15, v19\n"
"v_mul_f32 v23, v15, v23\n"
"v_cvt_pk_bf16_f32 v122, v19, v23\n"
"v_mul_f32 v27, v15, v27\n"
"v_mfma_f32_32x32x16_bf16 v[48:63], v[148:151], v[76:79], v[48:63]\n"
"s_waitcnt lgkmcnt(2)\n"
"ds_read_b128 v[72:75], v1 offset:320\n"
"v_mul_f32 v31, v15, v31\n"
"v_cvt_pk_bf16_f32 v123, v27, v31\n"
"v_mul_f32 v32, v96, v32\n"
"v_mul_f32 v33, v97, v33\n"
"v_mfma_f32_32x32x16_bf16 v[48:63], v[152:155], v[80:83], v[48:63]\n"
"ds_read_b128 v[76:79], v1 offset:352\n"
"buffer_store_dwordx4 v[120:123], v2, s[40:43], s50 offen offset:256 sc1\n"
"v_cvt_pk_bf16_f32 v16, v32, v33\n"
"v_mul_f32 v34, v98, v34\n"
"v_mul_f32 v35, v99, v35\n"
"v_cvt_pk_bf16_f32 v17, v34, v35\n"
"v_mfma_f32_32x32x16_bf16 v[48:63], v[156:159], v[84:87], v[48:63]\n"
"s_waitcnt lgkmcnt(2)\n"
"ds_read_b128 v[80:83], v1 offset:384\n"
"v_mul_f32 v36, v100, v36\n"
"v_mul_f32 v37, v101, v37\n"
"v_cvt_pk_bf16_f32 v18, v36, v37\n"
"v_mul_f32 v38, v102, v38\n"
"v_mfma_f32_32x32x16_bf16 v[48:63], v[160:163], v[64:67], v[48:63]\n"
"ds_read_b128 v[84:87], v1 offset:416\n"
"v_mul_f32 v39, v103, v39\n"
"v_cvt_pk_bf16_f32 v19, v38, v39\n"
"v_mul_f32 v40, v104, v40\n"
"v_mul_f32 v41, v105, v41\n"
"v_cvt_pk_bf16_f32 v20, v40, v41\n"
"v_mfma_f32_32x32x16_bf16 v[48:63], v[164:167], v[68:71], v[48:63]\n"
"s_waitcnt lgkmcnt(2)\n"
"ds_read_b128 v[64:67], v1 offset:448\n"
"buffer_store_dwordx4 v[16:19], v3, s[40:43], s90 offen offset:0 sc1\n"
"v_mul_f32 v42, v106, v42\n"
"v_mul_f32 v43, v107, v43\n"
"v_cvt_pk_bf16_f32 v21, v42, v43\n"
"v_mfma_f32_32x32x16_bf16 v[48:63], v[168:171], v[72:75], v[48:63]\n"
"ds_read_b128 v[68:71], v1 offset:480\n"
"v_mul_f32 v44, v108, v44\n"
"v_mul_f32 v45, v109, v45\n"
"v_cvt_pk_bf16_f32 v22, v44, v45\n"
"v_mul_f32 v46, v110, v46\n"
"v_mul_f32 v47, v111, v47\n"
"v_mfma_f32_32x32x16_bf16 v[48:63], v[172:175], v[76:79], v[48:63]\n"
"s_waitcnt lgkmcnt(2)\n"
"ds_read_b128 v[72:75], v1 offset:512\n"
"v_cvt_pk_bf16_f32 v23, v46, v47\n"
"v_mfma_f32_32x32x16_bf16 v[48:63], v[176:179], v[80:83], v[48:63]\n"
"ds_read_b128 v[76:79], v1 offset:544\n"
"buffer_store_dwordx4 v[20:23], v3, s[40:43], s90 offen offset:256 sc1\n"
"v_mfma_f32_32x32x16_bf16 v[48:63], v[180:183], v[84:87], v[48:63]\n"
"s_waitcnt lgkmcnt(2)\n"
"ds_read_b128 v[80:83], v1 offset:576\n"
"v_mfma_f32_32x32x16_bf16 v[48:63], v[184:187], v[64:67], v[48:63]\n"
"ds_read_b128 v[84:87], v1 offset:608\n"
"s_add_u32 s91, s50, 67108864\n"
"s_add_u32 s92, s92, 0x80000\n"
"v_mov_b32 v127, s44\n"
"v_mfma_f32_32x32x16_bf16 v[48:63], v[188:191], v[68:71], v[48:63]\n"
"s_waitcnt lgkmcnt(3)\n"
"ds_read_b128 v[64:67], v1 offset:640\n"
"s_bfe_u32 s50, s92, 0x10015\n"
"s_xor_b32 s50, s50, s99\n"
"s_lshl_b32 s50, s50, 13\n"
"s_add_u32 s50, s50, s92\n"
"v_mfma_f32_32x32x16_bf16 v[48:63], v[192:195], v[72:75], v[48:63]\n"
"s_waitcnt lgkmcnt(3)\n"
"ds_read_b128 v[68:71], v1 offset:672\n"
"s_mov_b32 s51, s93\n"
"s_and_b32 s46, s93, 0x1000\n"
"s_lshl1_add_u32 s93, s46, s93\n"
"s_add_u32 s93, s93, 0x1000\n"
"s_mov_b32 s46, s93\n"
"ds_read_b128 v[88:91], v5 offset:2048\n"
"v_mfma_f32_32x32x16_bf16 v[48:63], v[196:199], v[76:79], v[48:63]\n"
"s_waitcnt vmcnt(4)\n"
"ds_write_b32 v12, v127\n"
"s_waitcnt lgkmcnt(4)\n"
"ds_read_b128 v[72:75], v1 offset:704\n"
"v_mfma_f32_32x32x16_bf16 v[48:63], v[200:203], v[80:83], v[48:63]\n"
"s_waitcnt lgkmcnt(4)\n"
"ds_read_b128 v[76:79], v1 offset:736\n"
"v_mfma_f32_32x32x16_bf16 v[48:63], v[204:207], v[84:87], v[48:63]\n"
"s_waitcnt lgkmcnt(4)\n"
"ds_read_b128 v[80:83], v1 offset:768\n"
"v_mfma_f32_32x32x16_bf16 v[48:63], v[208:211], v[64:67], v[48:63]\n"
"s_waitcnt lgkmcnt(4)\n"
"ds_read_b128 v[84:87], v1 offset:800\n"
"v_mfma_f32_32x32x16_bf16 v[48:63], v[212:215], v[68:71], v[48:63]\n"
"s_waitcnt lgkmcnt(3)\n"
"ds_read_b128 v[64:67], v1 offset:832\n"
"v_mfma_f32_32x32x16_bf16 v[48:63], v[216:219], v[72:75], v[48:63]\n"
"s_waitcnt lgkmcnt(3)\n"
"ds_read_b128 v[68:71], v1 offset:864\n"
"v_mfma_f32_32x32x16_bf16 v[48:63], v[220:223], v[76:79], v[48:63]\n"
"s_waitcnt lgkmcnt(3)\n"
"ds_read_b128 v[72:75], v1 offset:896\n"
"ds_read_b32 v124, v13\n"
"v_mfma_f32_32x32x16_bf16 v[48:63], v[224:227], v[80:83], v[48:63]\n"
"s_waitcnt lgkmcnt(4)\n"
"ds_read_b128 v[76:79], v1 offset:928\n"
"v_mfma_f32_32x32x16_bf16 v[48:63], v[228:231], v[84:87], v[48:63]\n"
"s_waitcnt lgkmcnt(4)\n"
"ds_read_b128 v[80:83], v1 offset:960\n"
"v_mfma_f32_32x32x16_bf16 v[48:63], v[232:235], v[64:67], v[48:63]\n"
"s_waitcnt lgkmcnt(4)\n"
"ds_read_b128 v[84:87], v1 offset:992\n"
"v_xor_b32 v1, 0x10000, v1\n"
"v_xor_b32 v7, 0x10000, v7\n"
"s_waitcnt lgkmcnt(3)\n"
"v_cmp_ne_u32 vcc, s44, v124\n"
"s_cbranch_vccnz Lqps%=\n"
"v_mfma_f32_32x32x16_bf16 v[48:63], v[236:239], v[68:71], v[48:63]\n"
"ds_read_b128 v[64:67], v1 offset:0\n"
"v_mfma_f32_32x32x16_bf16 v[48:63], v[240:243], v[72:75], v[48:63]\n"
"s_waitcnt lgkmcnt(3)\n"
"ds_read_b128 v[68:71], v1 offset:32\n"
"v_mfma_f32_32x32x16_bf16 v[48:63], v[244:247], v[76:79], v[48:63]\n"
"s_waitcnt lgkmcnt(3)\n"
"ds_read_b128 v[72:75], v1 offset:64\n"
"v_mfma_f32_32x32x16_bf16 v[48:63], v[248:251], v[80:83], v[48:63]\n"
"s_waitcnt lgkmcnt(3)\n"
"ds_read_b128 v[76:79], v1 offset:96\n"
"v_mfma_f32_32x32x16_bf16 v[48:63], v[252:255], v[84:87], v[48:63]\n"
"ds_read_b128 v[80:83], v1 offset:128\n"
"v_mfma_f32_32x32x16_bf16 v[48:63], v[88:91], v[112:115], v[48:63]\n"
"s_waitcnt lgkmcnt(4)\n"
"s_waitcnt vmcnt(0)\n"
"buffer_store_dword v127, v11, s[40:43], s51 offen sc1\n"
"s_add_u32 s44, s44, 1\n"
"s_branch Lqloop%=\n"
"Lqps%=:\n"
"v_mfma_f32_32x32x16_bf16 v[48:63], v[236:239], v[68:71], v[48:63]\n"
"v_mfma_f32_32x32x16_bf16 v[48:63], v[240:243], v[72:75], v[48:63]\n"
"s_waitcnt lgkmcnt(2)\n"
"v_mfma_f32_32x32x16_bf16 v[48:63], v[244:247], v[76:79], v[48:63]\n"
"s_waitcnt lgkmcnt(1)\n"
"v_mfma_f32_32x32x16_bf16 v[48:63], v[248:251], v[80:83], v[48:63]\n"
"s_waitcnt lgkmcnt(0)\n"
"v_mfma_f32_32x32x16_bf16 v[48:63], v[252:255], v[84:87], v[48:63]\n"
"v_mfma_f32_32x32x16_bf16 v[48:63], v[88:91], v[112:115], v[48:63]\n"
"s_waitcnt vmcnt(0)\n"
"buffer_store_dword v127, v11, s[40:43], s51 offen sc1\n"
"Lqimm%=:\n"
"s_nop 7\n"
"s_nop 1\n"
"v_mul_f32 v48, v15, v48\n"
"v_mul_f32 v52, v15, v52\n"
"v_cvt_pk_bf16_f32 v116, v48, v52\n"
"v_mul_f32 v56, v15, v56\n"
"v_mul_f32 v60, v15, v60\n"
"v_cvt_pk_bf16_f32 v117, v56, v60\n"
"v_mul_f32 v49, v15, v49\n"
"v_mul_f32 v53, v15, v53\n"
"v_cvt_pk_bf16_f32 v118, v49, v53\n"
"v_mul_f32 v57, v15, v57\n"
"v_mul_f32 v61, v15, v61\n"
"v_cvt_pk_bf16_f32 v119, v57, v61\n"
"buffer_store_dwordx4 v[116:119], v4, s[40:43], s91 offen offset:0 sc1\n"
"v_mul_f32 v50, v15, v50\n"
"v_mul_f32 v54, v15, v54\n"
"v_cvt_pk_bf16_f32 v120, v50, v54\n"
"v_mul_f32 v58, v15, v58\n"
"v_mul_f32 v62, v15, v62\n"
"v_cvt_pk_bf16_f32 v121, v58, v62\n"
"v_mul_f32 v51, v15, v51\n"
"v_mul_f32 v55, v15, v55\n"
"v_cvt_pk_bf16_f32 v122, v51, v55\n"
"v_mul_f32 v59, v15, v59\n"
"v_mul_f32 v63, v15, v63\n"
"v_cvt_pk_bf16_f32 v123, v59, v63\n"
"buffer_store_dwordx4 v[120:123], v4, s[40:43], s91 offen offset:256 sc1\n"
"s_mov_b32 s91, 0x80000000\n"
"s_waitcnt vmcnt(3)\n"
"Lqland%=:\n"
"ds_write_b32 v12, v127\n"
"ds_read_b32 v124, v13\n"
"s_mov_b32 s88, 0\n"
"s_waitcnt lgkmcnt(0)\n"
"v_cmp_ne_u32 vcc, s44, v124\n"
"s_cbranch_vccz Lqgo2%=\n"
"s_waitcnt vmcnt(0)\n"
"buffer_store_dword v127, v11, s[40:43], s46 offen offset:16 sc1\n"
"s_mov_b32 s46, 0x80000000\n"
"Lqls%=:\n"
"ds_read_b32 v124, v13\n"
"s_add_u32 s88, s88, 1\n"
"s_cmp_gt_u32 s88, 2097152\n"
"s_cbranch_scc1 Lqend%=\n"
"s_waitcnt lgkmcnt(0)\n"
"v_cmp_ne_u32 vcc, s44, v124\n"
"s_cbranch_vccnz Lqls%=\n"
"Lqgo2%=:\n"
"ds_read_b128 v[64:67], v1 offset:0\n"
"ds_read_b128 v[68:71], v1 offset:32\n"
"ds_read_b128 v[72:75], v1 offset:64\n"
"ds_read_b128 v[76:79], v1 offset:96\n"
"ds_read_b128 v[80:83], v1 offset:128\n"
"s_add_u32 s44, s44, 1\n"
"s_waitcnt lgkmcnt(4)\n"
"s_branch Lqloop%=\n"
"Lqn1%=:\n"
"v_mfma_f32_32x32x16_bf16 v[32:47], v[76:79], a[188:191], v[32:47]\n"
"buffer_load_dword v14, v10, s[40:43], s47 offen sc1\n"
"s_waitcnt lgkmcnt(4)\n"
"v_mfma_f32_32x32x16_bf16 v[16:31], a[64:67], v[80:83], v[16:31]\n"
"ds_read_b128 v[76:79], v1 offset:672\n"
"v_mfma_f32_32x32x16_bf16 v[32:47], v[80:83], a[192:195], v[32:47]\n"
"s_waitcnt lgkmcnt(4)\n"
"v_mfma_f32_32x32x16_bf16 v[16:31], a[68:71], v[84:87], v[16:31]\n"
"ds_read_b128 v[80:83], v1 offset:704\n"
"v_mfma_f32_32x32x16_bf16 v[32:47], v[84:87], a[196:199], v[32:47]\n"
"s_waitcnt lgkmcnt(4)\n"
"v_mfma_f32_32x32x16_bf16 v[16:31], a[72:75], v[64:67], v[16:31]\n"
"ds_read_b128 v[84:87], v1 offset:736\n"
"v_mfma_f32_32x32x16_bf16 v[32:47], v[64:67], a[200:203], v[32:47]\n"
"s_waitcnt lgkmcnt(4)\n"
"v_mfma_f32_32x32x16_bf16 v[16:31], a[76:79], v[68:71], v[16:31]\n"
"ds_read_b128 v[64:67], v1 offset:768\n"
"v_mfma_f32_32x32x16_bf16 v[32:47], v[68:71], a[204:207], v[32:47]\n"
"s_waitcnt lgkmcnt(4)\n"
"v_mfma_f32_32x32x16_bf16 v[16:31], a[80:83], v[72:75], v[16:31]\n"
"ds_read_b128 v[68:71], v1 offset:800\n"
"v_mfma_f32_32x32x16_bf16 v[32:47], v[72:75], a[208:211], v[32:47]\n"
"s_waitcnt lgkmcnt(4)\n"
"v_mfma_f32_32x32x16_bf16 v[16:31], a[84:87], v[76:79], v[16:31]\n"
"ds_read_b128 v[72:75], v1 offset:832\n"
"v_mfma_f32_32x32x16_bf16 v[32:47], v[76:79], a[212:215], v[32:47]\n"
"s_waitcnt lgkmcnt(4)\n"
"v_mfma_f32_32x32x16_bf16 v[16:31], a[88:91], v[80:83], v[16:31]\n"
"s_waitcnt vmcnt(1)\n"
"buffer_store_dword v127, v11, s[40:43], s46 offen offset:16 sc1\n"
"s_mov_b32 s46, 0x80000000\n"
"ds_read_b128 v[76:79], v1 offset:864\n"
"v_mfma_f32_32x32x16_bf16 v[32:47], v[80:83], a[216:219], v[32:47]\n"
"s_waitcnt lgkmcnt(4)\n"
"v_mfma_f32_32x32x16_bf16 v[16:31], a[92:95], v[84:87], v[16:31]\n"
"ds_read_b128 v[80:83], v1 offset:896\n"
"v_mfma_f32_32x32x16_bf16 v[32:47], v[84:87], a[220:223], v[32:47]\n"
"s_waitcnt lgkmcnt(4)\n"
"v_mfma_f32_32x32x16_bf16 v[16:31], a[96:99], v[64:67], v[16:31]\n"
"ds_read_b128 v[84:87], v1 offset:928\n"
"v_mfma_f32_32x32x16_bf16 v[32:47], v[64:67], a[224:227], v[32:47]\n"
"s_waitcnt lgkmcnt(4)\n"
"v_mfma_f32_32x32x16_bf16 v[16:31], a[100:103], v[68:71], v[16:31]\n"
"ds_read_b128 v[64:67], v1 offset:960\n"
"v_mfma_f32_32x32x16_bf16 v[32:47], v[68:71], a[228:231], v[32:47]\n"
"s_waitcnt lgkmcnt(4)\n"
"v_mfma_f32_32x32x16_bf16 v[16:31], a[104:107], v[72:75], v[16:31]\n"
"ds_read_b128 v[68:71], v1 offset:992\n"
"v_mfma_f32_32x32x16_bf16 v[32:47], v[72:75], a[232:235], v[32:47]\n"
"s_waitcnt lgkmcnt(4)\n"
"v_mfma_f32_32x32x16_bf16 v[16:31], a[108:111], v[76:79], v[16:31]\n"
"ds_read_b128 v[72:75], v5 offset:0\n"
"s_waitcnt vmcnt(1)\n"
"v_cmp_ge_i32 vcc, s44, v14\n"
"s_cbranch_vccz Lqd2%=\n"
"v_mfma_f32_32x32x16_bf16 v[32:47], v[76:79], a[236:239], v[32:47]\n"
"buffer_load_dword v14, v10, s[40:43], s47 offen sc1\n"
"s_waitcnt lgkmcnt(4)\n"
"v_mfma_f32_32x32x16_bf16 v[16:31], a[112:115], v[80:83], v[16:31]\n"
"ds_read_b128 v[76:79], v5 offset:1024\n"
"v_mfma_f32_32x32x16_bf16 v[32:47], v[80:83], a[240:243], v[32:47]\n"
"s_waitcnt lgkmcnt(4)\n"
"v_mfma_f32_32x32x16_bf16 v[16:31], a[116:119], v[84:87], v[16:31]\n"
"ds_read_b128 v[80:83], v1 offset:0\n"
"v_mfma_f32_32x32x16_bf16 v[32:47], v[84:87], a[244:247], v[32:47]\n"
"s_waitcnt lgkmcnt(4)\n"
"v_mfma_f32_32x32x16_bf16 v[16:31], a[120:123], v[64:67], v[16:31]\n"
"ds_read_b128 v[84:87], v1 offset:32\n"
"v_mfma_f32_32x32x16_bf16 v[32:47], v[64:67], a[248:251], v[32:47]\n"
"s_waitcnt lgkmcnt(4)\n"
"v_mfma_f32_32x32x16_bf16 v[16:31], a[124:127], v[68:71], v[16:31]\n"
"ds_read_b128 v[64:67], v1 offset:64\n"
"v_mfma_f32_32x32x16_bf16 v[32:47], v[68:71], a[252:255], v[32:47]\n"
"s_waitcnt lgkmcnt(4)\n"
"v_mfma_f32_32x32x16_bf16 v[16:31], v[72:75], v[112:115], v[16:31]\n"
"s_waitcnt lgkmcnt(3)\n"
"ds_read_b128 v[68:71], v1 offset:96\n"
"v_mfma_f32_32x32x16_bf16 v[32:47], v[112:115], v[76:79], v[32:47]\n"
"s_waitcnt lgkmcnt(2)\n"
"ds_read_b128 v[72:75], v1 offset:128\n"
"s_add_u32 s90, s50, 33554432\n"
"v_mfma_f32_32x32x16_bf16 v[48:63], v[128:131], v[80:83], 0\n"
"ds_read_b128 v[76:79], v1 offset:160\n"
"v_mul_f32 v16, v15, v16\n"
"v_mul_f32 v20, v15, v20\n"
"v_cvt_pk_bf16_f32 v116, v16, v20\n"
"v_mul_f32 v24, v15, v24\n"
"v_mul_f32 v28, v15, v28\n"
"v_mfma_f32_32x32x16_bf16 v[48:63], v[132:135], v[84:87], v[48:63]\n"
"s_waitcnt lgkmcnt(2)\n"
"ds_read_b128 v[80:83], v1 offset:192\n"
"v_cvt_pk_bf16_f32 v117, v24, v28\n"
"v_mul_f32 v17, v15, v17\n"
"v_mul_f32 v21, v15, v21\n"
"v_cvt_pk_bf16_f32 v118, v17, v21\n"
"v_mfma_f32_32x32x16_bf16 v[48:63], v[136:139], v[64:67], v[48:63]\n"
"ds_read_b128 v[84:87], v1 offset:224\n"
"v_mul_f32 v25, v15, v25\n"
"v_mul_f32 v29, v15, v29\n"
"v_cvt_pk_bf16_f32 v119, v25, v29\n"
"v_mul_f32 v18, v15, v18\n"
"v_mul_f32 v22, v15, v22\n"
"v_mfma_f32_32x32x16_bf16 v[48:63], v[140:143], v[68:71], v[48:63]\n"
"s_waitcnt lgkmcnt(2)\n"
"ds_read_b128 v[64:67], v1 offset:256\n"
"buffer_store_dwordx4 v[116:119], v2, s[40:43], s50 offen offset:0 sc1\n"
"v_cvt_pk_bf16_f32 v120, v18, v22\n"
"v_mul_f32 v26, v15, v26\n"
"v_mul_f32 v30, v15, v30\n"
"v_mfma_f32_32x32x16_bf16 v[48:63], v[144:147], v[72:75], v[48:63]\n"
"ds_read_b128 v[68:71], v1 offset:288\n"
"v_cvt_pk_bf16_f32 v121, v26, v30\n"
"v_mul_f32 v19, v15, v19\n"
"v_mul_f32 v23, v15, v23\n"
"v_cvt_pk_bf16_f32 v122, v19, v23\n"
"v_mul_f32 v27, v15, v27\n"
"v_mfma_f32_32x32x16_bf16 v[48:63], v[148:151], v[76:79], v[48:63]\n"
"s_waitcnt lgkmcnt(2)\n"
"ds_read_b128 v[72:75], v1 offset:320\n"
"v_mul_f32 v31, v15, v31\n"
"v_cvt_pk_bf16_f32 v123, v27, v31\n"
"v_mul_f32 v32, v96, v32\n"
"v_mul_f32 v33, v97, v33\n"
"v_mfma_f32_32x32x16_bf16 v[48:63], v[152:155], v[80:83], v[48:63]\n"
"ds_read_b128 v[76:79], v1 offset:352\n"
"buffer_store_dwordx4 v[120:123], v2, s[40:43], s50 offen offset:256 sc1\n"
"v_cvt_pk_bf16_f32 v16, v32, v33\n"
"v_mul_f32 v34, v98, v34\n"
"v_mul_f32 v35, v99, v35\n"
"v_cvt_pk_bf16_f32 v17, v34, v35\n"
"v_mfma_f32_32x32x16_bf16 v[48:63], v[156:159], v[84:87], v[48:63]\n"
"s_waitcnt lgkmcnt(2)\n"
"ds_read_b128 v[80:83], v1 offset:384\n"
"v_mul_f32 v36, v100, v36\n"
"v_mul_f32 v37, v101, v37\n"
"v_cvt_pk_bf16_f32 v18, v36, v37\n"
"v_mul_f32 v38, v102, v38\n"
"v_mfma_f32_32x32x16_bf16 v[48:63], v[160:163], v[64:67], v[48:63]\n"
"ds_read_b128 v[84:87], v1 offset:416\n"
"v_mul_f32 v39, v103, v39\n"
"v_cvt_pk_bf16_f32 v19, v38, v39\n"
"v_mul_f32 v40, v104, v40\n"
"v_mul_f32 v41, v105, v41\n"
"v_cvt_pk_bf16_f32 v20, v40, v41\n"
"v_mfma_f32_32x32x16_bf16 v[48:63], v[164:167], v[68:71], v[48:63]\n"
"s_waitcnt lgkmcnt(2)\n"
"ds_read_b128 v[64:67], v1 offset:448\n"
"buffer_store_dwordx4 v[16:19], v3, s[40:43], s90 offen offset:0 sc1\n"
"v_mul_f32 v42, v106, v42\n"
"v_mul_f32 v43, v107, v43\n"
"v_cvt_pk_bf16_f32 v21, v42, v43\n"
"v_mfma_f32_32x32x16_bf16 v[48:63], v[168:171], v[72:75], v[48:63]\n"
"ds_read_b128 v[68:71], v1 offset:480\n"
"v_mul_f32 v44, v108, v44\n"
"v_mul_f32 v45, v109, v45\n"
"v_cvt_pk_bf16_f32 v22, v44, v45\n"
"v_mul_f32 v46, v110, v46\n"
"v_mul_f32 v47, v111, v47\n"
"v_mfma_f32_32x32x16_bf16 v[48:63], v[172:175], v[76:79], v[48:63]\n"
"s_waitcnt lgkmcnt(2)\n"
"ds_read_b128 v[72:75], v1 offset:512\n"
"v_cvt_pk_bf16_f32 v23, v46, v47\n"
"v_mfma_f32_32x32x16_bf16 v[48:63], v[176:179], v[80:83], v[48:63]\n"
"ds_read_b128 v[76:79], v1 offset:544\n"
"buffer_store_dwordx4 v[20:23], v3, s[40:43], s90 offen offset:256 sc1\n"
"s_waitcnt vmcnt(4)\n"
"v_cmp_ge_i32 vcc, s44, v14\n"
"s_cbranch_vccz Lqd3%=\n"
"v_mfma_f32_32x32x16_bf16 v[48:63], v[180:183], v[84:87], v[48:63]\n"
"buffer_load_dword v14, v10, s[40:43], s47 offen sc1\n"
"s_waitcnt lgkmcnt(2)\n"
"ds_read_b128 v[80:83], v1 offset:576\n"
"v_mfma_f32_32x32x16_bf16 v[48:63], v[184:187], v[64:67], v[48:63]\n"
"ds_read_b128 v[84:87], v1 offset:608\n"
"s_add_u32 s91, s50, 67108864\n"
"s_add_u32 s92, s92, 0x80000\n"
"v_mov_b32 v127, s44\n"
"v_mfma_f32_32x32x16_bf16 v[48:63], v[188:191], v[68:71], v[48:63]\n"
"s_waitcnt lgkmcnt(3)\n"
"ds_read_b128 v[64:67], v1 offset:640\n"
"s_bfe_u32 s50, s92, 0x10015\n"
"s_xor_b32 s50, s50, s99\n"
"s_lshl_b32 s50, s50, 13\n"
"s_add_u32 s50, s50, s92\n"
"v_mfma_f32_32x32x16_bf16 v[48:63], v[192:195], v[72:75], v[48:63]\n"
"s_waitcnt lgkmcnt(3)\n"
"ds_read_b128 v[68:71], v1 offset:672\n"
"s_mov_b32 s51, s93\n"
"s_and_b32 s46, s93, 0x1000\n"
"s_lshl1_add_u32 s93, s46, s93\n"
"s_add_u32 s93, s93, 0x1000\n"
"s_mov_b32 s46, s93\n"
"ds_read_b128 v[88:91], v5 offset:2048\n"
"v_mfma_f32_32x32x16_bf16 v[48:63], v[196:199], v[76:79], v[48:63]\n"
"s_waitcnt lgkmcnt(4)\n"
"ds_read_b128 v[72:75], v1 offset:704\n"
"v_mfma_f32_32x32x16_bf16 v[48:63], v[200:203], v[80:83], v[48:63]\n"
"s_waitcnt lgkmcnt(4)\n"
"ds_read_b128 v[76:79], v1 offset:736\n"
"v_mfma_f32_32x32x16_bf16 v[48:63], v[204:207], v[84:87], v[48:63]\n"
"s_waitcnt lgkmcnt(4)\n"
"ds_read_b128 v[80:83], v1 offset:768\n"
"v_mfma_f32_32x32x16_bf16 v[48:63], v[208:211], v[64:67], v[48:63]\n"
"s_waitcnt lgkmcnt(4)\n"
"ds_read_b128 v[84:87], v1 offset:800\n"
"v_mfma_f32_32x32x16_bf16 v[48:63], v[212:215], v[68:71], v[48:63]\n"
"s_waitcnt lgkmcnt(3)\n"
"ds_read_b128 v[64:67], v1 offset:832\n"
"v_mfma_f32_32x32x16_bf16 v[48:63], v[216:219], v[72:75], v[48:63]\n"
"s_waitcnt lgkmcnt(3)\n"
"ds_read_b128 v[68:71], v1 offset:864\n"
"v_mfma_f32_32x32x16_bf16 v[48:63], v[220:223], v[76:79], v[48:63]\n"
"s_waitcnt lgkmcnt(3)\n"
"ds_read_b128 v[72:75], v1 offset:896\n"
"ds_read_b32 v124, v13\n"
"v_mfma_f32_32x32x16_bf16 v[48:63], v[224:227], v[80:83], v[48:63]\n"
"s_waitcnt lgkmcnt(4)\n"
"ds_read_b128 v[76:79], v1 offset:928\n"
"v_mfma_f32_32x32x16_bf16 v[48:63], v[228:231], v[84:87], v[48:63]\n"
"s_waitcnt lgkmcnt(4)\n"
"ds_read_b128 v[80:83], v1 offset:960\n"
"v_mfma_f32_32x32x16_bf16 v[48:63], v[232:235], v[64:67], v[48:63]\n"
"s_waitcnt lgkmcnt(4)\n"
"ds_read_b128 v[84:87], v1 offset:992\n"
"v_xor_b32 v1, 0x10000, v1\n"
"v_xor_b32 v7, 0x10000, v7\n"
"s_waitcnt lgkmcnt(3)\n"
"v_mfma_f32_32x32x16_bf16 v[48:63], v[236:239], v[68:71], v[48:63]\n"
"v_mfma_f32_32x32x16_bf16 v[48:63], v[240:243], v[72:75], v[48:63]\n"
"s_waitcnt lgkmcnt(2)\n"
"v_mfma_f32_32x32x16_bf16 v[48:63], v[244:247], v[76:79], v[48:63]\n"
"s_waitcnt lgkmcnt(1)\n"
"v_mfma_f32_32x32x16_bf16 v[48:63], v[248:251], v[80:83], v[48:63]\n"
"s_waitcnt lgkmcnt(0)\n"
"v_mfma_f32_32x32x16_bf16 v[48:63], v[252:255], v[84:87], v[48:63]\n"
"v_mfma_f32_32x32x16_bf16 v[48:63], v[88:91], v[112:115], v[48:63]\n"
"s_waitcnt vmcnt(1)\n"
"buffer_store_dword v127, v11, s[40:43], s51 offen sc1\n"
"s_waitcnt vmcnt(1)\n"
"v_cmp_ge_i32 vcc, s44, v14\n"
"s_cbranch_vccnz Lqn4%=\n"
"s_add_u32 m0, s49, 0\n"
"s_add_u32 s89, s48, 0\n"
"buffer_load_dwordx4 v0, s[56:59], s89 offen sc1 lds\n"
"s_add_u32 m0, s49, 1040\n"
"s_add_u32 s89, s48, 1024\n"
"buffer_load_dwordx4 v0, s[56:59], s89 offen sc1 lds\n"
"s_add_u32 m0, s49, 2080\n"
"s_add_u32 s89, s48, 2048\n"
"buffer_load_dwordx4 v0, s[56:59], s89 offen sc1 lds\n"
"s_add_u32 m0, s49, 3120\n"
"s_add_u32 s89, s48, 3072\n"
"buffer_load_dwordx4 v0, s[56:59], s89 offen sc1 lds\n"
"s_add_u32 m0, s49, 4160\n"
"s_add_u32 s89, s68, 0\n"
"buffer_load_dwordx4 v0, s[56:59], s89 offen sc1 lds\n"
"s_add_u32 m0, s49, 5200\n"
"s_add_u32 s89, s68, 1024\n"
"buffer_load_dwordx4 v0, s[56:59], s89 offen sc1 lds\n"
"s_add_u32 m0, s49, 6240\n"
"s_add_u32 s89, s68, 2048\n"
"buffer_load_dwordx4 v0, s[56:59], s89 offen sc1 lds\n"
"s_add_u32 m0, s49, 7280\n"
"s_add_u32 s89, s68, 3072\n"
"buffer_load_dwordx4 v0, s[56:59], s89 offen sc1 lds\n"
"s_mov_b32 m0, s54\n"
"s_xor_b32 s54, s54, 0x10000\n"
"buffer_load_dwordx4 v6, s[40:43], s53 offen sc1 lds\n"
"s_add_u32 s62, s62, s64\n"
"s_add_u32 s63, s63, s65\n"
"s_xor_b32 s49, s49, 0x10000\n"
"s_lshr_b32 s89, s62, 8\n"
"s_xor_b32 s89, s89, s67\n"
"s_and_b32 s89, s89, s55\n"
"s_add_u32 s48, s62, s89\n"
"s_add_u32 s53, s63, s89\n"
"s_add_u32 s68, s48, s66\n"
"v_mul_f32 v48, v15, v48\n"
"v_mul_f32 v52, v15, v52\n"
"v_cvt_pk_bf16_f32 v116, v48, v52\n"
"v_mul_f32 v56, v15, v56\n"
"v_mul_f32 v60, v15, v60\n"
"v_cvt_pk_bf16_f32 v117, v56, v60\n"
"v_mul_f32 v49, v15, v49\n"
"v_mul_f32 v53, v15, v53\n"
"v_cvt_pk_bf16_f32 v118, v49, v53\n"
"v_mul_f32 v57, v15, v57\n"
"v_mul_f32 v61, v15, v61\n"
"v_cvt_pk_bf16_f32 v119, v57, v61\n"
"buffer_store_dwordx4 v[116:119], v4, s[40:43], s91 offen offset:0 sc1\n"
"v_mul_f32 v50, v15, v50\n"
"v_mul_f32 v54, v15, v54\n"
"v_cvt_pk_bf16_f32 v120, v50, v54\n"
"v_mul_f32 v58, v15, v58\n"
"v_mul_f32 v62, v15, v62\n"
"v_cvt_pk_bf16_f32 v121, v58, v62\n"
"v_mul_f32 v51, v15, v51\n"
"v_mul_f32 v55, v15, v55\n"
"v_cvt_pk_bf16_f32 v122, v51, v55\n"
"v_mul_f32 v59, v15, v59\n"
"v_mul_f32 v63, v15, v63\n"
"v_cvt_pk_bf16_f32 v123, v59, v63\n"
"buffer_store_dwordx4 v[120:123], v4, s[40:43], s91 offen offset:256 sc1\n"
"s_mov_b32 s91, 0x80000000\n"
"s_waitcnt vmcnt(0)\n"
"buffer_store_dword v127, v11, s[40:43], s46 offen offset:16 sc1\n"
"s_mov_b32 s46, 0x80000000\n"
"s_branch Lqland%=\n"
"Lqn4%=:\n"
"s_nop 6\n"
"v_mul_f32 v48, v15, v48\n"
"v_mul_f32 v52, v15, v52\n"
"v_cvt_pk_bf16_f32 v116, v48, v52\n"
"v_mul_f32 v56, v15, v56\n"
"v_mul_f32 v60, v15, v60\n"
"v_cvt_pk_bf16_f32 v117, v56, v60\n"
"v_mul_f32 v49, v15, v49\n"
"v_mul_f32 v53, v15, v53\n"
"v_cvt_pk_bf16_f32 v118, v49, v53\n"
"v_mul_f32 v57, v15, v57\n"
"v_mul_f32 v61, v15, v61\n"
"v_cvt_pk_bf16_f32 v119, v57, v61\n"
"buffer_store_dwordx4 v[116:119], v4, s[40:43], s91 offen offset:0 sc1\n"
"v_mul_f32 v50, v15, v50\n"
"v_mul_f32 v54, v15, v54\n"
"v_cvt_pk_bf16_f32 v120, v50, v54\n"
"v_mul_f32 v58, v15, v58\n"
"v_mul_f32 v62, v15, v62\n"
"v_cvt_pk_bf16_f32 v121, v58, v62\n"
"v_mul_f32 v51, v15, v51\n"
"v_mul_f32 v55, v15, v55\n"
"v_cvt_pk_bf16_f32 v122, v51, v55\n"
"v_mul_f32 v59, v15, v59\n"
"v_mul_f32 v63, v15, v63\n"
"v_cvt_pk_bf16_f32 v123, v59, v63\n"
"buffer_store_dwordx4 v[120:123], v4, s[40:43], s91 offen offset:256 sc1\n"
"s_mov_b32 s91, 0x80000000\n"
"buffer_load_dword v14, v10, s[40:43], s47 offen sc1\n"
"s_waitcnt vmcnt(1)\n"
"buffer_store_dword v127, v11, s[40:43], s46 offen offset:16 sc1\n"
"s_mov_b32 s46, 0x80000000\n"
"s_cmp_eq_u32 s44, 63\n"
"s_cbranch_scc1 Lqend%=\n"
"s_waitcnt vmcnt(1)\n"
"v_cmp_ge_i32 vcc, s44, v14\n"
"s_cbranch_vccz Lqsr%=\n"
"Lqnext%=:\n"
"s_mov_b32 s88, 0\n"
"Lqsp%=:\n"
"buffer_load_dword v14, v10, s[40:43], s47 offen sc1\n"
"s_add_u32 s88, s88, 1\n"
"s_cmp_gt_u32 s88, 131072\n"
"s_cbranch_scc1 Lqend%=\n"
"s_waitcnt vmcnt(0)\n"
"v_cmp_ge_i32 vcc, s44, v14\n"
"s_cbranch_vccz Lqsr%=\n"
"s_sleep 1\n"
"s_branch Lqsp%=\n"
"Lqsr%=:\n"
"s_add_u32 m0, s49, 0\n"
"s_add_u32 s89, s48, 0\n"
"buffer_load_dwordx4 v0, s[56:59], s89 offen sc1 lds\n"
"s_add_u32 m0, s49, 1040\n"
"s_add_u32 s89, s48, 1024\n"
"buffer_load_dwordx4 v0, s[56:59], s89 offen sc1 lds\n"
"s_add_u32 m0, s49, 2080\n"
"s_add_u32 s89, s48, 2048\n"
"buffer_load_dwordx4 v0, s[56:59], s89 offen sc1 lds\n"
"s_add_u32 m0, s49, 3120\n"
"s_add_u32 s89, s48, 3072\n"
"buffer_load_dwordx4 v0, s[56:59], s89 offen sc1 lds\n"
"s_add_u32 m0, s49, 4160\n"
"s_add_u32 s89, s68, 0\n"
"buffer_load_dwordx4 v0, s[56:59], s89 offen sc1 lds\n"
"s_add_u32 m0, s49, 5200\n"
"s_add_u32 s89, s68, 1024\n"
"buffer_load_dwordx4 v0, s[56:59], s89 offen sc1 lds\n"
"s_add_u32 m0, s49, 6240\n"
"s_add_u32 s89, s68, 2048\n"
"buffer_load_dwordx4 v0, s[56:59], s89 offen sc1 lds\n"
"s_add_u32 m0, s49, 7280\n"
"s_add_u32 s89, s68, 3072\n"
"buffer_load_dwordx4 v0, s[56:59], s89 offen sc1 lds\n"
"s_mov_b32 m0, s54\n"
"s_xor_b32 s54, s54, 0x10000\n"
"buffer_load_dwordx4 v6, s[40:43], s53 offen sc1 lds\n"
"s_add_u32 s62, s62, s64\n"
"s_add_u32 s63, s63, s65\n"
"s_xor_b32 s49, s49, 0x10000\n"
"s_lshr_b32 s89, s62, 8\n"
"s_xor_b32 s89, s89, s67\n"
"s_and_b32 s89, s89, s55\n"
"s_add_u32 s48, s62, s89\n"
"s_add_u32 s53, s63, s89\n"
"s_add_u32 s68, s48, s66\n"
"s_waitcnt vmcnt(0)\n"
"s_branch Lqland%=\n"
"Lqd2%=:\n"
"v_mfma_f32_32x32x16_bf16 v[32:47], v[76:79], a[236:239], v[32:47]\n"
"s_add_u32 m0, s49, 0\n"
"s_add_u32 s89, s48, 0\n"
"buffer_load_dwordx4 v0, s[56:59], s89 offen sc1 lds\n"
"s_waitcnt lgkmcnt(4)\n"
"v_mfma_f32_32x32x16_bf16 v[16:31], a[112:115], v[80:83], v[16:31]\n"
"s_add_u32 m0, s49, 1040\n"
"s_add_u32 s89, s48, 1024\n"
"buffer_load_dwordx4 v0, s[56:59], s89 offen sc1 lds\n"
"ds_read_b128 v[76:79], v5 offset:1024\n"
"v_mfma_f32_32x32x16_bf16 v[32:47], v[80:83], a[240:243], v[32:47]\n"
"s_add_u32 m0, s49, 2080\n"
"s_add_u32 s89, s48, 2048\n"
"buffer_load_dwordx4 v0, s[56:59], s89 offen sc1 lds\n"
"s_waitcnt lgkmcnt(4)\n"
"v_mfma_f32_32x32x16_bf16 v[16:31], a[116:119], v[84:87], v[16:31]\n"
"s_add_u32 m0, s49, 3120\n"
"s_add_u32 s89, s48, 3072\n"
"buffer_load_dwordx4 v0, s[56:59], s89 offen sc1 lds\n"
"ds_read_b128 v[80:83], v1 offset:0\n"
"v_mfma_f32_32x32x16_bf16 v[32:47], v[84:87], a[244:247], v[32:47]\n"
"s_add_u32 m0, s49, 4160\n"
"s_add_u32 s89, s68, 0\n"
"buffer_load_dwordx4 v0, s[56:59], s89 offen sc1 lds\n"
"s_waitcnt lgkmcnt(4)\n"
"v_mfma_f32_32x32x16_bf16 v[16:31], a[120:123], v[64:67], v[16:31]\n"
"s_add_u32 m0, s49, 5200\n"
"s_add_u32 s89, s68, 1024\n"
"buffer_load_dwordx4 v0, s[56:59], s89 offen sc1 lds\n"
"ds_read_b128 v[84:87], v1 offset:32\n"
"v_mfma_f32_32x32x16_bf16 v[32:47], v[64:67], a[248:251], v[32:47]\n"
"s_add_u32 m0, s49, 6240\n"
"s_add_u32 s89, s68, 2048\n"
"buffer_load_dwordx4 v0, s[56:59], s89 offen sc1 lds\n"
"s_waitcnt lgkmcnt(4)\n"
"v_mfma_f32_32x32x16_bf16 v[16:31], a[124:127], v[68:71], v[16:31]\n"
"s_add_u32 m0, s49, 7280\n"
"s_add_u32 s89, s68, 3072\n"
"buffer_load_dwordx4 v0, s[56:59], s89 offen sc1 lds\n"
"ds_read_b128 v[64:67], v1 offset:64\n"
"v_mfma_f32_32x32x16_bf16 v[32:47], v[68:71], a[252:255], v[32:47]\n"
"s_mov_b32 m0, s54\n"
"s_xor_b32 s54, s54, 0x10000\n"
"buffer_load_dwordx4 v6, s[40:43], s53 offen sc1 lds\n"
"s_waitcnt lgkmcnt(4)\n"
"v_mfma_f32_32x32x16_bf16 v[16:31], v[72:75], v[112:115], v[16:31]\n"
"s_add_u32 s62, s62, s64\n"
"s_add_u32 s63, s63, s65\n"
"s_xor_b32 s49, s49, 0x10000\n"
"s_lshr_b32 s89, s62, 8\n"
"s_waitcnt lgkmcnt(3)\n"
"ds_read_b128 v[68:71], v1 offset:96\n"
"v_mfma_f32_32x32x16_bf16 v[32:47], v[112:115], v[76:79], v[32:47]\n"
"s_xor_b32 s89, s89, s67\n"
"s_and_b32 s89, s89, s55\n"
"s_add_u32 s48, s62, s89\n"
"s_add_u32 s53, s63, s89\n"
"s_add_u32 s68, s48, s66\n"
"s_waitcnt lgkmcnt(2)\n"
"ds_read_b128 v[72:75], v1 offset:128\n"
"s_add_u32 s90, s50, 33554432\n"
"s_branch Lqp2%=\n"
"Lqd3%=:\n"
"v_mfma_f32_32x32x16_bf16 v[48:63], v[180:183], v[84:87], v[48:63]\n"
"s_add_u32 m0, s49, 0\n"
"s_add_u32 s89, s48, 0\n"
"buffer_load_dwordx4 v0, s[56:59], s89 offen sc1 lds\n"
"s_waitcnt lgkmcnt(2)\n"
"ds_read_b128 v[80:83], v1 offset:576\n"
"v_mfma_f32_32x32x16_bf16 v[48:63], v[184:187], v[64:67], v[48:63]\n"
"s_add_u32 m0, s49, 1040\n"
"s_add_u32 s89, s48, 1024\n"
"buffer_load_dwordx4 v0, s[56:59], s89 offen sc1 lds\n"
"ds_read_b128 v[84:87], v1 offset:608\n"
"s_add_u32 s91, s50, 67108864\n"
"s_add_u32 s92, s92, 0x80000\n"
"v_mov_b32 v127, s44\n"
"v_mfma_f32_32x32x16_bf16 v[48:63], v[188:191], v[68:71], v[48:63]\n"
"s_add_u32 m0, s49, 2080\n"
"s_add_u32 s89, s48, 2048\n"
"buffer_load_dwordx4 v0, s[56:59], s89 offen sc1 lds\n"
"s_waitcnt lgkmcnt(3)\n"
"ds_read_b128 v[64:67], v1 offset:640\n"
"s_bfe_u32 s50, s92, 0x10015\n"
"s_xor_b32 s50, s50, s99\n"
"s_lshl_b32 s50, s50, 13\n"
"s_add_u32 s50, s50, s92\n"
"v_mfma_f32_32x32x16_bf16 v[48:63], v[192:195], v[72:75], v[48:63]\n"
"s_add_u32 m0, s49, 3120\n"
"s_add_u32 s89, s48, 3072\n"
"buffer_load_dwordx4 v0, s[56:59], s89 offen sc1 lds\n"
"s_waitcnt lgkmcnt(3)\n"
"ds_read_b128 v[68:71], v1 offset:672\n"
"s_mov_b32 s51, s93\n"
"s_and_b32 s46, s93, 0x1000\n"
"s_lshl1_add_u32 s93, s46, s93\n"
"s_add_u32 s93, s93, 0x1000\n"
"s_mov_b32 s46, s93\n"
"ds_read_b128 v[88:91], v5 offset:2048\n"
"v_mfma_f32_32x32x16_bf16 v[48:63], v[196:199], v[76:79], v[48:63]\n"
"s_add_u32 m0, s49, 4160\n"
"s_add_u32 s89, s68, 0\n"
"buffer_load_dwordx4 v0, s[56:59], s89 offen sc1 lds\n"
"s_waitcnt lgkmcnt(4)\n"
"ds_read_b128 v[72:75], v1 offset:704\n"
"v_mfma_f32_32x32x16_bf16 v[48:63], v[200:203], v[80:83], v[48:63]\n"
"s_add_u32 m0, s49, 5200\n"
"s_add_u32 s89, s68, 1024\n"
"buffer_load_dwordx4 v0, s[56:59], s89 offen sc1 lds\n"
"s_waitcnt lgkmcnt(4)\n"
"ds_read_b128 v[76:79], v1 offset:736\n"
"v_mfma_f32_32x32x16_bf16 v[48:63], v[204:207], v[84:87], v[48:63]\n"
"s_add_u32 m0, s49, 6240\n"
"s_add_u32 s89, s68, 2048\n"
"buffer_load_dwordx4 v0, s[56:59], s89 offen sc1 lds\n"
"s_waitcnt lgkmcnt(4)\n"
"ds_read_b128 v[80:83], v1 offset:768\n"
"v_mfma_f32_32x32x16_bf16 v[48:63], v[208:211], v[64:67], v[48:63]\n"
"s_add_u32 m0, s49, 7280\n"
"s_add_u32 s89, s68, 3072\n"
"buffer_load_dwordx4 v0, s[56:59], s89 offen sc1 lds\n"
"s_waitcnt lgkmcnt(4)\n"
"ds_read_b128 v[84:87], v1 offset:800\n"
"v_mfma_f32_32x32x16_bf16 v[48:63], v[212:215], v[68:71], v[48:63]\n"
"s_mov_b32 m0, s54\n"
"s_xor_b32 s54, s54, 0x10000\n"
"buffer_load_dwordx4 v6, s[40:43], s53 offen sc1 lds\n"
"s_waitcnt lgkmcnt(3)\n"
"ds_read_b128 v[64:67], v1 offset:832\n"
"v_mfma_f32_32x32x16_bf16 v[48:63], v[216:219], v[72:75], v[48:63]\n"
"s_add_u32 s62, s62, s64\n"
"s_add_u32 s63, s63, s65\n"
"s_xor_b32 s49, s49, 0x10000\n"
"s_lshr_b32 s89, s62, 8\n"
"s_waitcnt lgkmcnt(3)\n"
"ds_read_b128 v[68:71], v1 offset:864\n"
"v_mfma_f32_32x32x16_bf16 v[48:63], v[220:223], v[76:79], v[48:63]\n"
"s_xor_b32 s89, s89, s67\n"
"s_and_b32 s89, s89, s55\n"
"s_add_u32 s48, s62, s89\n"
"s_add_u32 s53, s63, s89\n"
"s_add_u32 s68, s48, s66\n"
"s_waitcnt lgkmcnt(3)\n"
"ds_read_b128 v[72:75], v1 offset:896\n"
"ds_read_b32 v124, v13\n"
"v_mfma_f32_32x32x16_bf16 v[48:63], v[224:227], v[80:83], v[48:63]\n"
"s_waitcnt lgkmcnt(4)\n"
"ds_read_b128 v[76:79], v1 offset:928\n"
"v_mfma_f32_32x32x16_bf16 v[48:63], v[228:231], v[84:87], v[48:63]\n"
"s_waitcnt lgkmcnt(4)\n"
"ds_read_b128 v[80:83], v1 offset:960\n"
"v_mfma_f32_32x32x16_bf16 v[48:63], v[232:235], v[64:67], v[48:63]\n"
"s_waitcnt lgkmcnt(4)\n"
"ds_read_b128 v[84:87], v1 offset:992\n"
"v_xor_b32 v1, 0x10000, v1\n"
"v_xor_b32 v7, 0x10000, v7\n"
"s_waitcnt lgkmcnt(3)\n"
"v_mfma_f32_32x32x16_bf16 v[48:63], v[236:239], v[68:71], v[48:63]\n"
"v_mfma_f32_32x32x16_bf16 v[48:63], v[240:243], v[72:75], v[48:63]\n"
"s_waitcnt lgkmcnt(2)\n"
"v_mfma_f32_32x32x16_bf16 v[48:63], v[244:247], v[76:79], v[48:63]\n"
"s_waitcnt lgkmcnt(1)\n"
"v_mfma_f32_32x32x16_bf16 v[48:63], v[248:251], v[80:83], v[48:63]\n"
"s_waitcnt lgkmcnt(0)\n"
"v_mfma_f32_32x32x16_bf16 v[48:63], v[252:255], v[84:87], v[48:63]\n"
"v_mfma_f32_32x32x16_bf16 v[48:63], v[88:91], v[112:115], v[48:63]\n"
"s_waitcnt vmcnt(9)\n"
"buffer_store_dword v127, v11, s[40:43], s51 offen sc1\n"
"s_branch Lqimm%=\n"
"Lqend%=:\n"
"s_waitcnt vmcnt(0) lgkmcnt(0)\n"
"LKQKV:\n"
    OPS);
  } else {
    if(role==1){
      VC[0]=lane*16; VC[1]=(lane&7)*2*FS; SA[12]=OFF_Q+hf*4096+w*131072; SA[13]=w*32768; SA[14]=OFF_FLAG+F_O+(p*2+hf)*16384+((SDO>>hf)&1)*8192+w*8*2*FS;
      if(p) setrs(SB,0,ws,WS_USED); else setrs(SB,0,x,2048*512*2);
      setrs(SB,4,ws,WS_USED); setrs(SC,0,Wo,512*4096*2);
      { const int rq=2*hf+(j>>3), nb=j&7, tokA=2*(c>>2)+(c&1), hfA=(c>>1)&1, rw=16*(g&1)+8*(g>>1)+4*(q4>>1)+(q4&1), tk=2*w+hh;
        VC[2]=(1024*w+(lane>>3))*1024+nb*128+(((lane&7)^(hh<<1))*16); VC[3]=w*32768+rw*128+(((q4>>1)|((g&1)<<1))<<5)+8*p4;
        VC[4]=131072+w*2304+(LL3? 4*(g&1)+2*(g>>1) : 2*g)*288+c*4; VC[5]=131072+tk*288+i32*8; VC[6]=tk*1024+nb*128+i32*4; VC[7]=(i32==31)? (tk&3)*8 : 0x40000000;
        VC[8]=(g>>1)*1024+((2*(g&1)+(LL3? c>>3 : hfA))*16+8*(rq&1)+(LL3? c&7 : tokA))*16; VC[9]=(c&(LL3? 8:2))?0xEEEE:0x4444; VC[10]=w*32768+lane*16; VC[11]=((j*4+w)*64+lane)*1024+c3(Wo);
        SA[15]=OFF_FLAG+F_X1+(p*2+hf)*16384+((SD1>>hf)&1)*8192+((rq&1)*32+nb*4+w)*FS; SC[4]=pv+OFF_V+8*16384+(2*rq+(w>>1))*256+nb*32;
        SA[5]=0x2000; SB[8]=p? 0x80000 : 0x8000; SB[9]=p? 0x2000 : 0; SB[10]=p? OFF_V+(4+rq)*16384 : rq*8192; SB[11]=pv+OFF_V+rq*16384; }
      asm volatile(
"s_waitcnt lgkmcnt(0)\n"
"s_nop 4\n"
"s_lshl_b32 s93, s38, 17\n"
"s_lshl_b32 s94, s38, 14\n"
"v_xor_b32 v12, 64, v2\n"
"buffer_load_dword v11, v11, s[72:75], 0 offen\n"
"s_add_u32 m0, s53, 0\n"
"s_xor_b32 s89, s93, 0x0\n"
"buffer_load_dwordx4 v2, s[72:75], s89 offen lds\n"
"s_add_u32 m0, s53, 1024\n"
"s_xor_b32 s89, s93, 0x2000\n"
"buffer_load_dwordx4 v2, s[72:75], s89 offen lds\n"
"s_add_u32 m0, s53, 2048\n"
"s_xor_b32 s89, s93, 0x4000\n"
"buffer_load_dwordx4 v12, s[72:75], s89 offen lds\n"
"s_add_u32 m0, s53, 3072\n"
"s_xor_b32 s89, s93, 0x6000\n"
"buffer_load_dwordx4 v12, s[72:75], s89 offen lds\n"
"s_add_u32 m0, s53, 4096\n"
"s_xor_b32 s89, s93, 0x8000\n"
"buffer_load_dwordx4 v2, s[72:75], s89 offen lds\n"
"s_add_u32 m0, s53, 5120\n"
"s_xor_b32 s89, s93, 0xa000\n"
"buffer_load_dwordx4 v2, s[72:75], s89 offen lds\n"
"s_add_u32 m0, s53, 6144\n"
"s_xor_b32 s89, s93, 0xc000\n"
"buffer_load_dwordx4 v12, s[72:75], s89 offen lds\n"
"s_add_u32 m0, s53, 7168\n"
"s_xor_b32 s89, s93, 0xe000\n"
"buffer_load_dwordx4 v12, s[72:75], s89 offen lds\n"
"s_add_u32 m0, s53, 8192\n"
"s_xor_b32 s89, s93, 0x10000\n"
"buffer_load_dwordx4 v2, s[72:75], s89 offen lds\n"
"s_add_u32 m0, s53, 9216\n"
"s_xor_b32 s89, s93, 0x12000\n"
"buffer_load_dwordx4 v2, s[72:75], s89 offen lds\n"
"s_add_u32 m0, s53, 10240\n"
"s_xor_b32 s89, s93, 0x14000\n"
"buffer_load_dwordx4 v12, s[72:75], s89 offen lds\n"
"s_add_u32 m0, s53, 11264\n"
"s_xor_b32 s89, s93, 0x16000\n"
"buffer_load_dwordx4 v12, s[72:75], s89 offen lds\n"
"s_add_u32 m0, s53, 12288\n"
"s_xor_b32 s89, s93, 0x18000\n"
"buffer_load_dwordx4 v2, s[72:75], s89 offen lds\n"
"s_add_u32 m0, s53, 13312\n"
"s_xor_b32 s89, s93, 0x1a000\n"
"buffer_load_dwordx4 v2, s[72:75], s89 offen lds\n"
"s_add_u32 m0, s53, 14336\n"
"s_xor_b32 s89, s93, 0x1c000\n"
"buffer_load_dwordx4 v12, s[72:75], s89 offen lds\n"
"s_add_u32 m0, s53, 15360\n"
"s_xor_b32 s89, s93, 0x1e000\n"
"buffer_load_dwordx4 v12, s[72:75], s89 offen lds\n"
"s_add_u32 m0, s53, 16384\n"
"s_xor_b32 s89, s93, 0x20000\n"
"buffer_load_dwordx4 v2, s[72:75], s89 offen lds\n"
"s_add_u32 m0, s53, 17408\n"
"s_xor_b32 s89, s93, 0x22000\n"
"buffer_load_dwordx4 v2, s[72:75], s89 offen lds\n"
"s_add_u32 m0, s53, 18432\n"
"s_xor_b32 s89, s93, 0x24000\n"
"buffer_load_dwordx4 v12, s[72:75], s89 offen lds\n"
"s_add_u32 m0, s53, 19456\n"
"s_xor_b32 s89, s93, 0x26000\n"
"buffer_load_dwordx4 v12, s[72:75], s89 offen lds\n"
"s_add_u32 m0, s53, 20480\n"
"s_xor_b32 s89, s93, 0x28000\n"
"buffer_load_dwordx4 v2, s[72:75], s89 offen lds\n"
"s_add_u32 m0, s53, 21504\n"
"s_xor_b32 s89, s93, 0x2a000\n"
"buffer_load_dwordx4 v2, s[72:75], s89 offen lds\n"
"s_add_u32 m0, s53, 22528\n"
"s_xor_b32 s89, s93, 0x2c000\n"
"buffer_load_dwordx4 v12, s[72:75], s89 offen lds\n"
"s_add_u32 m0, s53, 23552\n"
"s_xor_b32 s89, s93, 0x2e000\n"
"buffer_load_dwordx4 v12, s[72:75], s89 offen lds\n"
"s_add_u32 m0, s53, 24576\n"
"s_xor_b32 s89, s93, 0x30000\n"
"buffer_load_dwordx4 v2, s[72:75], s89 offen lds\n"
"s_add_u32 m0, s53, 25600\n"
"s_xor_b32 s89, s93, 0x32000\n"
"buffer_load_dwordx4 v2, s[72:75], s89 offen lds\n"
"s_add_u32 m0, s53, 26624\n"
"s_xor_b32 s89, s93, 0x34000\n"
"buffer_load_dwordx4 v12, s[72:75], s89 offen lds\n"
"s_add_u32 m0, s53, 27648\n"
"s_xor_b32 s89, s93, 0x36000\n"
"buffer_load_dwordx4 v12, s[72:75], s89 offen lds\n"
"s_add_u32 m0, s53, 28672\n"
"s_xor_b32 s89, s93, 0x38000\n"
"buffer_load_dwordx4 v2, s[72:75], s89 offen lds\n"
"s_add_u32 m0, s53, 29696\n"
"s_xor_b32 s89, s93, 0x3a000\n"
"buffer_load_dwordx4 v2, s[72:75], s89 offen lds\n"
"s_add_u32 m0, s53, 30720\n"
"s_xor_b32 s89, s93, 0x3c000\n"
"buffer_load_dwordx4 v12, s[72:75], s89 offen lds\n"
"s_add_u32 m0, s53, 31744\n"
"s_xor_b32 s89, s93, 0x3e000\n"
"buffer_load_dwordx4 v12, s[72:75], s89 offen lds\n"
"s_waitcnt vmcnt(16)\n"
"v_xor_b32 v13, 0, v3\n"
"ds_read_b64_tr_b16 v[16:17], v13 offset:0\n"
"ds_read_b64_tr_b16 v[18:19], v13 offset:256\n"
"ds_read_b64_tr_b16 v[20:21], v13 offset:4096\n"
"ds_read_b64_tr_b16 v[22:23], v13 offset:4352\n"
"ds_read_b64_tr_b16 a[0:1], v13 offset:8192\n"
"ds_read_b64_tr_b16 a[2:3], v13 offset:8448\n"
"ds_read_b64_tr_b16 a[4:5], v13 offset:12288\n"
"ds_read_b64_tr_b16 a[6:7], v13 offset:12544\n"
"s_waitcnt lgkmcnt(4)\n"
"v_xor_b32 v13, 32, v3\n"
"ds_read_b64_tr_b16 v[24:25], v13 offset:0\n"
"ds_read_b64_tr_b16 v[26:27], v13 offset:256\n"
"ds_read_b64_tr_b16 v[28:29], v13 offset:4096\n"
"ds_read_b64_tr_b16 v[30:31], v13 offset:4352\n"
"ds_read_b64_tr_b16 a[8:9], v13 offset:8192\n"
"ds_read_b64_tr_b16 a[10:11], v13 offset:8448\n"
"ds_read_b64_tr_b16 a[12:13], v13 offset:12288\n"
"ds_read_b64_tr_b16 a[14:15], v13 offset:12544\n"
"s_waitcnt lgkmcnt(4)\n"
"v_xor_b32 v13, 64, v3\n"
"ds_read_b64_tr_b16 v[32:33], v13 offset:0\n"
"ds_read_b64_tr_b16 v[34:35], v13 offset:256\n"
"ds_read_b64_tr_b16 v[36:37], v13 offset:4096\n"
"ds_read_b64_tr_b16 v[38:39], v13 offset:4352\n"
"ds_read_b64_tr_b16 a[16:17], v13 offset:8192\n"
"ds_read_b64_tr_b16 a[18:19], v13 offset:8448\n"
"ds_read_b64_tr_b16 a[20:21], v13 offset:12288\n"
"ds_read_b64_tr_b16 a[22:23], v13 offset:12544\n"
"s_waitcnt lgkmcnt(4)\n"
"v_xor_b32 v13, 96, v3\n"
"ds_read_b64_tr_b16 v[40:41], v13 offset:0\n"
"ds_read_b64_tr_b16 v[42:43], v13 offset:256\n"
"ds_read_b64_tr_b16 v[44:45], v13 offset:4096\n"
"ds_read_b64_tr_b16 v[46:47], v13 offset:4352\n"
"ds_read_b64_tr_b16 a[24:25], v13 offset:8192\n"
"ds_read_b64_tr_b16 a[26:27], v13 offset:8448\n"
"ds_read_b64_tr_b16 a[28:29], v13 offset:12288\n"
"ds_read_b64_tr_b16 a[30:31], v13 offset:12544\n"
"s_waitcnt lgkmcnt(0)\n"
"s_add_u32 m0, s53, 0\n"
"s_xor_b32 s89, s93, 0x40000\n"
"buffer_load_dwordx4 v2, s[72:75], s89 offen lds\n"
"s_add_u32 m0, s53, 1024\n"
"s_xor_b32 s89, s93, 0x42000\n"
"buffer_load_dwordx4 v2, s[72:75], s89 offen lds\n"
"s_add_u32 m0, s53, 2048\n"
"s_xor_b32 s89, s93, 0x44000\n"
"buffer_load_dwordx4 v12, s[72:75], s89 offen lds\n"
"s_add_u32 m0, s53, 3072\n"
"s_xor_b32 s89, s93, 0x46000\n"
"buffer_load_dwordx4 v12, s[72:75], s89 offen lds\n"
"s_add_u32 m0, s53, 4096\n"
"s_xor_b32 s89, s93, 0x48000\n"
"buffer_load_dwordx4 v2, s[72:75], s89 offen lds\n"
"s_add_u32 m0, s53, 5120\n"
"s_xor_b32 s89, s93, 0x4a000\n"
"buffer_load_dwordx4 v2, s[72:75], s89 offen lds\n"
"s_add_u32 m0, s53, 6144\n"
"s_xor_b32 s89, s93, 0x4c000\n"
"buffer_load_dwordx4 v12, s[72:75], s89 offen lds\n"
"s_add_u32 m0, s53, 7168\n"
"s_xor_b32 s89, s93, 0x4e000\n"
"buffer_load_dwordx4 v12, s[72:75], s89 offen lds\n"
"s_add_u32 m0, s53, 8192\n"
"s_xor_b32 s89, s93, 0x50000\n"
"buffer_load_dwordx4 v2, s[72:75], s89 offen lds\n"
"s_add_u32 m0, s53, 9216\n"
"s_xor_b32 s89, s93, 0x52000\n"
"buffer_load_dwordx4 v2, s[72:75], s89 offen lds\n"
"s_add_u32 m0, s53, 10240\n"
"s_xor_b32 s89, s93, 0x54000\n"
"buffer_load_dwordx4 v12, s[72:75], s89 offen lds\n"
"s_add_u32 m0, s53, 11264\n"
"s_xor_b32 s89, s93, 0x56000\n"
"buffer_load_dwordx4 v12, s[72:75], s89 offen lds\n"
"s_add_u32 m0, s53, 12288\n"
"s_xor_b32 s89, s93, 0x58000\n"
"buffer_load_dwordx4 v2, s[72:75], s89 offen lds\n"
"s_add_u32 m0, s53, 13312\n"
"s_xor_b32 s89, s93, 0x5a000\n"
"buffer_load_dwordx4 v2, s[72:75], s89 offen lds\n"
"s_add_u32 m0, s53, 14336\n"
"s_xor_b32 s89, s93, 0x5c000\n"
"buffer_load_dwordx4 v12, s[72:75], s89 offen lds\n"
"s_add_u32 m0, s53, 15360\n"
"s_xor_b32 s89, s93, 0x5e000\n"
"buffer_load_dwordx4 v12, s[72:75], s89 offen lds\n"
"s_waitcnt vmcnt(16)\n"
"v_xor_b32 v13, 0, v3\n"
"ds_read_b64_tr_b16 a[32:33], v13 offset:16384\n"
"ds_read_b64_tr_b16 a[34:35], v13 offset:16640\n"
"ds_read_b64_tr_b16 a[36:37], v13 offset:20480\n"
"ds_read_b64_tr_b16 a[38:39], v13 offset:20736\n"
"ds_read_b64_tr_b16 a[64:65], v13 offset:24576\n"
"ds_read_b64_tr_b16 a[66:67], v13 offset:24832\n"
"ds_read_b64_tr_b16 a[68:69], v13 offset:28672\n"
"ds_read_b64_tr_b16 a[70:71], v13 offset:28928\n"
"s_waitcnt lgkmcnt(4)\n"
"v_xor_b32 v13, 32, v3\n"
"ds_read_b64_tr_b16 a[40:41], v13 offset:16384\n"
"ds_read_b64_tr_b16 a[42:43], v13 offset:16640\n"
"ds_read_b64_tr_b16 a[44:45], v13 offset:20480\n"
"ds_read_b64_tr_b16 a[46:47], v13 offset:20736\n"
"ds_read_b64_tr_b16 a[72:73], v13 offset:24576\n"
"ds_read_b64_tr_b16 a[74:75], v13 offset:24832\n"
"ds_read_b64_tr_b16 a[76:77], v13 offset:28672\n"
"ds_read_b64_tr_b16 a[78:79], v13 offset:28928\n"
"s_waitcnt lgkmcnt(4)\n"
"v_xor_b32 v13, 64, v3\n"
"ds_read_b64_tr_b16 a[48:49], v13 offset:16384\n"
"ds_read_b64_tr_b16 a[50:51], v13 offset:16640\n"
"ds_read_b64_tr_b16 a[52:53], v13 offset:20480\n"
"ds_read_b64_tr_b16 a[54:55], v13 offset:20736\n"
"ds_read_b64_tr_b16 a[80:81], v13 offset:24576\n"
"ds_read_b64_tr_b16 a[82:83], v13 offset:24832\n"
"ds_read_b64_tr_b16 a[84:85], v13 offset:28672\n"
"ds_read_b64_tr_b16 a[86:87], v13 offset:28928\n"
"s_waitcnt lgkmcnt(4)\n"
"v_xor_b32 v13, 96, v3\n"
"ds_read_b64_tr_b16 a[56:57], v13 offset:16384\n"
"ds_read_b64_tr_b16 a[58:59], v13 offset:16640\n"
"ds_read_b64_tr_b16 a[60:61], v13 offset:20480\n"
"ds_read_b64_tr_b16 a[62:63], v13 offset:20736\n"
"ds_read_b64_tr_b16 a[88:89], v13 offset:24576\n"
"ds_read_b64_tr_b16 a[90:91], v13 offset:24832\n"
"ds_read_b64_tr_b16 a[92:93], v13 offset:28672\n"
"ds_read_b64_tr_b16 a[94:95], v13 offset:28928\n"
"s_waitcnt lgkmcnt(0)\n"
"s_add_u32 m0, s53, 16384\n"
"s_xor_b32 s89, s93, 0x60000\n"
"buffer_load_dwordx4 v2, s[72:75], s89 offen lds\n"
"s_add_u32 m0, s53, 17408\n"
"s_xor_b32 s89, s93, 0x62000\n"
"buffer_load_dwordx4 v2, s[72:75], s89 offen lds\n"
"s_add_u32 m0, s53, 18432\n"
"s_xor_b32 s89, s93, 0x64000\n"
"buffer_load_dwordx4 v12, s[72:75], s89 offen lds\n"
"s_add_u32 m0, s53, 19456\n"
"s_xor_b32 s89, s93, 0x66000\n"
"buffer_load_dwordx4 v12, s[72:75], s89 offen lds\n"
"s_add_u32 m0, s53, 20480\n"
"s_xor_b32 s89, s93, 0x68000\n"
"buffer_load_dwordx4 v2, s[72:75], s89 offen lds\n"
"s_add_u32 m0, s53, 21504\n"
"s_xor_b32 s89, s93, 0x6a000\n"
"buffer_load_dwordx4 v2, s[72:75], s89 offen lds\n"
"s_add_u32 m0, s53, 22528\n"
"s_xor_b32 s89, s93, 0x6c000\n"
"buffer_load_dwordx4 v12, s[72:75], s89 offen lds\n"
"s_add_u32 m0, s53, 23552\n"
"s_xor_b32 s89, s93, 0x6e000\n"
"buffer_load_dwordx4 v12, s[72:75], s89 offen lds\n"
"s_add_u32 m0, s53, 24576\n"
"s_xor_b32 s89, s93, 0x70000\n"
"buffer_load_dwordx4 v2, s[72:75], s89 offen lds\n"
"s_add_u32 m0, s53, 25600\n"
"s_xor_b32 s89, s93, 0x72000\n"
"buffer_load_dwordx4 v2, s[72:75], s89 offen lds\n"
"s_add_u32 m0, s53, 26624\n"
"s_xor_b32 s89, s93, 0x74000\n"
"buffer_load_dwordx4 v12, s[72:75], s89 offen lds\n"
"s_add_u32 m0, s53, 27648\n"
"s_xor_b32 s89, s93, 0x76000\n"
"buffer_load_dwordx4 v12, s[72:75], s89 offen lds\n"
"s_add_u32 m0, s53, 28672\n"
"s_xor_b32 s89, s93, 0x78000\n"
"buffer_load_dwordx4 v2, s[72:75], s89 offen lds\n"
"s_add_u32 m0, s53, 29696\n"
"s_xor_b32 s89, s93, 0x7a000\n"
"buffer_load_dwordx4 v2, s[72:75], s89 offen lds\n"
"s_add_u32 m0, s53, 30720\n"
"s_xor_b32 s89, s93, 0x7c000\n"
"buffer_load_dwordx4 v12, s[72:75], s89 offen lds\n"
"s_add_u32 m0, s53, 31744\n"
"s_xor_b32 s89, s93, 0x7e000\n"
"buffer_load_dwordx4 v12, s[72:75], s89 offen lds\n"
"s_waitcnt vmcnt(16)\n"
"v_xor_b32 v13, 0, v3\n"
"ds_read_b64_tr_b16 a[96:97], v13 offset:0\n"
"ds_read_b64_tr_b16 a[98:99], v13 offset:256\n"
"ds_read_b64_tr_b16 a[100:101], v13 offset:4096\n"
"ds_read_b64_tr_b16 a[102:103], v13 offset:4352\n"
"ds_read_b64_tr_b16 v[48:49], v13 offset:8192\n"
"ds_read_b64_tr_b16 v[50:51], v13 offset:8448\n"
"ds_read_b64_tr_b16 v[52:53], v13 offset:12288\n"
"ds_read_b64_tr_b16 v[54:55], v13 offset:12544\n"
"s_waitcnt lgkmcnt(4)\n"
"v_xor_b32 v13, 32, v3\n"
"ds_read_b64_tr_b16 a[104:105], v13 offset:0\n"
"ds_read_b64_tr_b16 a[106:107], v13 offset:256\n"
"ds_read_b64_tr_b16 a[108:109], v13 offset:4096\n"
"ds_read_b64_tr_b16 a[110:111], v13 offset:4352\n"
"ds_read_b64_tr_b16 v[56:57], v13 offset:8192\n"
"ds_read_b64_tr_b16 v[58:59], v13 offset:8448\n"
"ds_read_b64_tr_b16 v[60:61], v13 offset:12288\n"
"ds_read_b64_tr_b16 v[62:63], v13 offset:12544\n"
"s_waitcnt lgkmcnt(4)\n"
"v_xor_b32 v13, 64, v3\n"
"ds_read_b64_tr_b16 a[112:113], v13 offset:0\n"
"ds_read_b64_tr_b16 a[114:115], v13 offset:256\n"
"ds_read_b64_tr_b16 a[116:117], v13 offset:4096\n"
"ds_read_b64_tr_b16 a[118:119], v13 offset:4352\n"
"ds_read_b64_tr_b16 v[64:65], v13 offset:8192\n"
"ds_read_b64_tr_b16 v[66:67], v13 offset:8448\n"
"ds_read_b64_tr_b16 v[68:69], v13 offset:12288\n"
"ds_read_b64_tr_b16 v[70:71], v13 offset:12544\n"
"s_waitcnt lgkmcnt(4)\n"
"v_xor_b32 v13, 96, v3\n"
"ds_read_b64_tr_b16 a[120:121], v13 offset:0\n"
"ds_read_b64_tr_b16 a[122:123], v13 offset:256\n"
"ds_read_b64_tr_b16 a[124:125], v13 offset:4096\n"
"ds_read_b64_tr_b16 a[126:127], v13 offset:4352\n"
"ds_read_b64_tr_b16 v[72:73], v13 offset:8192\n"
"ds_read_b64_tr_b16 v[74:75], v13 offset:8448\n"
"ds_read_b64_tr_b16 v[76:77], v13 offset:12288\n"
"ds_read_b64_tr_b16 v[78:79], v13 offset:12544\n"
"s_waitcnt lgkmcnt(0)\n"
"s_add_u32 m0, s53, 0\n"
"s_xor_b32 s89, s93, 0x80000\n"
"buffer_load_dwordx4 v2, s[72:75], s89 offen lds\n"
"s_add_u32 m0, s53, 1024\n"
"s_xor_b32 s89, s93, 0x82000\n"
"buffer_load_dwordx4 v2, s[72:75], s89 offen lds\n"
"s_add_u32 m0, s53, 2048\n"
"s_xor_b32 s89, s93, 0x84000\n"
"buffer_load_dwordx4 v12, s[72:75], s89 offen lds\n"
"s_add_u32 m0, s53, 3072\n"
"s_xor_b32 s89, s93, 0x86000\n"
"buffer_load_dwordx4 v12, s[72:75], s89 offen lds\n"
"s_add_u32 m0, s53, 4096\n"
"s_xor_b32 s89, s93, 0x88000\n"
"buffer_load_dwordx4 v2, s[72:75], s89 offen lds\n"
"s_add_u32 m0, s53, 5120\n"
"s_xor_b32 s89, s93, 0x8a000\n"
"buffer_load_dwordx4 v2, s[72:75], s89 offen lds\n"
"s_add_u32 m0, s53, 6144\n"
"s_xor_b32 s89, s93, 0x8c000\n"
"buffer_load_dwordx4 v12, s[72:75], s89 offen lds\n"
"s_add_u32 m0, s53, 7168\n"
"s_xor_b32 s89, s93, 0x8e000\n"
"buffer_load_dwordx4 v12, s[72:75], s89 offen lds\n"
"s_add_u32 m0, s53, 8192\n"
"s_xor_b32 s89, s93, 0x90000\n"
"buffer_load_dwordx4 v2, s[72:75], s89 offen lds\n"
"s_add_u32 m0, s53, 9216\n"
"s_xor_b32 s89, s93, 0x92000\n"
"buffer_load_dwordx4 v2, s[72:75], s89 offen lds\n"
"s_add_u32 m0, s53, 10240\n"
"s_xor_b32 s89, s93, 0x94000\n"
"buffer_load_dwordx4 v12, s[72:75], s89 offen lds\n"
"s_add_u32 m0, s53, 11264\n"
"s_xor_b32 s89, s93, 0x96000\n"
"buffer_load_dwordx4 v12, s[72:75], s89 offen lds\n"
"s_add_u32 m0, s53, 12288\n"
"s_xor_b32 s89, s93, 0x98000\n"
"buffer_load_dwordx4 v2, s[72:75], s89 offen lds\n"
"s_add_u32 m0, s53, 13312\n"
"s_xor_b32 s89, s93, 0x9a000\n"
"buffer_load_dwordx4 v2, s[72:75], s89 offen lds\n"
"s_add_u32 m0, s53, 14336\n"
"s_xor_b32 s89, s93, 0x9c000\n"
"buffer_load_dwordx4 v12, s[72:75], s89 offen lds\n"
"s_add_u32 m0, s53, 15360\n"
"s_xor_b32 s89, s93, 0x9e000\n"
"buffer_load_dwordx4 v12, s[72:75], s89 offen lds\n"
"s_waitcnt vmcnt(16)\n"
"v_xor_b32 v13, 0, v3\n"
"ds_read_b64_tr_b16 a[128:129], v13 offset:16384\n"
"ds_read_b64_tr_b16 a[130:131], v13 offset:16640\n"
"ds_read_b64_tr_b16 a[132:133], v13 offset:20480\n"
"ds_read_b64_tr_b16 a[134:135], v13 offset:20736\n"
"ds_read_b64_tr_b16 a[160:161], v13 offset:24576\n"
"ds_read_b64_tr_b16 a[162:163], v13 offset:24832\n"
"ds_read_b64_tr_b16 a[164:165], v13 offset:28672\n"
"ds_read_b64_tr_b16 a[166:167], v13 offset:28928\n"
"s_waitcnt lgkmcnt(4)\n"
"v_xor_b32 v13, 32, v3\n"
"ds_read_b64_tr_b16 a[136:137], v13 offset:16384\n"
"ds_read_b64_tr_b16 a[138:139], v13 offset:16640\n"
"ds_read_b64_tr_b16 a[140:141], v13 offset:20480\n"
"ds_read_b64_tr_b16 a[142:143], v13 offset:20736\n"
"ds_read_b64_tr_b16 a[168:169], v13 offset:24576\n"
"ds_read_b64_tr_b16 a[170:171], v13 offset:24832\n"
"ds_read_b64_tr_b16 a[172:173], v13 offset:28672\n"
"ds_read_b64_tr_b16 a[174:175], v13 offset:28928\n"
"s_waitcnt lgkmcnt(4)\n"
"v_xor_b32 v13, 64, v3\n"
"ds_read_b64_tr_b16 a[144:145], v13 offset:16384\n"
"ds_read_b64_tr_b16 a[146:147], v13 offset:16640\n"
"ds_read_b64_tr_b16 a[148:149], v13 offset:20480\n"
"ds_read_b64_tr_b16 a[150:151], v13 offset:20736\n"
"ds_read_b64_tr_b16 a[176:177], v13 offset:24576\n"
"ds_read_b64_tr_b16 a[178:179], v13 offset:24832\n"
"ds_read_b64_tr_b16 a[180:181], v13 offset:28672\n"
"ds_read_b64_tr_b16 a[182:183], v13 offset:28928\n"
"s_waitcnt lgkmcnt(4)\n"
"v_xor_b32 v13, 96, v3\n"
"ds_read_b64_tr_b16 a[152:153], v13 offset:16384\n"
"ds_read_b64_tr_b16 a[154:155], v13 offset:16640\n"
"ds_read_b64_tr_b16 a[156:157], v13 offset:20480\n"
"ds_read_b64_tr_b16 a[158:159], v13 offset:20736\n"
"ds_read_b64_tr_b16 a[184:185], v13 offset:24576\n"
"ds_read_b64_tr_b16 a[186:187], v13 offset:24832\n"
"ds_read_b64_tr_b16 a[188:189], v13 offset:28672\n"
"ds_read_b64_tr_b16 a[190:191], v13 offset:28928\n"
"s_waitcnt lgkmcnt(0)\n"
"s_add_u32 m0, s53, 16384\n"
"s_xor_b32 s89, s93, 0xa0000\n"
"buffer_load_dwordx4 v2, s[72:75], s89 offen lds\n"
"s_add_u32 m0, s53, 17408\n"
"s_xor_b32 s89, s93, 0xa2000\n"
"buffer_load_dwordx4 v2, s[72:75], s89 offen lds\n"
"s_add_u32 m0, s53, 18432\n"
"s_xor_b32 s89, s93, 0xa4000\n"
"buffer_load_dwordx4 v12, s[72:75], s89 offen lds\n"
"s_add_u32 m0, s53, 19456\n"
"s_xor_b32 s89, s93, 0xa6000\n"
"buffer_load_dwordx4 v12, s[72:75], s89 offen lds\n"
"s_add_u32 m0, s53, 20480\n"
"s_xor_b32 s89, s93, 0xa8000\n"
"buffer_load_dwordx4 v2, s[72:75], s89 offen lds\n"
"s_add_u32 m0, s53, 21504\n"
"s_xor_b32 s89, s93, 0xaa000\n"
"buffer_load_dwordx4 v2, s[72:75], s89 offen lds\n"
"s_add_u32 m0, s53, 22528\n"
"s_xor_b32 s89, s93, 0xac000\n"
"buffer_load_dwordx4 v12, s[72:75], s89 offen lds\n"
"s_add_u32 m0, s53, 23552\n"
"s_xor_b32 s89, s93, 0xae000\n"
"buffer_load_dwordx4 v12, s[72:75], s89 offen lds\n"
"s_add_u32 m0, s53, 24576\n"
"s_xor_b32 s89, s93, 0xb0000\n"
"buffer_load_dwordx4 v2, s[72:75], s89 offen lds\n"
"s_add_u32 m0, s53, 25600\n"
"s_xor_b32 s89, s93, 0xb2000\n"
"buffer_load_dwordx4 v2, s[72:75], s89 offen lds\n"
"s_add_u32 m0, s53, 26624\n"
"s_xor_b32 s89, s93, 0xb4000\n"
"buffer_load_dwordx4 v12, s[72:75], s89 offen lds\n"
"s_add_u32 m0, s53, 27648\n"
"s_xor_b32 s89, s93, 0xb6000\n"
"buffer_load_dwordx4 v12, s[72:75], s89 offen lds\n"
"s_add_u32 m0, s53, 28672\n"
"s_xor_b32 s89, s93, 0xb8000\n"
"buffer_load_dwordx4 v2, s[72:75], s89 offen lds\n"
"s_add_u32 m0, s53, 29696\n"
"s_xor_b32 s89, s93, 0xba000\n"
"buffer_load_dwordx4 v2, s[72:75], s89 offen lds\n"
"s_add_u32 m0, s53, 30720\n"
"s_xor_b32 s89, s93, 0xbc000\n"
"buffer_load_dwordx4 v12, s[72:75], s89 offen lds\n"
"s_add_u32 m0, s53, 31744\n"
"s_xor_b32 s89, s93, 0xbe000\n"
"buffer_load_dwordx4 v12, s[72:75], s89 offen lds\n"
"s_waitcnt vmcnt(16)\n"
"v_xor_b32 v13, 0, v3\n"
"ds_read_b64_tr_b16 a[192:193], v13 offset:0\n"
"ds_read_b64_tr_b16 a[194:195], v13 offset:256\n"
"ds_read_b64_tr_b16 a[196:197], v13 offset:4096\n"
"ds_read_b64_tr_b16 a[198:199], v13 offset:4352\n"
"ds_read_b64_tr_b16 a[224:225], v13 offset:8192\n"
"ds_read_b64_tr_b16 a[226:227], v13 offset:8448\n"
"ds_read_b64_tr_b16 a[228:229], v13 offset:12288\n"
"ds_read_b64_tr_b16 a[230:231], v13 offset:12544\n"
"s_waitcnt lgkmcnt(4)\n"
"v_xor_b32 v13, 32, v3\n"
"ds_read_b64_tr_b16 a[200:201], v13 offset:0\n"
"ds_read_b64_tr_b16 a[202:203], v13 offset:256\n"
"ds_read_b64_tr_b16 a[204:205], v13 offset:4096\n"
"ds_read_b64_tr_b16 a[206:207], v13 offset:4352\n"
"ds_read_b64_tr_b16 a[232:233], v13 offset:8192\n"
"ds_read_b64_tr_b16 a[234:235], v13 offset:8448\n"
"ds_read_b64_tr_b16 a[236:237], v13 offset:12288\n"
"ds_read_b64_tr_b16 a[238:239], v13 offset:12544\n"
"s_waitcnt lgkmcnt(4)\n"
"v_xor_b32 v13, 64, v3\n"
"ds_read_b64_tr_b16 a[208:209], v13 offset:0\n"
"ds_read_b64_tr_b16 a[210:211], v13 offset:256\n"
"ds_read_b64_tr_b16 a[212:213], v13 offset:4096\n"
"ds_read_b64_tr_b16 a[214:215], v13 offset:4352\n"
"ds_read_b64_tr_b16 a[240:241], v13 offset:8192\n"
"ds_read_b64_tr_b16 a[242:243], v13 offset:8448\n"
"ds_read_b64_tr_b16 a[244:245], v13 offset:12288\n"
"ds_read_b64_tr_b16 a[246:247], v13 offset:12544\n"
"s_waitcnt lgkmcnt(4)\n"
"v_xor_b32 v13, 96, v3\n"
"ds_read_b64_tr_b16 a[216:217], v13 offset:0\n"
"ds_read_b64_tr_b16 a[218:219], v13 offset:256\n"
"ds_read_b64_tr_b16 a[220:221], v13 offset:4096\n"
"ds_read_b64_tr_b16 a[222:223], v13 offset:4352\n"
"ds_read_b64_tr_b16 a[248:249], v13 offset:8192\n"
"ds_read_b64_tr_b16 a[250:251], v13 offset:8448\n"
"ds_read_b64_tr_b16 a[252:253], v13 offset:12288\n"
"ds_read_b64_tr_b16 a[254:255], v13 offset:12544\n"
"s_waitcnt lgkmcnt(0)\n"
"s_add_u32 m0, s53, 0\n"
"s_xor_b32 s89, s93, 0xc0000\n"
"buffer_load_dwordx4 v2, s[72:75], s89 offen lds\n"
"s_add_u32 m0, s53, 1024\n"
"s_xor_b32 s89, s93, 0xc2000\n"
"buffer_load_dwordx4 v2, s[72:75], s89 offen lds\n"
"s_add_u32 m0, s53, 2048\n"
"s_xor_b32 s89, s93, 0xc4000\n"
"buffer_load_dwordx4 v12, s[72:75], s89 offen lds\n"
"s_add_u32 m0, s53, 3072\n"
"s_xor_b32 s89, s93, 0xc6000\n"
"buffer_load_dwordx4 v12, s[72:75], s89 offen lds\n"
"s_add_u32 m0, s53, 4096\n"
"s_xor_b32 s89, s93, 0xc8000\n"
"buffer_load_dwordx4 v2, s[72:75], s89 offen lds\n"
"s_add_u32 m0, s53, 5120\n"
"s_xor_b32 s89, s93, 0xca000\n"
"buffer_load_dwordx4 v2, s[72:75], s89 offen lds\n"
"s_add_u32 m0, s53, 6144\n"
"s_xor_b32 s89, s93, 0xcc000\n"
"buffer_load_dwordx4 v12, s[72:75], s89 offen lds\n"
"s_add_u32 m0, s53, 7168\n"
"s_xor_b32 s89, s93, 0xce000\n"
"buffer_load_dwordx4 v12, s[72:75], s89 offen lds\n"
"s_add_u32 m0, s53, 8192\n"
"s_xor_b32 s89, s93, 0xd0000\n"
"buffer_load_dwordx4 v2, s[72:75], s89 offen lds\n"
"s_add_u32 m0, s53, 9216\n"
"s_xor_b32 s89, s93, 0xd2000\n"
"buffer_load_dwordx4 v2, s[72:75], s89 offen lds\n"
"s_add_u32 m0, s53, 10240\n"
"s_xor_b32 s89, s93, 0xd4000\n"
"buffer_load_dwordx4 v12, s[72:75], s89 offen lds\n"
"s_add_u32 m0, s53, 11264\n"
"s_xor_b32 s89, s93, 0xd6000\n"
"buffer_load_dwordx4 v12, s[72:75], s89 offen lds\n"
"s_add_u32 m0, s53, 12288\n"
"s_xor_b32 s89, s93, 0xd8000\n"
"buffer_load_dwordx4 v2, s[72:75], s89 offen lds\n"
"s_add_u32 m0, s53, 13312\n"
"s_xor_b32 s89, s93, 0xda000\n"
"buffer_load_dwordx4 v2, s[72:75], s89 offen lds\n"
"s_add_u32 m0, s53, 14336\n"
"s_xor_b32 s89, s93, 0xdc000\n"
"buffer_load_dwordx4 v12, s[72:75], s89 offen lds\n"
"s_add_u32 m0, s53, 15360\n"
"s_xor_b32 s89, s93, 0xde000\n"
"buffer_load_dwordx4 v12, s[72:75], s89 offen lds\n"
"s_waitcnt vmcnt(16)\n"
"v_xor_b32 v13, 0, v3\n"
"ds_read_b64_tr_b16 v[80:81], v13 offset:16384\n"
"ds_read_b64_tr_b16 v[82:83], v13 offset:16640\n"
"ds_read_b64_tr_b16 v[84:85], v13 offset:20480\n"
"ds_read_b64_tr_b16 v[86:87], v13 offset:20736\n"
"ds_read_b64_tr_b16 v[128:129], v13 offset:24576\n"
"ds_read_b64_tr_b16 v[130:131], v13 offset:24832\n"
"ds_read_b64_tr_b16 v[132:133], v13 offset:28672\n"
"ds_read_b64_tr_b16 v[134:135], v13 offset:28928\n"
"s_waitcnt lgkmcnt(4)\n"
"v_xor_b32 v13, 32, v3\n"
"ds_read_b64_tr_b16 v[88:89], v13 offset:16384\n"
"ds_read_b64_tr_b16 v[90:91], v13 offset:16640\n"
"ds_read_b64_tr_b16 v[92:93], v13 offset:20480\n"
"ds_read_b64_tr_b16 v[94:95], v13 offset:20736\n"
"ds_read_b64_tr_b16 v[136:137], v13 offset:24576\n"
"ds_read_b64_tr_b16 v[138:139], v13 offset:24832\n"
"ds_read_b64_tr_b16 v[140:141], v13 offset:28672\n"
"ds_read_b64_tr_b16 v[142:143], v13 offset:28928\n"
"s_waitcnt lgkmcnt(4)\n"
"v_xor_b32 v13, 64, v3\n"
"ds_read_b64_tr_b16 v[96:97], v13 offset:16384\n"
"ds_read_b64_tr_b16 v[98:99], v13 offset:16640\n"
"ds_read_b64_tr_b16 v[100:101], v13 offset:20480\n"
"ds_read_b64_tr_b16 v[102:103], v13 offset:20736\n"
"ds_read_b64_tr_b16 v[144:145], v13 offset:24576\n"
"ds_read_b64_tr_b16 v[146:147], v13 offset:24832\n"
"ds_read_b64_tr_b16 v[148:149], v13 offset:28672\n"
"ds_read_b64_tr_b16 v[150:151], v13 offset:28928\n"
"s_waitcnt lgkmcnt(4)\n"
"v_xor_b32 v13, 96, v3\n"
"ds_read_b64_tr_b16 v[104:105], v13 offset:16384\n"
"ds_read_b64_tr_b16 v[106:107], v13 offset:16640\n"
"ds_read_b64_tr_b16 v[108:109], v13 offset:20480\n"
"ds_read_b64_tr_b16 v[110:111], v13 offset:20736\n"
"ds_read_b64_tr_b16 v[152:153], v13 offset:24576\n"
"ds_read_b64_tr_b16 v[154:155], v13 offset:24832\n"
"ds_read_b64_tr_b16 v[156:157], v13 offset:28672\n"
"ds_read_b64_tr_b16 v[158:159], v13 offset:28928\n"
"s_waitcnt lgkmcnt(0)\n"
"s_add_u32 m0, s53, 16384\n"
"s_xor_b32 s89, s93, 0xe0000\n"
"buffer_load_dwordx4 v2, s[72:75], s89 offen lds\n"
"s_add_u32 m0, s53, 17408\n"
"s_xor_b32 s89, s93, 0xe2000\n"
"buffer_load_dwordx4 v2, s[72:75], s89 offen lds\n"
"s_add_u32 m0, s53, 18432\n"
"s_xor_b32 s89, s93, 0xe4000\n"
"buffer_load_dwordx4 v12, s[72:75], s89 offen lds\n"
"s_add_u32 m0, s53, 19456\n"
"s_xor_b32 s89, s93, 0xe6000\n"
"buffer_load_dwordx4 v12, s[72:75], s89 offen lds\n"
"s_add_u32 m0, s53, 20480\n"
"s_xor_b32 s89, s93, 0xe8000\n"
"buffer_load_dwordx4 v2, s[72:75], s89 offen lds\n"
"s_add_u32 m0, s53, 21504\n"
"s_xor_b32 s89, s93, 0xea000\n"
"buffer_load_dwordx4 v2, s[72:75], s89 offen lds\n"
"s_add_u32 m0, s53, 22528\n"
"s_xor_b32 s89, s93, 0xec000\n"
"buffer_load_dwordx4 v12, s[72:75], s89 offen lds\n"
"s_add_u32 m0, s53, 23552\n"
"s_xor_b32 s89, s93, 0xee000\n"
"buffer_load_dwordx4 v12, s[72:75], s89 offen lds\n"
"s_add_u32 m0, s53, 24576\n"
"s_xor_b32 s89, s93, 0xf0000\n"
"buffer_load_dwordx4 v2, s[72:75], s89 offen lds\n"
"s_add_u32 m0, s53, 25600\n"
"s_xor_b32 s89, s93, 0xf2000\n"
"buffer_load_dwordx4 v2, s[72:75], s89 offen lds\n"
"s_add_u32 m0, s53, 26624\n"
"s_xor_b32 s89, s93, 0xf4000\n"
"buffer_load_dwordx4 v12, s[72:75], s89 offen lds\n"
"s_add_u32 m0, s53, 27648\n"
"s_xor_b32 s89, s93, 0xf6000\n"
"buffer_load_dwordx4 v12, s[72:75], s89 offen lds\n"
"s_add_u32 m0, s53, 28672\n"
"s_xor_b32 s89, s93, 0xf8000\n"
"buffer_load_dwordx4 v2, s[72:75], s89 offen lds\n"
"s_add_u32 m0, s53, 29696\n"
"s_xor_b32 s89, s93, 0xfa000\n"
"buffer_load_dwordx4 v2, s[72:75], s89 offen lds\n"
"s_add_u32 m0, s53, 30720\n"
"s_xor_b32 s89, s93, 0xfc000\n"
"buffer_load_dwordx4 v12, s[72:75], s89 offen lds\n"
"s_add_u32 m0, s53, 31744\n"
"s_xor_b32 s89, s93, 0xfe000\n"
"buffer_load_dwordx4 v12, s[72:75], s89 offen lds\n"
"s_waitcnt vmcnt(16)\n"
"v_xor_b32 v13, 0, v3\n"
"ds_read_b64_tr_b16 v[160:161], v13 offset:0\n"
"ds_read_b64_tr_b16 v[162:163], v13 offset:256\n"
"ds_read_b64_tr_b16 v[164:165], v13 offset:4096\n"
"ds_read_b64_tr_b16 v[166:167], v13 offset:4352\n"
"ds_read_b64_tr_b16 v[192:193], v13 offset:8192\n"
"ds_read_b64_tr_b16 v[194:195], v13 offset:8448\n"
"ds_read_b64_tr_b16 v[196:197], v13 offset:12288\n"
"ds_read_b64_tr_b16 v[198:199], v13 offset:12544\n"
"s_waitcnt lgkmcnt(4)\n"
"v_xor_b32 v13, 32, v3\n"
"ds_read_b64_tr_b16 v[168:169], v13 offset:0\n"
"ds_read_b64_tr_b16 v[170:171], v13 offset:256\n"
"ds_read_b64_tr_b16 v[172:173], v13 offset:4096\n"
"ds_read_b64_tr_b16 v[174:175], v13 offset:4352\n"
"ds_read_b64_tr_b16 v[200:201], v13 offset:8192\n"
"ds_read_b64_tr_b16 v[202:203], v13 offset:8448\n"
"ds_read_b64_tr_b16 v[204:205], v13 offset:12288\n"
"ds_read_b64_tr_b16 v[206:207], v13 offset:12544\n"
"s_waitcnt lgkmcnt(4)\n"
"v_xor_b32 v13, 64, v3\n"
"ds_read_b64_tr_b16 v[176:177], v13 offset:0\n"
"ds_read_b64_tr_b16 v[178:179], v13 offset:256\n"
"ds_read_b64_tr_b16 v[180:181], v13 offset:4096\n"
"ds_read_b64_tr_b16 v[182:183], v13 offset:4352\n"
"ds_read_b64_tr_b16 v[208:209], v13 offset:8192\n"
"ds_read_b64_tr_b16 v[210:211], v13 offset:8448\n"
"ds_read_b64_tr_b16 v[212:213], v13 offset:12288\n"
"ds_read_b64_tr_b16 v[214:215], v13 offset:12544\n"
"s_waitcnt lgkmcnt(4)\n"
"v_xor_b32 v13, 96, v3\n"
"ds_read_b64_tr_b16 v[184:185], v13 offset:0\n"
"ds_read_b64_tr_b16 v[186:187], v13 offset:256\n"
"ds_read_b64_tr_b16 v[188:189], v13 offset:4096\n"
"ds_read_b64_tr_b16 v[190:191], v13 offset:4352\n"
"ds_read_b64_tr_b16 v[216:217], v13 offset:8192\n"
"ds_read_b64_tr_b16 v[218:219], v13 offset:8448\n"
"ds_read_b64_tr_b16 v[220:221], v13 offset:12288\n"
"ds_read_b64_tr_b16 v[222:223], v13 offset:12544\n"
"s_waitcnt lgkmcnt(0)\n"
"ds_write_b128 v10, v[16:19] offset:0\n"
"ds_write_b128 v10, v[20:23] offset:1024\n"
"ds_write_b128 v10, v[24:27] offset:2048\n"
"ds_write_b128 v10, v[28:31] offset:3072\n"
"ds_write_b128 v10, v[32:35] offset:4096\n"
"ds_write_b128 v10, v[36:39] offset:5120\n"
"ds_write_b128 v10, v[40:43] offset:6144\n"
"ds_write_b128 v10, v[44:47] offset:7168\n"
"ds_write_b128 v10, v[48:51] offset:8192\n"
"ds_write_b128 v10, v[52:55] offset:9216\n"
"ds_write_b128 v10, v[56:59] offset:10240\n"
"ds_write_b128 v10, v[60:63] offset:11264\n"
"ds_write_b128 v10, v[64:67] offset:12288\n"
"ds_write_b128 v10, v[68:71] offset:13312\n"
"ds_write_b128 v10, v[72:75] offset:14336\n"
"ds_write_b128 v10, v[76:79] offset:15360\n"
"s_waitcnt lgkmcnt(0)\n"
"s_waitcnt vmcnt(0)\n"
"v_xor_b32 v13, 0, v3\n"
"ds_read_b64_tr_b16 v[16:17], v13 offset:16384\n"
"ds_read_b64_tr_b16 v[18:19], v13 offset:16640\n"
"ds_read_b64_tr_b16 v[20:21], v13 offset:20480\n"
"ds_read_b64_tr_b16 v[22:23], v13 offset:20736\n"
"ds_read_b64_tr_b16 v[224:225], v13 offset:24576\n"
"ds_read_b64_tr_b16 v[226:227], v13 offset:24832\n"
"ds_read_b64_tr_b16 v[228:229], v13 offset:28672\n"
"ds_read_b64_tr_b16 v[230:231], v13 offset:28928\n"
"s_waitcnt lgkmcnt(4)\n"
"v_xor_b32 v13, 32, v3\n"
"ds_read_b64_tr_b16 v[24:25], v13 offset:16384\n"
"ds_read_b64_tr_b16 v[26:27], v13 offset:16640\n"
"ds_read_b64_tr_b16 v[28:29], v13 offset:20480\n"
"ds_read_b64_tr_b16 v[30:31], v13 offset:20736\n"
"ds_read_b64_tr_b16 v[232:233], v13 offset:24576\n"
"ds_read_b64_tr_b16 v[234:235], v13 offset:24832\n"
"ds_read_b64_tr_b16 v[236:237], v13 offset:28672\n"
"ds_read_b64_tr_b16 v[238:239], v13 offset:28928\n"
"s_waitcnt lgkmcnt(4)\n"
"v_xor_b32 v13, 64, v3\n"
"ds_read_b64_tr_b16 v[32:33], v13 offset:16384\n"
"ds_read_b64_tr_b16 v[34:35], v13 offset:16640\n"
"ds_read_b64_tr_b16 v[36:37], v13 offset:20480\n"
"ds_read_b64_tr_b16 v[38:39], v13 offset:20736\n"
"ds_read_b64_tr_b16 v[240:241], v13 offset:24576\n"
"ds_read_b64_tr_b16 v[242:243], v13 offset:24832\n"
"ds_read_b64_tr_b16 v[244:245], v13 offset:28672\n"
"ds_read_b64_tr_b16 v[246:247], v13 offset:28928\n"
"s_waitcnt lgkmcnt(4)\n"
"v_xor_b32 v13, 96, v3\n"
"ds_read_b64_tr_b16 v[40:41], v13 offset:16384\n"
"ds_read_b64_tr_b16 v[42:43], v13 offset:16640\n"
"ds_read_b64_tr_b16 v[44:45], v13 offset:20480\n"
"ds_read_b64_tr_b16 v[46:47], v13 offset:20736\n"
"ds_read_b64_tr_b16 v[248:249], v13 offset:24576\n"
"ds_read_b64_tr_b16 v[250:251], v13 offset:24832\n"
"ds_read_b64_tr_b16 v[252:253], v13 offset:28672\n"
"ds_read_b64_tr_b16 v[254:255], v13 offset:28928\n"
"s_waitcnt lgkmcnt(0)\n"
"ds_write_b128 v10, v[80:83] offset:16384\n"
"ds_write_b128 v10, v[84:87] offset:17408\n"
"ds_write_b128 v10, v[88:91] offset:18432\n"
"ds_write_b128 v10, v[92:95] offset:19456\n"
"ds_write_b128 v10, v[96:99] offset:20480\n"
"ds_write_b128 v10, v[100:103] offset:21504\n"
"ds_write_b128 v10, v[104:107] offset:22528\n"
"ds_write_b128 v10, v[108:111] offset:23552\n"
"ds_write_b128 v10, v[16:19] offset:24576\n"
"ds_write_b128 v10, v[20:23] offset:25600\n"
"ds_write_b128 v10, v[24:27] offset:26624\n"
"ds_write_b128 v10, v[28:31] offset:27648\n"
"ds_write_b128 v10, v[32:35] offset:28672\n"
"ds_write_b128 v10, v[36:39] offset:29696\n"
"ds_write_b128 v10, v[40:43] offset:30720\n"
"ds_write_b128 v10, v[44:47] offset:31744\n"
"s_waitcnt lgkmcnt(0)\n"
"s_mov_b32 s95, 0x3f803f80\n"
"v_lshrrev_b32 v2, 2, v0\n"
"v_and_b32 v2, 0x7f, v2\n"
"s_mov_b32 s46, -1\n"
"s_mov_b32 s33, 4917504\n"
"s_mov_b32 s88, 0\n"
"Lwgo%=:\n"
"buffer_load_dwordx2 v[12:13], off, s[40:43], s33 sc1\n"
"s_add_u32 s88, s88, 1\n"
"s_cmp_gt_u32 s88, 131072\n"
"s_cbranch_scc1 Lwend%=\n"
"Lwgp%=:\n"
"s_waitcnt vmcnt(0)\n"
"v_cmp_ne_u32 vcc, s36, v12\n"
"s_cbranch_vccnz Lwgo%=\n"
"v_readfirstlane_b32 s99, v13\n"
"s_lshl_b32 s33, s99, 13\n"
"s_xor_b32 s99, s99, s37\n"
"s_xor_b32 s54, s54, s33\n"
"s_xor_b32 s55, s55, s33\n"
"s_bfe_u32 s48, s52, 0x10015\n"
"s_xor_b32 s48, s48, s99\n"
"s_lshl_b32 s48, s48, 13\n"
"s_add_u32 s48, s48, s52\n"
"s_add_u32 s52, s52, 524288\n"
"ds_read_b128 v[32:35], v10 offset:0\n"
"ds_read_b128 v[36:39], v10 offset:1024\n"
"ds_read_b128 v[40:43], v10 offset:2048\n"
"ds_read_b128 v[44:47], v10 offset:3072\n"
"ds_read_b128 v[48:51], v10 offset:4096\n"
"ds_read_b128 v[52:55], v10 offset:5120\n"
"ds_read_b128 v[56:59], v10 offset:6144\n"
"ds_read_b128 v[60:63], v10 offset:7168\n"
"v_mov_b64 v[16:17], 0\n"
"v_mov_b64 v[18:19], 0\n"
"v_mov_b64 v[20:21], 0\n"
"v_mov_b64 v[22:23], 0\n"
"v_mov_b64 v[24:25], 0\n"
"v_mov_b64 v[26:27], 0\n"
"v_mov_b64 v[28:29], 0\n"
"v_mov_b64 v[30:31], 0\n"
"s_branch Lwwait%=\n"
"Lwloop%=:\n"
"s_xor_b32 s89, s48, s45\n"
"s_and_b32 s89, s89, 0x2000\n"
"s_add_u32 s69, s67, s89\n"
"s_add_u32 s70, s76, s89\n"
"s_and_b32 s89, s89, s65\n"
"s_add_u32 s89, s89, s66\n"
"s_add_u32 s46, s46, 1\n"
"buffer_load_dword v15, v6, s[56:59], s89 offen sc1\n"
"s_add_u32 s66, s66, s64\n"
"s_bfe_u32 s48, s52, 0x10015\n"
"s_xor_b32 s48, s48, s99\n"
"s_lshl_b32 s48, s48, 13\n"
"s_add_u32 s48, s48, s52\n"
"s_add_u32 s52, s52, 524288\n"
"s_waitcnt lgkmcnt(0)\n"
"s_waitcnt vmcnt(16)\n"
"v_smfmac_f32_16x16x64_bf16 v[16:19], v[64:67], v[32:39], v9\n"
"v_smfmac_f32_16x16x64_bf16 v[20:23], v[64:67], v[40:47], v9\n"
"v_smfmac_f32_16x16x64_bf16 v[24:27], v[64:67], v[48:55], v9\n"
"v_smfmac_f32_16x16x64_bf16 v[28:31], v[64:67], v[56:63], v9\n"
"s_waitcnt vmcnt(15)\n"
"v_smfmac_f32_16x16x64_bf16 v[16:19], v[68:71], a[0:7], v9\n"
"ds_read_b128 v[32:35], v10 offset:8192\n"
"v_smfmac_f32_16x16x64_bf16 v[20:23], v[68:71], a[8:15], v9\n"
"ds_read_b128 v[36:39], v10 offset:9216\n"
"v_smfmac_f32_16x16x64_bf16 v[24:27], v[68:71], a[16:23], v9\n"
"ds_read_b128 v[40:43], v10 offset:10240\n"
"v_smfmac_f32_16x16x64_bf16 v[28:31], v[68:71], a[24:31], v9\n"
"ds_read_b128 v[44:47], v10 offset:11264\n"
"s_waitcnt vmcnt(14)\n"
"v_smfmac_f32_16x16x64_bf16 v[16:19], v[72:75], a[32:39], v9\n"
"ds_read_b128 v[48:51], v10 offset:12288\n"
"v_smfmac_f32_16x16x64_bf16 v[20:23], v[72:75], a[40:47], v9\n"
"ds_read_b128 v[52:55], v10 offset:13312\n"
"v_smfmac_f32_16x16x64_bf16 v[24:27], v[72:75], a[48:55], v9\n"
"ds_read_b128 v[56:59], v10 offset:14336\n"
"v_smfmac_f32_16x16x64_bf16 v[28:31], v[72:75], a[56:63], v9\n"
"ds_read_b128 v[60:63], v10 offset:15360\n"
"s_waitcnt vmcnt(13)\n"
"v_smfmac_f32_16x16x64_bf16 v[16:19], v[76:79], a[64:71], v9\n"
"v_smfmac_f32_16x16x64_bf16 v[20:23], v[76:79], a[72:79], v9\n"
"v_smfmac_f32_16x16x64_bf16 v[24:27], v[76:79], a[80:87], v9\n"
"v_smfmac_f32_16x16x64_bf16 v[28:31], v[76:79], a[88:95], v9\n"
"s_waitcnt vmcnt(12)\n"
"v_smfmac_f32_16x16x64_bf16 v[16:19], v[80:83], a[96:103], v9\n"
"v_smfmac_f32_16x16x64_bf16 v[20:23], v[80:83], a[104:111], v9\n"
"v_smfmac_f32_16x16x64_bf16 v[24:27], v[80:83], a[112:119], v9\n"
"v_smfmac_f32_16x16x64_bf16 v[28:31], v[80:83], a[120:127], v9\n"
"s_waitcnt vmcnt(11)\n"
"s_waitcnt lgkmcnt(6)\n"
"v_smfmac_f32_16x16x64_bf16 v[16:19], v[84:87], v[32:39], v9\n"
"s_waitcnt lgkmcnt(4)\n"
"v_smfmac_f32_16x16x64_bf16 v[20:23], v[84:87], v[40:47], v9\n"
"s_waitcnt lgkmcnt(2)\n"
"v_smfmac_f32_16x16x64_bf16 v[24:27], v[84:87], v[48:55], v9\n"
"s_waitcnt lgkmcnt(0)\n"
"v_smfmac_f32_16x16x64_bf16 v[28:31], v[84:87], v[56:63], v9\n"
"s_waitcnt vmcnt(10)\n"
"v_smfmac_f32_16x16x64_bf16 v[16:19], v[88:91], a[128:135], v9\n"
"ds_read_b128 v[32:35], v10 offset:16384\n"
"v_smfmac_f32_16x16x64_bf16 v[20:23], v[88:91], a[136:143], v9\n"
"ds_read_b128 v[36:39], v10 offset:17408\n"
"v_smfmac_f32_16x16x64_bf16 v[24:27], v[88:91], a[144:151], v9\n"
"ds_read_b128 v[40:43], v10 offset:18432\n"
"v_smfmac_f32_16x16x64_bf16 v[28:31], v[88:91], a[152:159], v9\n"
"ds_read_b128 v[44:47], v10 offset:19456\n"
"s_waitcnt vmcnt(9)\n"
"v_smfmac_f32_16x16x64_bf16 v[16:19], v[92:95], a[160:167], v9\n"
"ds_read_b128 v[48:51], v10 offset:20480\n"
"v_smfmac_f32_16x16x64_bf16 v[20:23], v[92:95], a[168:175], v9\n"
"ds_read_b128 v[52:55], v10 offset:21504\n"
"v_smfmac_f32_16x16x64_bf16 v[24:27], v[92:95], a[176:183], v9\n"
"ds_read_b128 v[56:59], v10 offset:22528\n"
"v_smfmac_f32_16x16x64_bf16 v[28:31], v[92:95], a[184:191], v9\n"
"ds_read_b128 v[60:63], v10 offset:23552\n"
"s_waitcnt vmcnt(8)\n"
"v_smfmac_f32_16x16x64_bf16 v[16:19], v[96:99], a[192:199], v9\n"
"v_smfmac_f32_16x16x64_bf16 v[20:23], v[96:99], a[200:207], v9\n"
"v_smfmac_f32_16x16x64_bf16 v[24:27], v[96:99], a[208:215], v9\n"
"v_smfmac_f32_16x16x64_bf16 v[28:31], v[96:99], a[216:223], v9\n"
"s_waitcnt vmcnt(7)\n"
"v_smfmac_f32_16x16x64_bf16 v[16:19], v[100:103], a[224:231], v9\n"
"v_smfmac_f32_16x16x64_bf16 v[20:23], v[100:103], a[232:239], v9\n"
"v_smfmac_f32_16x16x64_bf16 v[24:27], v[100:103], a[240:247], v9\n"
"v_smfmac_f32_16x16x64_bf16 v[28:31], v[100:103], a[248:255], v9\n"
"s_waitcnt vmcnt(6)\n"
"s_waitcnt lgkmcnt(6)\n"
"v_smfmac_f32_16x16x64_bf16 v[16:19], v[104:107], v[32:39], v9\n"
"s_waitcnt lgkmcnt(4)\n"
"v_smfmac_f32_16x16x64_bf16 v[20:23], v[104:107], v[40:47], v9\n"
"s_waitcnt lgkmcnt(2)\n"
"v_smfmac_f32_16x16x64_bf16 v[24:27], v[104:107], v[48:55], v9\n"
"s_waitcnt lgkmcnt(0)\n"
"v_smfmac_f32_16x16x64_bf16 v[28:31], v[104:107], v[56:63], v9\n"
"s_waitcnt vmcnt(5)\n"
"v_smfmac_f32_16x16x64_bf16 v[16:19], v[108:111], v[128:135], v9\n"
"ds_read_b128 v[32:35], v10 offset:24576\n"
"v_smfmac_f32_16x16x64_bf16 v[20:23], v[108:111], v[136:143], v9\n"
"ds_read_b128 v[36:39], v10 offset:25600\n"
"v_smfmac_f32_16x16x64_bf16 v[24:27], v[108:111], v[144:151], v9\n"
"ds_read_b128 v[40:43], v10 offset:26624\n"
"v_smfmac_f32_16x16x64_bf16 v[28:31], v[108:111], v[152:159], v9\n"
"ds_read_b128 v[44:47], v10 offset:27648\n"
"s_waitcnt vmcnt(4)\n"
"v_smfmac_f32_16x16x64_bf16 v[16:19], v[112:115], v[160:167], v9\n"
"ds_read_b128 v[48:51], v10 offset:28672\n"
"v_smfmac_f32_16x16x64_bf16 v[20:23], v[112:115], v[168:175], v9\n"
"ds_read_b128 v[52:55], v10 offset:29696\n"
"v_smfmac_f32_16x16x64_bf16 v[24:27], v[112:115], v[176:183], v9\n"
"ds_read_b128 v[56:59], v10 offset:30720\n"
"v_smfmac_f32_16x16x64_bf16 v[28:31], v[112:115], v[184:191], v9\n"
"ds_read_b128 v[60:63], v10 offset:31744\n"
"v_mov_b32 v11, 0x7fffffff\n"
"s_mov_b64 exec, 0xff\n"
"buffer_atomic_smin v11, v1, s[40:43], s54 offen sc0 sc1\n"
"s_mov_b64 exec, -1\n"
"s_waitcnt vmcnt(4)\n"
"v_smfmac_f32_16x16x64_bf16 v[16:19], v[116:119], v[192:199], v9\n"
"v_smfmac_f32_16x16x64_bf16 v[20:23], v[116:119], v[200:207], v9\n"
"v_smfmac_f32_16x16x64_bf16 v[24:27], v[116:119], v[208:215], v9\n"
"v_smfmac_f32_16x16x64_bf16 v[28:31], v[116:119], v[216:223], v9\n"
"s_waitcnt vmcnt(3)\n"
"s_waitcnt lgkmcnt(6)\n"
"v_smfmac_f32_16x16x64_bf16 v[16:19], v[120:123], v[32:39], v9\n"
"s_waitcnt lgkmcnt(4)\n"
"v_smfmac_f32_16x16x64_bf16 v[20:23], v[120:123], v[40:47], v9\n"
"s_waitcnt lgkmcnt(2)\n"
"v_smfmac_f32_16x16x64_bf16 v[24:27], v[120:123], v[48:55], v9\n"
"s_waitcnt lgkmcnt(0)\n"
"v_smfmac_f32_16x16x64_bf16 v[28:31], v[120:123], v[56:63], v9\n"
"s_waitcnt vmcnt(2)\n"
"v_smfmac_f32_16x16x64_bf16 v[16:19], v[124:127], v[224:231], v9\n"
"v_smfmac_f32_16x16x64_bf16 v[20:23], v[124:127], v[232:239], v9\n"
"v_smfmac_f32_16x16x64_bf16 v[24:27], v[124:127], v[240:247], v9\n"
"v_smfmac_f32_16x16x64_bf16 v[28:31], v[124:127], v[248:255], v9\n"
"s_waitcnt vmcnt(1)\n"
"v_lshlrev_b32 v40, 16, v15\n"
"v_and_b32 v41, 0xffff0000, v15\n"
"v_mov_b64 v[42:43], 0\n"
"v_mov_b32 v14, s46\n"
"v_permlane32_swap_b32 v16, v18\n"
"v_permlane32_swap_b32 v17, v19\n"
"v_pk_add_f32 v[16:17], v[16:17], v[18:19]\n"
"v_permlane32_swap_b32 v20, v22\n"
"v_permlane32_swap_b32 v21, v23\n"
"v_pk_add_f32 v[20:21], v[20:21], v[22:23]\n"
"v_permlane32_swap_b32 v24, v26\n"
"v_permlane32_swap_b32 v25, v27\n"
"v_pk_add_f32 v[24:25], v[24:25], v[26:27]\n"
"v_permlane32_swap_b32 v28, v30\n"
"v_permlane32_swap_b32 v29, v31\n"
"v_pk_add_f32 v[28:29], v[28:29], v[30:31]\n"
"ds_write2_b32 v4, v16, v17 offset0:0 offset1:72\n"
"ds_write2_b32 v4, v20, v21 offset0:16 offset1:88\n"
"ds_write2_b32 v4, v24, v25 offset0:32 offset1:104\n"
"ds_write2_b32 v4, v28, v29 offset0:48 offset1:120\n"
"s_waitcnt lgkmcnt(0)\n"
"s_barrier\n"
"ds_read_b64 v[32:33], v5 offset:0\n"
"ds_read_b64 v[34:35], v5 offset:2304\n"
"ds_read_b64 v[36:37], v5 offset:4608\n"
"ds_read_b64 v[38:39], v5 offset:6912\n"
"v_xor_b32 v4, 0x4000, v4\n"
"v_xor_b32 v5, 0x4000, v5\n"
"s_waitcnt lgkmcnt(2)\n"
"v_pk_add_f32 v[32:33], v[32:33], v[34:35]\n"
"s_waitcnt lgkmcnt(0)\n"
"v_pk_add_f32 v[36:37], v[36:37], v[38:39]\n"
"v_pk_add_f32 v[32:33], v[32:33], v[36:37]\n"
"v_pk_add_f32 v[32:33], v[32:33], v[40:41]\n"
"v_cvt_pk_bf16_f32 v12, v32, v33\n"
"v_dot2c_f32_bf16 v42, s95, v12\n"
"v_dot2c_f32_bf16 v43, v12, v12\n"
"buffer_store_dword v12, v6, s[60:63], s69 offen sc1\n"
"s_add_u32 s67, s67, 0x80000\n"
"s_nop 0\n"
"v_add_f32_dpp v42, v42, v42 row_shr:1 row_mask:0xf bank_mask:0xf bound_ctrl:0\n"
"v_add_f32_dpp v43, v43, v43 row_shr:1 row_mask:0xf bank_mask:0xf bound_ctrl:0\n"
"s_nop 0\n"
"v_add_f32_dpp v42, v42, v42 row_shr:2 row_mask:0xf bank_mask:0xf bound_ctrl:0\n"
"v_add_f32_dpp v43, v43, v43 row_shr:2 row_mask:0xf bank_mask:0xf bound_ctrl:0\n"
"s_nop 0\n"
"v_add_f32_dpp v42, v42, v42 row_shr:4 row_mask:0xf bank_mask:0xf bound_ctrl:0\n"
"v_add_f32_dpp v43, v43, v43 row_shr:4 row_mask:0xf bank_mask:0xf bound_ctrl:0\n"
"s_nop 0\n"
"v_add_f32_dpp v42, v42, v42 row_shr:8 row_mask:0xf bank_mask:0xf bound_ctrl:0\n"
"v_add_f32_dpp v43, v43, v43 row_shr:8 row_mask:0xf bank_mask:0xf bound_ctrl:0\n"
"s_nop 0\n"
"v_add_f32_dpp v42, v42, v42 row_bcast:15 row_mask:0xf bank_mask:0xf bound_ctrl:0\n"
"v_add_f32_dpp v43, v43, v43 row_bcast:15 row_mask:0xf bank_mask:0xf bound_ctrl:0\n"
"buffer_store_dwordx2 v[42:43], v7, s[40:43], s70 offen sc1\n"
"s_add_u32 s76, s76, 0x80000\n"
"s_waitcnt vmcnt(2)\n"
"v_cmp_ge_i32 vcc, s46, v11\n"
"s_cbranch_vccnz Lwslow%=\n"
"s_xor_b32 s89, s94, 0\n"
"s_add_u32 s89, s48, s89\n"
"buffer_load_dwordx4 v[64:67], v8, s[40:43], s89 offen offset:0 sc1\n"
"buffer_load_dwordx4 v[68:71], v8, s[40:43], s89 offen offset:2048 sc1\n"
"s_xor_b32 s89, s94, 16384\n"
"s_add_u32 s89, s48, s89\n"
"buffer_load_dwordx4 v[72:75], v8, s[40:43], s89 offen offset:0 sc1\n"
"buffer_load_dwordx4 v[76:79], v8, s[40:43], s89 offen offset:2048 sc1\n"
"s_xor_b32 s89, s94, 32768\n"
"s_add_u32 s89, s48, s89\n"
"buffer_load_dwordx4 v[80:83], v8, s[40:43], s89 offen offset:0 sc1\n"
"buffer_load_dwordx4 v[84:87], v8, s[40:43], s89 offen offset:2048 sc1\n"
"s_xor_b32 s89, s94, 49152\n"
"s_add_u32 s89, s48, s89\n"
"buffer_load_dwordx4 v[88:91], v8, s[40:43], s89 offen offset:0 sc1\n"
"buffer_load_dwordx4 v[92:95], v8, s[40:43], s89 offen offset:2048 sc1\n"
"s_xor_b32 s89, s94, 65536\n"
"s_add_u32 s89, s48, s89\n"
"buffer_load_dwordx4 v[96:99], v8, s[40:43], s89 offen offset:0 sc1\n"
"buffer_load_dwordx4 v[100:103], v8, s[40:43], s89 offen offset:2048 sc1\n"
"s_xor_b32 s89, s94, 81920\n"
"s_add_u32 s89, s48, s89\n"
"buffer_load_dwordx4 v[104:107], v8, s[40:43], s89 offen offset:0 sc1\n"
"buffer_load_dwordx4 v[108:111], v8, s[40:43], s89 offen offset:2048 sc1\n"
"s_xor_b32 s89, s94, 98304\n"
"s_add_u32 s89, s48, s89\n"
"buffer_load_dwordx4 v[112:115], v8, s[40:43], s89 offen offset:0 sc1\n"
"buffer_load_dwordx4 v[116:119], v8, s[40:43], s89 offen offset:2048 sc1\n"
"s_xor_b32 s89, s94, 114688\n"
"s_add_u32 s89, s48, s89\n"
"buffer_load_dwordx4 v[120:123], v8, s[40:43], s89 offen offset:0 sc1\n"
"buffer_load_dwordx4 v[124:127], v8, s[40:43], s89 offen offset:2048 sc1\n"
"ds_read_b128 v[32:35], v10 offset:0\n"
"ds_read_b128 v[36:39], v10 offset:1024\n"
"ds_read_b128 v[40:43], v10 offset:2048\n"
"ds_read_b128 v[44:47], v10 offset:3072\n"
"ds_read_b128 v[48:51], v10 offset:4096\n"
"ds_read_b128 v[52:55], v10 offset:5120\n"
"ds_read_b128 v[56:59], v10 offset:6144\n"
"ds_read_b128 v[60:63], v10 offset:7168\n"
"v_mov_b64 v[16:17], 0\n"
"v_mov_b64 v[18:19], 0\n"
"v_mov_b64 v[20:21], 0\n"
"v_mov_b64 v[22:23], 0\n"
"v_mov_b64 v[24:25], 0\n"
"v_mov_b64 v[26:27], 0\n"
"v_mov_b64 v[28:29], 0\n"
"v_mov_b64 v[30:31], 0\n"
"s_waitcnt vmcnt(16)\n"
"buffer_store_dword v14, v2, s[40:43], s55 offen sc1\n"
"s_branch Lwloop%=\n"
"Lwslow%=:\n"
"v_mov_b32 v11, 0x7fffffff\n"
"s_mov_b64 exec, 0xff\n"
"buffer_atomic_smin v11, v1, s[40:43], s54 offen sc0 sc1\n"
"s_mov_b64 exec, -1\n"
"ds_read_b128 v[32:35], v10 offset:0\n"
"ds_read_b128 v[36:39], v10 offset:1024\n"
"ds_read_b128 v[40:43], v10 offset:2048\n"
"ds_read_b128 v[44:47], v10 offset:3072\n"
"ds_read_b128 v[48:51], v10 offset:4096\n"
"ds_read_b128 v[52:55], v10 offset:5120\n"
"ds_read_b128 v[56:59], v10 offset:6144\n"
"ds_read_b128 v[60:63], v10 offset:7168\n"
"v_mov_b64 v[16:17], 0\n"
"v_mov_b64 v[18:19], 0\n"
"v_mov_b64 v[20:21], 0\n"
"v_mov_b64 v[22:23], 0\n"
"v_mov_b64 v[24:25], 0\n"
"v_mov_b64 v[26:27], 0\n"
"v_mov_b64 v[28:29], 0\n"
"v_mov_b64 v[30:31], 0\n"
"s_waitcnt vmcnt(1)\n"
"buffer_store_dword v14, v2, s[40:43], s55 offen sc1\n"
"s_cmp_eq_u32 s46, 63\n"
"s_cbranch_scc1 Lwend%=\n"
"s_waitcnt vmcnt(1)\n"
"v_cmp_ge_i32 vcc, s46, v11\n"
"s_cbranch_vccz Lwr%=\n"
"Lwwait%=:\n"
"s_mov_b32 s88, 0\n"
"Lwp%=:\n"
"v_mov_b32 v11, 0x7fffffff\n"
"s_mov_b64 exec, 0xff\n"
"buffer_atomic_smin v11, v1, s[40:43], s54 offen sc0 sc1\n"
"s_mov_b64 exec, -1\n"
"s_add_u32 s88, s88, 1\n"
"s_cmp_gt_u32 s88, 131072\n"
"s_cbranch_scc1 Lwend%=\n"
"s_waitcnt vmcnt(0)\n"
"v_cmp_ge_i32 vcc, s46, v11\n"
"s_cbranch_vccz Lwr%=\n"
"s_sleep 1\n"
"s_branch Lwp%=\n"
"Lwr%=:\n"
"s_xor_b32 s89, s94, 0\n"
"s_add_u32 s89, s48, s89\n"
"buffer_load_dwordx4 v[64:67], v8, s[40:43], s89 offen offset:0 sc1\n"
"buffer_load_dwordx4 v[68:71], v8, s[40:43], s89 offen offset:2048 sc1\n"
"s_xor_b32 s89, s94, 16384\n"
"s_add_u32 s89, s48, s89\n"
"buffer_load_dwordx4 v[72:75], v8, s[40:43], s89 offen offset:0 sc1\n"
"buffer_load_dwordx4 v[76:79], v8, s[40:43], s89 offen offset:2048 sc1\n"
"s_xor_b32 s89, s94, 32768\n"
"s_add_u32 s89, s48, s89\n"
"buffer_load_dwordx4 v[80:83], v8, s[40:43], s89 offen offset:0 sc1\n"
"buffer_load_dwordx4 v[84:87], v8, s[40:43], s89 offen offset:2048 sc1\n"
"s_xor_b32 s89, s94, 49152\n"
"s_add_u32 s89, s48, s89\n"
"buffer_load_dwordx4 v[88:91], v8, s[40:43], s89 offen offset:0 sc1\n"
"buffer_load_dwordx4 v[92:95], v8, s[40:43], s89 offen offset:2048 sc1\n"
"s_xor_b32 s89, s94, 65536\n"
"s_add_u32 s89, s48, s89\n"
"buffer_load_dwordx4 v[96:99], v8, s[40:43], s89 offen offset:0 sc1\n"
"buffer_load_dwordx4 v[100:103], v8, s[40:43], s89 offen offset:2048 sc1\n"
"s_xor_b32 s89, s94, 81920\n"
"s_add_u32 s89, s48, s89\n"
"buffer_load_dwordx4 v[104:107], v8, s[40:43], s89 offen offset:0 sc1\n"
"buffer_load_dwordx4 v[108:111], v8, s[40:43], s89 offen offset:2048 sc1\n"
"s_xor_b32 s89, s94, 98304\n"
"s_add_u32 s89, s48, s89\n"
"buffer_load_dwordx4 v[112:115], v8, s[40:43], s89 offen offset:0 sc1\n"
"buffer_load_dwordx4 v[116:119], v8, s[40:43], s89 offen offset:2048 sc1\n"
"s_xor_b32 s89, s94, 114688\n"
"s_add_u32 s89, s48, s89\n"
"buffer_load_dwordx4 v[120:123], v8, s[40:43], s89 offen offset:0 sc1\n"
"buffer_load_dwordx4 v[124:127], v8, s[40:43], s89 offen offset:2048 sc1\n"
"s_branch Lwloop%=\n"
"Lwend%=:\n"
"s_waitcnt vmcnt(0) lgkmcnt(0)\n"
"LKWO:\n"
      OPS);
    } else if(role==2){
      {
        VC[0]=lane*16; VC[1]=i32*1040+hh*16; VC[2]=0x9400+w*3072+lane*16; VC[5]=hh? 0x7f000000 : i32*FS; VC[14]=(i32>>4)*131072+(hh*16+c)*16;
        VC[6]=(4*w+g)*3584+j*256+((c^(g<<2))*16); VC[7]=(8*(g>>1)+q4)*256+((64*w+32*(g&1)+16*(p4&1)+8*(p4>>1))^(q4<<6));
        SA[9]=8*w*1040; SA[10]=phh+OFF_H+((4*j+w)/14)*32768+(((4*j+w)%14)>>3)*16384+(((4*j+w)%14)&7)*1024; SA[11]=OFF_FLAG+F_H+p*16384+SDH*8192+(4*j+w)*FS; SA[12]=w*1024; SA[7]=OFF_FLAG+F_X1+(p*2+(w>>1))*16384+((SD1>>(w>>1))&1)*8192+(w&1)*32*FS;
        { VC[4]=33280+(i32>>3)*1088+(i32&7)*8+hh*256; VC[12]=(lane<32)? ((lane>>1)&1)*256+(lane>>2)*32+(lane&1)*16 : 0x7f000000; SA[14]=33280+w*1088; SA[15]=0x2000;
          SB[6]=pv+OFF_V+w*16384; SB[7]=pv+OFF_V+8*16384+w*512; SB[8]=0x80000; SB[9]=0x80000; SB[10]=4096; SB[11]=0x2000; }
        SB[4]=0; SB[5]=~0u;
        setrs(SC,0,Wg,512*1792*2); setrs(SC,4,Wu,512*1792*2);
        asm volatile(
"s_waitcnt lgkmcnt(0)\n"
"s_nop 4\n"
"v_mov_b32 v56, 0x3f803f80\n"
"v_mov_b32 v57, 0x3f803f80\n"
"v_mov_b32 v58, 0x3f803f80\n"
"v_mov_b32 v59, 0x3f803f80\n"
"s_add_u32 m0, s52, 0\n"
"s_mov_b32 s89, 0x0\n"
"buffer_load_dwordx4 v6, s[72:75], s89 offen lds\n"
"s_add_u32 m0, s52, 4096\n"
"s_mov_b32 s89, 0xe000\n"
"buffer_load_dwordx4 v6, s[72:75], s89 offen lds\n"
"s_add_u32 m0, s52, 8192\n"
"s_mov_b32 s89, 0x1c000\n"
"buffer_load_dwordx4 v6, s[72:75], s89 offen lds\n"
"s_add_u32 m0, s52, 12288\n"
"s_mov_b32 s89, 0x2a000\n"
"buffer_load_dwordx4 v6, s[72:75], s89 offen lds\n"
"s_add_u32 m0, s52, 16384\n"
"s_mov_b32 s89, 0x0\n"
"buffer_load_dwordx4 v6, s[76:79], s89 offen lds\n"
"s_add_u32 m0, s52, 20480\n"
"s_mov_b32 s89, 0xe000\n"
"buffer_load_dwordx4 v6, s[76:79], s89 offen lds\n"
"s_add_u32 m0, s52, 24576\n"
"s_mov_b32 s89, 0x1c000\n"
"buffer_load_dwordx4 v6, s[76:79], s89 offen lds\n"
"s_add_u32 m0, s52, 28672\n"
"s_mov_b32 s89, 0x2a000\n"
"buffer_load_dwordx4 v6, s[76:79], s89 offen lds\n"
"s_add_u32 m0, s52, 32768\n"
"s_mov_b32 s89, 0x38000\n"
"buffer_load_dwordx4 v6, s[72:75], s89 offen lds\n"
"s_add_u32 m0, s52, 36864\n"
"s_mov_b32 s89, 0x46000\n"
"buffer_load_dwordx4 v6, s[72:75], s89 offen lds\n"
"s_add_u32 m0, s52, 40960\n"
"s_mov_b32 s89, 0x54000\n"
"buffer_load_dwordx4 v6, s[72:75], s89 offen lds\n"
"s_add_u32 m0, s52, 45056\n"
"s_mov_b32 s89, 0x62000\n"
"buffer_load_dwordx4 v6, s[72:75], s89 offen lds\n"
"s_add_u32 m0, s52, 49152\n"
"s_mov_b32 s89, 0x38000\n"
"buffer_load_dwordx4 v6, s[76:79], s89 offen lds\n"
"s_add_u32 m0, s52, 53248\n"
"s_mov_b32 s89, 0x46000\n"
"buffer_load_dwordx4 v6, s[76:79], s89 offen lds\n"
"s_add_u32 m0, s52, 57344\n"
"s_mov_b32 s89, 0x54000\n"
"buffer_load_dwordx4 v6, s[76:79], s89 offen lds\n"
"s_add_u32 m0, s52, 61440\n"
"s_mov_b32 s89, 0x62000\n"
"buffer_load_dwordx4 v6, s[76:79], s89 offen lds\n"
"s_add_u32 m0, s52, 65536\n"
"s_mov_b32 s89, 0x70000\n"
"buffer_load_dwordx4 v6, s[72:75], s89 offen lds\n"
"s_add_u32 m0, s52, 69632\n"
"s_mov_b32 s89, 0x7e000\n"
"buffer_load_dwordx4 v6, s[72:75], s89 offen lds\n"
"s_add_u32 m0, s52, 73728\n"
"s_mov_b32 s89, 0x8c000\n"
"buffer_load_dwordx4 v6, s[72:75], s89 offen lds\n"
"s_add_u32 m0, s52, 77824\n"
"s_mov_b32 s89, 0x9a000\n"
"buffer_load_dwordx4 v6, s[72:75], s89 offen lds\n"
"s_add_u32 m0, s52, 81920\n"
"s_mov_b32 s89, 0x70000\n"
"buffer_load_dwordx4 v6, s[76:79], s89 offen lds\n"
"s_add_u32 m0, s52, 86016\n"
"s_mov_b32 s89, 0x7e000\n"
"buffer_load_dwordx4 v6, s[76:79], s89 offen lds\n"
"s_add_u32 m0, s52, 90112\n"
"s_mov_b32 s89, 0x8c000\n"
"buffer_load_dwordx4 v6, s[76:79], s89 offen lds\n"
"s_add_u32 m0, s52, 94208\n"
"s_mov_b32 s89, 0x9a000\n"
"buffer_load_dwordx4 v6, s[76:79], s89 offen lds\n"
"s_add_u32 m0, s52, 98304\n"
"s_mov_b32 s89, 0xa8000\n"
"buffer_load_dwordx4 v6, s[72:75], s89 offen lds\n"
"s_add_u32 m0, s52, 102400\n"
"s_mov_b32 s89, 0xb6000\n"
"buffer_load_dwordx4 v6, s[72:75], s89 offen lds\n"
"s_add_u32 m0, s52, 106496\n"
"s_mov_b32 s89, 0xc4000\n"
"buffer_load_dwordx4 v6, s[72:75], s89 offen lds\n"
"s_add_u32 m0, s52, 110592\n"
"s_mov_b32 s89, 0xd2000\n"
"buffer_load_dwordx4 v6, s[72:75], s89 offen lds\n"
"s_add_u32 m0, s52, 114688\n"
"s_mov_b32 s89, 0xa8000\n"
"buffer_load_dwordx4 v6, s[76:79], s89 offen lds\n"
"s_add_u32 m0, s52, 118784\n"
"s_mov_b32 s89, 0xb6000\n"
"buffer_load_dwordx4 v6, s[76:79], s89 offen lds\n"
"s_add_u32 m0, s52, 122880\n"
"s_mov_b32 s89, 0xc4000\n"
"buffer_load_dwordx4 v6, s[76:79], s89 offen lds\n"
"s_add_u32 m0, s52, 126976\n"
"s_mov_b32 s89, 0xd2000\n"
"buffer_load_dwordx4 v6, s[76:79], s89 offen lds\n"
"s_waitcnt vmcnt(24)\n"
"s_barrier\n"
"v_add_u32 v68, 0x0, v7\n"
"ds_read_b64_tr_b16 a[0:1], v68 offset:0\n"
"ds_read_b64_tr_b16 a[2:3], v68 offset:1024\n"
"ds_read_b64_tr_b16 a[4:5], v68 offset:4096\n"
"ds_read_b64_tr_b16 a[6:7], v68 offset:5120\n"
"ds_read_b64_tr_b16 a[8:9], v68 offset:8192\n"
"ds_read_b64_tr_b16 a[10:11], v68 offset:9216\n"
"ds_read_b64_tr_b16 a[12:13], v68 offset:12288\n"
"ds_read_b64_tr_b16 a[14:15], v68 offset:13312\n"
"s_waitcnt lgkmcnt(6)\n"
"v_add_u32 v68, 0x4000, v7\n"
"ds_read_b64_tr_b16 a[128:129], v68 offset:0\n"
"ds_read_b64_tr_b16 a[130:131], v68 offset:1024\n"
"ds_read_b64_tr_b16 a[132:133], v68 offset:4096\n"
"ds_read_b64_tr_b16 a[134:135], v68 offset:5120\n"
"ds_read_b64_tr_b16 a[136:137], v68 offset:8192\n"
"ds_read_b64_tr_b16 a[138:139], v68 offset:9216\n"
"ds_read_b64_tr_b16 a[140:141], v68 offset:12288\n"
"ds_read_b64_tr_b16 a[142:143], v68 offset:13312\n"
"s_waitcnt lgkmcnt(0)\n"
"s_barrier\n"
"s_add_u32 m0, s52, 0\n"
"s_mov_b32 s89, 0xe0000\n"
"buffer_load_dwordx4 v6, s[72:75], s89 offen lds\n"
"s_add_u32 m0, s52, 4096\n"
"s_mov_b32 s89, 0xee000\n"
"buffer_load_dwordx4 v6, s[72:75], s89 offen lds\n"
"s_add_u32 m0, s52, 8192\n"
"s_mov_b32 s89, 0xfc000\n"
"buffer_load_dwordx4 v6, s[72:75], s89 offen lds\n"
"s_add_u32 m0, s52, 12288\n"
"s_mov_b32 s89, 0x10a000\n"
"buffer_load_dwordx4 v6, s[72:75], s89 offen lds\n"
"s_add_u32 m0, s52, 16384\n"
"s_mov_b32 s89, 0xe0000\n"
"buffer_load_dwordx4 v6, s[76:79], s89 offen lds\n"
"s_add_u32 m0, s52, 20480\n"
"s_mov_b32 s89, 0xee000\n"
"buffer_load_dwordx4 v6, s[76:79], s89 offen lds\n"
"s_add_u32 m0, s52, 24576\n"
"s_mov_b32 s89, 0xfc000\n"
"buffer_load_dwordx4 v6, s[76:79], s89 offen lds\n"
"s_add_u32 m0, s52, 28672\n"
"s_mov_b32 s89, 0x10a000\n"
"buffer_load_dwordx4 v6, s[76:79], s89 offen lds\n"
"v_mfma_f32_32x32x16_bf16 v[16:31], v[56:59], a[0:3], 0\n"
"v_mfma_f32_32x32x16_bf16 v[16:31], v[56:59], a[4:7], v[16:31]\n"
"v_mfma_f32_32x32x16_bf16 v[16:31], v[56:59], a[8:11], v[16:31]\n"
"v_mfma_f32_32x32x16_bf16 v[16:31], v[56:59], a[12:15], v[16:31]\n"
"v_mfma_f32_32x32x16_bf16 v[32:47], v[56:59], a[128:131], 0\n"
"v_mfma_f32_32x32x16_bf16 v[32:47], v[56:59], a[132:135], v[32:47]\n"
"v_mfma_f32_32x32x16_bf16 v[32:47], v[56:59], a[136:139], v[32:47]\n"
"v_mfma_f32_32x32x16_bf16 v[32:47], v[56:59], a[140:143], v[32:47]\n"
"s_waitcnt vmcnt(24)\n"
"s_barrier\n"
"v_add_u32 v68, 0x8000, v7\n"
"ds_read_b64_tr_b16 a[16:17], v68 offset:0\n"
"ds_read_b64_tr_b16 a[18:19], v68 offset:1024\n"
"ds_read_b64_tr_b16 a[20:21], v68 offset:4096\n"
"ds_read_b64_tr_b16 a[22:23], v68 offset:5120\n"
"ds_read_b64_tr_b16 a[24:25], v68 offset:8192\n"
"ds_read_b64_tr_b16 a[26:27], v68 offset:9216\n"
"ds_read_b64_tr_b16 a[28:29], v68 offset:12288\n"
"ds_read_b64_tr_b16 a[30:31], v68 offset:13312\n"
"s_waitcnt lgkmcnt(6)\n"
"v_add_u32 v68, 0xc000, v7\n"
"ds_read_b64_tr_b16 a[144:145], v68 offset:0\n"
"ds_read_b64_tr_b16 a[146:147], v68 offset:1024\n"
"ds_read_b64_tr_b16 a[148:149], v68 offset:4096\n"
"ds_read_b64_tr_b16 a[150:151], v68 offset:5120\n"
"ds_read_b64_tr_b16 a[152:153], v68 offset:8192\n"
"ds_read_b64_tr_b16 a[154:155], v68 offset:9216\n"
"ds_read_b64_tr_b16 a[156:157], v68 offset:12288\n"
"ds_read_b64_tr_b16 a[158:159], v68 offset:13312\n"
"s_waitcnt lgkmcnt(0)\n"
"s_barrier\n"
"s_add_u32 m0, s52, 32768\n"
"s_mov_b32 s89, 0x118000\n"
"buffer_load_dwordx4 v6, s[72:75], s89 offen lds\n"
"s_add_u32 m0, s52, 36864\n"
"s_mov_b32 s89, 0x126000\n"
"buffer_load_dwordx4 v6, s[72:75], s89 offen lds\n"
"s_add_u32 m0, s52, 40960\n"
"s_mov_b32 s89, 0x134000\n"
"buffer_load_dwordx4 v6, s[72:75], s89 offen lds\n"
"s_add_u32 m0, s52, 45056\n"
"s_mov_b32 s89, 0x142000\n"
"buffer_load_dwordx4 v6, s[72:75], s89 offen lds\n"
"s_add_u32 m0, s52, 49152\n"
"s_mov_b32 s89, 0x118000\n"
"buffer_load_dwordx4 v6, s[76:79], s89 offen lds\n"
"s_add_u32 m0, s52, 53248\n"
"s_mov_b32 s89, 0x126000\n"
"buffer_load_dwordx4 v6, s[76:79], s89 offen lds\n"
"s_add_u32 m0, s52, 57344\n"
"s_mov_b32 s89, 0x134000\n"
"buffer_load_dwordx4 v6, s[76:79], s89 offen lds\n"
"s_add_u32 m0, s52, 61440\n"
"s_mov_b32 s89, 0x142000\n"
"buffer_load_dwordx4 v6, s[76:79], s89 offen lds\n"
"v_mfma_f32_32x32x16_bf16 v[16:31], v[56:59], a[16:19], v[16:31]\n"
"v_mfma_f32_32x32x16_bf16 v[16:31], v[56:59], a[20:23], v[16:31]\n"
"v_mfma_f32_32x32x16_bf16 v[16:31], v[56:59], a[24:27], v[16:31]\n"
"v_mfma_f32_32x32x16_bf16 v[16:31], v[56:59], a[28:31], v[16:31]\n"
"v_mfma_f32_32x32x16_bf16 v[32:47], v[56:59], a[144:147], v[32:47]\n"
"v_mfma_f32_32x32x16_bf16 v[32:47], v[56:59], a[148:151], v[32:47]\n"
"v_mfma_f32_32x32x16_bf16 v[32:47], v[56:59], a[152:155], v[32:47]\n"
"v_mfma_f32_32x32x16_bf16 v[32:47], v[56:59], a[156:159], v[32:47]\n"
"s_waitcnt vmcnt(24)\n"
"s_barrier\n"
"v_add_u32 v68, 0x10000, v7\n"
"ds_read_b64_tr_b16 a[32:33], v68 offset:0\n"
"ds_read_b64_tr_b16 a[34:35], v68 offset:1024\n"
"ds_read_b64_tr_b16 a[36:37], v68 offset:4096\n"
"ds_read_b64_tr_b16 a[38:39], v68 offset:5120\n"
"ds_read_b64_tr_b16 a[40:41], v68 offset:8192\n"
"ds_read_b64_tr_b16 a[42:43], v68 offset:9216\n"
"ds_read_b64_tr_b16 a[44:45], v68 offset:12288\n"
"ds_read_b64_tr_b16 a[46:47], v68 offset:13312\n"
"s_waitcnt lgkmcnt(6)\n"
"v_add_u32 v68, 0x14000, v7\n"
"ds_read_b64_tr_b16 a[160:161], v68 offset:0\n"
"ds_read_b64_tr_b16 a[162:163], v68 offset:1024\n"
"ds_read_b64_tr_b16 a[164:165], v68 offset:4096\n"
"ds_read_b64_tr_b16 a[166:167], v68 offset:5120\n"
"ds_read_b64_tr_b16 a[168:169], v68 offset:8192\n"
"ds_read_b64_tr_b16 a[170:171], v68 offset:9216\n"
"ds_read_b64_tr_b16 a[172:173], v68 offset:12288\n"
"ds_read_b64_tr_b16 a[174:175], v68 offset:13312\n"
"s_waitcnt lgkmcnt(0)\n"
"s_barrier\n"
"s_add_u32 m0, s52, 65536\n"
"s_mov_b32 s89, 0x150000\n"
"buffer_load_dwordx4 v6, s[72:75], s89 offen lds\n"
"s_add_u32 m0, s52, 69632\n"
"s_mov_b32 s89, 0x15e000\n"
"buffer_load_dwordx4 v6, s[72:75], s89 offen lds\n"
"s_add_u32 m0, s52, 73728\n"
"s_mov_b32 s89, 0x16c000\n"
"buffer_load_dwordx4 v6, s[72:75], s89 offen lds\n"
"s_add_u32 m0, s52, 77824\n"
"s_mov_b32 s89, 0x17a000\n"
"buffer_load_dwordx4 v6, s[72:75], s89 offen lds\n"
"s_add_u32 m0, s52, 81920\n"
"s_mov_b32 s89, 0x150000\n"
"buffer_load_dwordx4 v6, s[76:79], s89 offen lds\n"
"s_add_u32 m0, s52, 86016\n"
"s_mov_b32 s89, 0x15e000\n"
"buffer_load_dwordx4 v6, s[76:79], s89 offen lds\n"
"s_add_u32 m0, s52, 90112\n"
"s_mov_b32 s89, 0x16c000\n"
"buffer_load_dwordx4 v6, s[76:79], s89 offen lds\n"
"s_add_u32 m0, s52, 94208\n"
"s_mov_b32 s89, 0x17a000\n"
"buffer_load_dwordx4 v6, s[76:79], s89 offen lds\n"
"v_mfma_f32_32x32x16_bf16 v[16:31], v[56:59], a[32:35], v[16:31]\n"
"v_mfma_f32_32x32x16_bf16 v[16:31], v[56:59], a[36:39], v[16:31]\n"
"v_mfma_f32_32x32x16_bf16 v[16:31], v[56:59], a[40:43], v[16:31]\n"
"v_mfma_f32_32x32x16_bf16 v[16:31], v[56:59], a[44:47], v[16:31]\n"
"v_mfma_f32_32x32x16_bf16 v[32:47], v[56:59], a[160:163], v[32:47]\n"
"v_mfma_f32_32x32x16_bf16 v[32:47], v[56:59], a[164:167], v[32:47]\n"
"v_mfma_f32_32x32x16_bf16 v[32:47], v[56:59], a[168:171], v[32:47]\n"
"v_mfma_f32_32x32x16_bf16 v[32:47], v[56:59], a[172:175], v[32:47]\n"
"s_waitcnt vmcnt(24)\n"
"s_barrier\n"
"v_add_u32 v68, 0x18000, v7\n"
"ds_read_b64_tr_b16 a[48:49], v68 offset:0\n"
"ds_read_b64_tr_b16 a[50:51], v68 offset:1024\n"
"ds_read_b64_tr_b16 a[52:53], v68 offset:4096\n"
"ds_read_b64_tr_b16 a[54:55], v68 offset:5120\n"
"ds_read_b64_tr_b16 a[56:57], v68 offset:8192\n"
"ds_read_b64_tr_b16 a[58:59], v68 offset:9216\n"
"ds_read_b64_tr_b16 a[60:61], v68 offset:12288\n"
"ds_read_b64_tr_b16 a[62:63], v68 offset:13312\n"
"s_waitcnt lgkmcnt(6)\n"
"v_add_u32 v68, 0x1c000, v7\n"
"ds_read_b64_tr_b16 a[176:177], v68 offset:0\n"
"ds_read_b64_tr_b16 a[178:179], v68 offset:1024\n"
"ds_read_b64_tr_b16 a[180:181], v68 offset:4096\n"
"ds_read_b64_tr_b16 a[182:183], v68 offset:5120\n"
"ds_read_b64_tr_b16 a[184:185], v68 offset:8192\n"
"ds_read_b64_tr_b16 a[186:187], v68 offset:9216\n"
"ds_read_b64_tr_b16 a[188:189], v68 offset:12288\n"
"ds_read_b64_tr_b16 a[190:191], v68 offset:13312\n"
"s_waitcnt lgkmcnt(0)\n"
"s_barrier\n"
"s_add_u32 m0, s52, 98304\n"
"s_mov_b32 s89, 0x188000\n"
"buffer_load_dwordx4 v6, s[72:75], s89 offen lds\n"
"s_add_u32 m0, s52, 102400\n"
"s_mov_b32 s89, 0x196000\n"
"buffer_load_dwordx4 v6, s[72:75], s89 offen lds\n"
"s_add_u32 m0, s52, 106496\n"
"s_mov_b32 s89, 0x1a4000\n"
"buffer_load_dwordx4 v6, s[72:75], s89 offen lds\n"
"s_add_u32 m0, s52, 110592\n"
"s_mov_b32 s89, 0x1b2000\n"
"buffer_load_dwordx4 v6, s[72:75], s89 offen lds\n"
"s_add_u32 m0, s52, 114688\n"
"s_mov_b32 s89, 0x188000\n"
"buffer_load_dwordx4 v6, s[76:79], s89 offen lds\n"
"s_add_u32 m0, s52, 118784\n"
"s_mov_b32 s89, 0x196000\n"
"buffer_load_dwordx4 v6, s[76:79], s89 offen lds\n"
"s_add_u32 m0, s52, 122880\n"
"s_mov_b32 s89, 0x1a4000\n"
"buffer_load_dwordx4 v6, s[76:79], s89 offen lds\n"
"s_add_u32 m0, s52, 126976\n"
"s_mov_b32 s89, 0x1b2000\n"
"buffer_load_dwordx4 v6, s[76:79], s89 offen lds\n"
"v_mfma_f32_32x32x16_bf16 v[16:31], v[56:59], a[48:51], v[16:31]\n"
"v_mfma_f32_32x32x16_bf16 v[16:31], v[56:59], a[52:55], v[16:31]\n"
"v_mfma_f32_32x32x16_bf16 v[16:31], v[56:59], a[56:59], v[16:31]\n"
"v_mfma_f32_32x32x16_bf16 v[16:31], v[56:59], a[60:63], v[16:31]\n"
"v_mfma_f32_32x32x16_bf16 v[32:47], v[56:59], a[176:179], v[32:47]\n"
"v_mfma_f32_32x32x16_bf16 v[32:47], v[56:59], a[180:183], v[32:47]\n"
"v_mfma_f32_32x32x16_bf16 v[32:47], v[56:59], a[184:187], v[32:47]\n"
"v_mfma_f32_32x32x16_bf16 v[32:47], v[56:59], a[188:191], v[32:47]\n"
"s_waitcnt vmcnt(24)\n"
"s_barrier\n"
"v_add_u32 v68, 0x0, v7\n"
"ds_read_b64_tr_b16 a[64:65], v68 offset:0\n"
"ds_read_b64_tr_b16 a[66:67], v68 offset:1024\n"
"ds_read_b64_tr_b16 a[68:69], v68 offset:4096\n"
"ds_read_b64_tr_b16 a[70:71], v68 offset:5120\n"
"ds_read_b64_tr_b16 a[72:73], v68 offset:8192\n"
"ds_read_b64_tr_b16 a[74:75], v68 offset:9216\n"
"ds_read_b64_tr_b16 a[76:77], v68 offset:12288\n"
"ds_read_b64_tr_b16 a[78:79], v68 offset:13312\n"
"s_waitcnt lgkmcnt(6)\n"
"v_add_u32 v68, 0x4000, v7\n"
"ds_read_b64_tr_b16 a[192:193], v68 offset:0\n"
"ds_read_b64_tr_b16 a[194:195], v68 offset:1024\n"
"ds_read_b64_tr_b16 a[196:197], v68 offset:4096\n"
"ds_read_b64_tr_b16 a[198:199], v68 offset:5120\n"
"ds_read_b64_tr_b16 a[200:201], v68 offset:8192\n"
"ds_read_b64_tr_b16 a[202:203], v68 offset:9216\n"
"ds_read_b64_tr_b16 a[204:205], v68 offset:12288\n"
"ds_read_b64_tr_b16 a[206:207], v68 offset:13312\n"
"s_waitcnt lgkmcnt(0)\n"
"s_barrier\n"
"v_mfma_f32_32x32x16_bf16 v[16:31], v[56:59], a[64:67], v[16:31]\n"
"v_mfma_f32_32x32x16_bf16 v[16:31], v[56:59], a[68:71], v[16:31]\n"
"v_mfma_f32_32x32x16_bf16 v[16:31], v[56:59], a[72:75], v[16:31]\n"
"v_mfma_f32_32x32x16_bf16 v[16:31], v[56:59], a[76:79], v[16:31]\n"
"v_mfma_f32_32x32x16_bf16 v[32:47], v[56:59], a[192:195], v[32:47]\n"
"v_mfma_f32_32x32x16_bf16 v[32:47], v[56:59], a[196:199], v[32:47]\n"
"v_mfma_f32_32x32x16_bf16 v[32:47], v[56:59], a[200:203], v[32:47]\n"
"v_mfma_f32_32x32x16_bf16 v[32:47], v[56:59], a[204:207], v[32:47]\n"
"s_waitcnt vmcnt(16)\n"
"s_barrier\n"
"v_add_u32 v68, 0x8000, v7\n"
"ds_read_b64_tr_b16 a[80:81], v68 offset:0\n"
"ds_read_b64_tr_b16 a[82:83], v68 offset:1024\n"
"ds_read_b64_tr_b16 a[84:85], v68 offset:4096\n"
"ds_read_b64_tr_b16 a[86:87], v68 offset:5120\n"
"ds_read_b64_tr_b16 a[88:89], v68 offset:8192\n"
"ds_read_b64_tr_b16 a[90:91], v68 offset:9216\n"
"ds_read_b64_tr_b16 a[92:93], v68 offset:12288\n"
"ds_read_b64_tr_b16 a[94:95], v68 offset:13312\n"
"s_waitcnt lgkmcnt(6)\n"
"v_add_u32 v68, 0xc000, v7\n"
"ds_read_b64_tr_b16 a[208:209], v68 offset:0\n"
"ds_read_b64_tr_b16 a[210:211], v68 offset:1024\n"
"ds_read_b64_tr_b16 a[212:213], v68 offset:4096\n"
"ds_read_b64_tr_b16 a[214:215], v68 offset:5120\n"
"ds_read_b64_tr_b16 a[216:217], v68 offset:8192\n"
"ds_read_b64_tr_b16 a[218:219], v68 offset:9216\n"
"ds_read_b64_tr_b16 a[220:221], v68 offset:12288\n"
"ds_read_b64_tr_b16 a[222:223], v68 offset:13312\n"
"s_waitcnt lgkmcnt(0)\n"
"s_barrier\n"
"v_mfma_f32_32x32x16_bf16 v[16:31], v[56:59], a[80:83], v[16:31]\n"
"v_mfma_f32_32x32x16_bf16 v[16:31], v[56:59], a[84:87], v[16:31]\n"
"v_mfma_f32_32x32x16_bf16 v[16:31], v[56:59], a[88:91], v[16:31]\n"
"v_mfma_f32_32x32x16_bf16 v[16:31], v[56:59], a[92:95], v[16:31]\n"
"v_mfma_f32_32x32x16_bf16 v[32:47], v[56:59], a[208:211], v[32:47]\n"
"v_mfma_f32_32x32x16_bf16 v[32:47], v[56:59], a[212:215], v[32:47]\n"
"v_mfma_f32_32x32x16_bf16 v[32:47], v[56:59], a[216:219], v[32:47]\n"
"v_mfma_f32_32x32x16_bf16 v[32:47], v[56:59], a[220:223], v[32:47]\n"
"s_waitcnt vmcnt(8)\n"
"s_barrier\n"
"v_add_u32 v68, 0x10000, v7\n"
"ds_read_b64_tr_b16 a[96:97], v68 offset:0\n"
"ds_read_b64_tr_b16 a[98:99], v68 offset:1024\n"
"ds_read_b64_tr_b16 a[100:101], v68 offset:4096\n"
"ds_read_b64_tr_b16 a[102:103], v68 offset:5120\n"
"ds_read_b64_tr_b16 a[104:105], v68 offset:8192\n"
"ds_read_b64_tr_b16 a[106:107], v68 offset:9216\n"
"ds_read_b64_tr_b16 a[108:109], v68 offset:12288\n"
"ds_read_b64_tr_b16 a[110:111], v68 offset:13312\n"
"s_waitcnt lgkmcnt(6)\n"
"v_add_u32 v68, 0x14000, v7\n"
"ds_read_b64_tr_b16 a[224:225], v68 offset:0\n"
"ds_read_b64_tr_b16 a[226:227], v68 offset:1024\n"
"ds_read_b64_tr_b16 a[228:229], v68 offset:4096\n"
"ds_read_b64_tr_b16 a[230:231], v68 offset:5120\n"
"ds_read_b64_tr_b16 a[232:233], v68 offset:8192\n"
"ds_read_b64_tr_b16 a[234:235], v68 offset:9216\n"
"ds_read_b64_tr_b16 a[236:237], v68 offset:12288\n"
"ds_read_b64_tr_b16 a[238:239], v68 offset:13312\n"
"s_waitcnt lgkmcnt(0)\n"
"s_barrier\n"
"v_mfma_f32_32x32x16_bf16 v[16:31], v[56:59], a[96:99], v[16:31]\n"
"v_mfma_f32_32x32x16_bf16 v[16:31], v[56:59], a[100:103], v[16:31]\n"
"v_mfma_f32_32x32x16_bf16 v[16:31], v[56:59], a[104:107], v[16:31]\n"
"v_mfma_f32_32x32x16_bf16 v[16:31], v[56:59], a[108:111], v[16:31]\n"
"v_mfma_f32_32x32x16_bf16 v[32:47], v[56:59], a[224:227], v[32:47]\n"
"v_mfma_f32_32x32x16_bf16 v[32:47], v[56:59], a[228:231], v[32:47]\n"
"v_mfma_f32_32x32x16_bf16 v[32:47], v[56:59], a[232:235], v[32:47]\n"
"v_mfma_f32_32x32x16_bf16 v[32:47], v[56:59], a[236:239], v[32:47]\n"
"s_waitcnt vmcnt(0)\n"
"s_barrier\n"
"v_add_u32 v68, 0x18000, v7\n"
"ds_read_b64_tr_b16 a[112:113], v68 offset:0\n"
"ds_read_b64_tr_b16 a[114:115], v68 offset:1024\n"
"ds_read_b64_tr_b16 a[116:117], v68 offset:4096\n"
"ds_read_b64_tr_b16 a[118:119], v68 offset:5120\n"
"ds_read_b64_tr_b16 a[120:121], v68 offset:8192\n"
"ds_read_b64_tr_b16 a[122:123], v68 offset:9216\n"
"ds_read_b64_tr_b16 a[124:125], v68 offset:12288\n"
"ds_read_b64_tr_b16 a[126:127], v68 offset:13312\n"
"s_waitcnt lgkmcnt(6)\n"
"v_add_u32 v68, 0x1c000, v7\n"
"ds_read_b64_tr_b16 a[240:241], v68 offset:0\n"
"ds_read_b64_tr_b16 a[242:243], v68 offset:1024\n"
"ds_read_b64_tr_b16 a[244:245], v68 offset:4096\n"
"ds_read_b64_tr_b16 a[246:247], v68 offset:5120\n"
"ds_read_b64_tr_b16 a[248:249], v68 offset:8192\n"
"ds_read_b64_tr_b16 a[250:251], v68 offset:9216\n"
"ds_read_b64_tr_b16 a[252:253], v68 offset:12288\n"
"ds_read_b64_tr_b16 a[254:255], v68 offset:13312\n"
"s_waitcnt lgkmcnt(0)\n"
"s_barrier\n"
"v_mfma_f32_32x32x16_bf16 v[16:31], v[56:59], a[112:115], v[16:31]\n"
"v_mfma_f32_32x32x16_bf16 v[16:31], v[56:59], a[116:119], v[16:31]\n"
"v_mfma_f32_32x32x16_bf16 v[16:31], v[56:59], a[120:123], v[16:31]\n"
"v_mfma_f32_32x32x16_bf16 v[16:31], v[56:59], a[124:127], v[16:31]\n"
"v_mfma_f32_32x32x16_bf16 v[32:47], v[56:59], a[240:243], v[32:47]\n"
"v_mfma_f32_32x32x16_bf16 v[32:47], v[56:59], a[244:247], v[32:47]\n"
"v_mfma_f32_32x32x16_bf16 v[32:47], v[56:59], a[248:251], v[32:47]\n"
"v_mfma_f32_32x32x16_bf16 v[32:47], v[56:59], a[252:255], v[32:47]\n"
"s_nop 7\n"
"s_nop 3\n"
"v_cvt_pk_bf16_f32 v20, v16, v16\n"
"v_and_b32 v21, 0xffff0000, v20\n"
"v_sub_f32 v21, v16, v21\n"
"v_cvt_pk_bf16_f32 v20, v16, v21\n"
"v_cndmask_b32 v20, v20, 0, s[60:61]\n"
"v_mov_b32 v21, v20\n"
"v_mov_b64 v[22:23], 0\n"
"v_cvt_pk_bf16_f32 v36, v32, v32\n"
"v_and_b32 v37, 0xffff0000, v36\n"
"v_sub_f32 v37, v32, v37\n"
"v_cvt_pk_bf16_f32 v36, v32, v37\n"
"v_cndmask_b32 v36, v36, 0, s[60:61]\n"
"v_mov_b32 v37, v36\n"
"v_mov_b64 v[38:39], 0\n"
"ds_write_b128 v2, v[20:23] offset:0\n"
"ds_write_b128 v2, v[36:39] offset:1024\n"
"ds_read_b128 v[60:63], v2\n"
"ds_read_b128 v[64:67], v2 offset:1024\n"
"v_lshrrev_b32 v3, 2, v0\n"
"v_and_b32 v3, 0x7f, v3\n"
"v_and_b32 v7, 12, v3\n"
"v_add_u32 v7, 0xcc00, v7\n"
"s_lshr_b32 s89, s52, 8\n"
"s_add_u32 s89, s89, 0xcc00\n"
"v_mov_b32 v6, s89\n"
"v_mov_b32 v10, -2\n"
"ds_write_b32 v6, v10\n"
"s_waitcnt lgkmcnt(0)\n"
"s_mov_b32 s92, 0xc0135761\n"
"s_mov_b32 s93, 0xbdd2d3e8\n"
"v_mov_b32 v13, s92\n"
"s_mov_b32 s96, -1\n"
"s_mov_b32 s97, 0\n"
"s_mov_b32 s33, 4917504\n"
"s_mov_b32 s88, 0\n"
"Lggo%=:\n"
"buffer_load_dwordx2 v[68:69], off, s[40:43], s33 sc1\n"
"s_add_u32 s88, s88, 1\n"
"s_cmp_gt_u32 s88, 131072\n"
"s_cbranch_scc1 Lgend%=\n"
"Lggp%=:\n"
"s_waitcnt vmcnt(0)\n"
"v_cmp_ne_u32 vcc, s36, v68\n"
"s_cbranch_vccnz Lggo%=\n"
"v_readfirstlane_b32 s99, v69\n"
"s_lshl_b32 s33, s99, 13\n"
"s_xor_b32 s99, s99, s37\n"
"s_xor_b32 s47, s47, s33\n"
"s_xor_b32 s51, s51, s33\n"
"s_xor_b32 s67, s67, s33\n"
"s_lshr_b32 s89, s62, 8\n"
"s_xor_b32 s89, s89, s67\n"
"s_and_b32 s89, s89, s55\n"
"s_add_u32 s48, s62, s89\n"
"s_add_u32 s53, s63, s89\n"
"s_add_u32 s68, s48, s66\n"
"s_mov_b32 s94, s50\n"
"s_mov_b32 s44, -1\n"
"s_mov_b32 s46, 0x80000000\n"
"s_branch Lgnext%=\n"
"Lgloop%=:\n"
"s_barrier\n"
"ds_read_b128 v[128:131], v1 offset:0\n"
"ds_read_b128 v[132:135], v1 offset:32\n"
"ds_read_b128 v[136:139], v1 offset:64\n"
"ds_read_b128 v[140:143], v1 offset:96\n"
"ds_read_b128 v[144:147], v1 offset:128\n"
"s_bfe_u32 s50, s94, 0x10015\n"
"s_xor_b32 s50, s50, s99\n"
"s_lshl_b32 s50, s50, 13\n"
"s_add_u32 s50, s50, s94\n"
"s_waitcnt lgkmcnt(4)\n"
"v_mfma_f32_32x32x16_bf16 v[16:31], a[0:3], v[128:131], 0\n"
"s_waitcnt lgkmcnt(3)\n"
"ds_read_b128 v[148:151], v1 offset:160\n"
"v_mfma_f32_32x32x16_bf16 v[16:31], a[4:7], v[132:135], v[16:31]\n"
"s_waitcnt lgkmcnt(3)\n"
"ds_read_b128 v[152:155], v1 offset:192\n"
"v_mfma_f32_32x32x16_bf16 v[16:31], a[8:11], v[136:139], v[16:31]\n"
"s_waitcnt lgkmcnt(3)\n"
"ds_read_b128 v[156:159], v1 offset:224\n"
"v_mfma_f32_32x32x16_bf16 v[16:31], a[12:15], v[140:143], v[16:31]\n"
"s_waitcnt lgkmcnt(3)\n"
"ds_read_b128 v[160:163], v1 offset:256\n"
"ds_read2_b64 v[88:91], v4 offset0:0 offset1:8\n"
"v_mfma_f32_32x32x16_bf16 v[16:31], a[16:19], v[144:147], v[16:31]\n"
"s_waitcnt lgkmcnt(4)\n"
"ds_read_b128 v[164:167], v1 offset:288\n"
"ds_read2_b64 v[92:95], v4 offset0:16 offset1:24\n"
"v_mfma_f32_32x32x16_bf16 v[16:31], a[20:23], v[148:151], v[16:31]\n"
"s_waitcnt lgkmcnt(5)\n"
"ds_read_b128 v[168:171], v1 offset:320\n"
"v_mfma_f32_32x32x16_bf16 v[16:31], a[24:27], v[152:155], v[16:31]\n"
"s_waitcnt lgkmcnt(5)\n"
"ds_read_b128 v[172:175], v1 offset:352\n"
"v_mfma_f32_32x32x16_bf16 v[16:31], a[28:31], v[156:159], v[16:31]\n"
"s_waitcnt lgkmcnt(5)\n"
"ds_read_b128 v[176:179], v1 offset:384\n"
"v_mfma_f32_32x32x16_bf16 v[16:31], a[32:35], v[160:163], v[16:31]\n"
"s_waitcnt lgkmcnt(4)\n"
"ds_read_b128 v[180:183], v1 offset:416\n"
"s_waitcnt lgkmcnt(4)\n"
"v_add_f32 v88, v88, v92\n"
"v_add_f32 v89, v89, v93\n"
"v_add_f32 v90, v90, v94\n"
"v_mfma_f32_32x32x16_bf16 v[16:31], a[36:39], v[164:167], v[16:31]\n"
"s_waitcnt lgkmcnt(3)\n"
"ds_read_b128 v[184:187], v1 offset:448\n"
"v_add_f32 v91, v91, v95\n"
"v_add_f32 v88, v88, v90\n"
"v_add_f32 v89, v89, v91\n"
"v_mov_b32 v68, v88\n"
"v_mfma_f32_32x32x16_bf16 v[16:31], a[40:43], v[168:171], v[16:31]\n"
"s_waitcnt lgkmcnt(3)\n"
"ds_read_b128 v[188:191], v1 offset:480\n"
"v_mov_b32 v69, v89\n"
"s_nop 0\n"
"v_permlane32_swap_b32 v88, v68\n"
"v_permlane32_swap_b32 v89, v69\n"
"v_mfma_f32_32x32x16_bf16 v[16:31], a[44:47], v[172:175], v[16:31]\n"
"s_waitcnt lgkmcnt(3)\n"
"ds_read_b128 v[192:195], v1 offset:512\n"
"v_add_f32 v88, v88, v68\n"
"v_add_f32 v89, v89, v69\n"
"v_mul_f32 v68, 0xbb000000, v88\n"
"v_mul_f32 v69, 0x3b000000, v89\n"
"v_mfma_f32_32x32x16_bf16 v[16:31], a[48:51], v[176:179], v[16:31]\n"
"s_waitcnt lgkmcnt(3)\n"
"ds_read_b128 v[196:199], v1 offset:544\n"
"v_fma_f32 v69, -v68, v68, v69\n"
"v_max_f32 v69, 0, v69\n"
"v_add_f32 v69, 0x3727c5ac, v69\n"
"v_rsq_f32 v9, v69\n"
"v_mfma_f32_32x32x16_bf16 v[16:31], a[52:55], v[180:183], v[16:31]\n"
"s_waitcnt lgkmcnt(3)\n"
"ds_read_b128 v[200:203], v1 offset:576\n"
"v_cvt_pk_bf16_f32 v56, v68, v68\n"
"v_and_b32 v70, 0xffff0000, v56\n"
"v_sub_f32 v70, v68, v70\n"
"v_cvt_pk_bf16_f32 v57, v70, v70\n"
"v_mfma_f32_32x32x16_bf16 v[16:31], a[56:59], v[184:187], v[16:31]\n"
"s_waitcnt lgkmcnt(3)\n"
"ds_read_b128 v[204:207], v1 offset:608\n"
"v_mov_b64 v[58:59], 0\n"
"v_mfma_f32_32x32x16_bf16 v[16:31], a[60:63], v[188:191], v[16:31]\n"
"s_waitcnt lgkmcnt(3)\n"
"ds_read_b128 v[208:211], v1 offset:640\n"
"v_mfma_f32_32x32x16_bf16 v[16:31], a[64:67], v[192:195], v[16:31]\n"
"s_waitcnt lgkmcnt(3)\n"
"ds_read_b128 v[212:215], v1 offset:672\n"
"v_mfma_f32_32x32x16_bf16 v[16:31], a[68:71], v[196:199], v[16:31]\n"
"s_waitcnt lgkmcnt(3)\n"
"ds_read_b128 v[216:219], v1 offset:704\n"
"v_mfma_f32_32x32x16_bf16 v[16:31], a[72:75], v[200:203], v[16:31]\n"
"s_waitcnt lgkmcnt(3)\n"
"ds_read_b128 v[220:223], v1 offset:736\n"
"v_mfma_f32_32x32x16_bf16 v[16:31], a[76:79], v[204:207], v[16:31]\n"
"s_waitcnt lgkmcnt(3)\n"
"ds_read_b128 v[224:227], v1 offset:768\n"
"v_mfma_f32_32x32x16_bf16 v[16:31], a[80:83], v[208:211], v[16:31]\n"
"s_waitcnt lgkmcnt(3)\n"
"ds_read_b128 v[228:231], v1 offset:800\n"
"v_mfma_f32_32x32x16_bf16 v[16:31], a[84:87], v[212:215], v[16:31]\n"
"s_waitcnt lgkmcnt(3)\n"
"ds_read_b128 v[232:235], v1 offset:832\n"
"v_mfma_f32_32x32x16_bf16 v[16:31], a[88:91], v[216:219], v[16:31]\n"
"s_waitcnt vmcnt(0)\n"
"buffer_store_dword v10, v3, s[40:43], s46 offen sc1\n"
"s_mov_b32 s46, 0x80000000\n"
"s_waitcnt lgkmcnt(3)\n"
"ds_read_b128 v[236:239], v1 offset:864\n"
"v_mfma_f32_32x32x16_bf16 v[16:31], a[92:95], v[220:223], v[16:31]\n"
"v_mov_b32 v8, 0x7fffffff\n"
"buffer_atomic_smin v8, v5, s[40:43], s47 offen sc0 sc1\n"
"s_waitcnt lgkmcnt(3)\n"
"ds_read_b128 v[240:243], v1 offset:896\n"
"v_mfma_f32_32x32x16_bf16 v[16:31], a[96:99], v[224:227], v[16:31]\n"
"s_waitcnt lgkmcnt(3)\n"
"ds_read_b128 v[244:247], v1 offset:928\n"
"v_mfma_f32_32x32x16_bf16 v[16:31], a[100:103], v[228:231], v[16:31]\n"
"s_waitcnt lgkmcnt(3)\n"
"ds_read_b128 v[248:251], v1 offset:960\n"
"v_mfma_f32_32x32x16_bf16 v[16:31], a[104:107], v[232:235], v[16:31]\n"
"s_waitcnt lgkmcnt(3)\n"
"ds_read_b128 v[252:255], v1 offset:992\n"
"v_mfma_f32_32x32x16_bf16 v[16:31], a[108:111], v[236:239], v[16:31]\n"
"s_waitcnt lgkmcnt(3)\n"
"v_mfma_f32_32x32x16_bf16 v[16:31], a[112:115], v[240:243], v[16:31]\n"
"s_waitcnt lgkmcnt(2)\n"
"v_mfma_f32_32x32x16_bf16 v[16:31], a[116:119], v[244:247], v[16:31]\n"
"s_waitcnt lgkmcnt(1)\n"
"v_mfma_f32_32x32x16_bf16 v[16:31], a[120:123], v[248:251], v[16:31]\n"
"s_waitcnt lgkmcnt(0)\n"
"v_mfma_f32_32x32x16_bf16 v[16:31], a[124:127], v[252:255], v[16:31]\n"
"v_mfma_f32_32x32x16_bf16 v[16:31], v[60:63], v[56:59], v[16:31]\n"
"s_add_u32 s94, s94, 0x40000\n"
"s_mov_b32 s46, s51\n"
"v_xor_b32 v1, 0x10000, v1\n"
"v_xor_b32 v4, 0x10000, v4\n"
"v_mov_b32 v10, s44\n"
"v_mfma_f32_32x32x16_bf16 v[32:47], v[64:67], v[56:59], 0\n"
"s_nop 3\n"
"v_mul_f32 v16, v9, v16\n"
"v_mul_f32 v17, v9, v17\n"
"v_mfma_f32_32x32x16_bf16 v[32:47], a[128:131], v[128:131], v[32:47]\n"
"v_mul_f32 v18, v9, v18\n"
"v_mul_f32 v19, v9, v19\n"
"v_mul_f32 v72, v16, v16\n"
"v_mul_f32 v73, v17, v17\n"
"v_mul_f32 v74, v18, v18\n"
"v_mul_f32 v75, v19, v19\n"
"v_mfma_f32_32x32x16_bf16 v[32:47], a[132:135], v[132:135], v[32:47]\n"
"v_fma_f32 v72, v72, s93, v13\n"
"v_fma_f32 v73, v73, s93, v13\n"
"v_fma_f32 v74, v74, s93, v13\n"
"v_fma_f32 v75, v75, s93, v13\n"
"v_mul_f32 v72, v72, v16\n"
"v_mul_f32 v73, v73, v17\n"
"v_mfma_f32_32x32x16_bf16 v[32:47], a[136:139], v[136:139], v[32:47]\n"
"v_mul_f32 v74, v74, v18\n"
"v_mul_f32 v75, v75, v19\n"
"v_exp_f32 v72, v72\n"
"v_exp_f32 v73, v73\n"
"v_mfma_f32_32x32x16_bf16 v[32:47], a[140:143], v[140:143], v[32:47]\n"
"v_exp_f32 v74, v74\n"
"v_exp_f32 v75, v75\n"
"v_mul_f32 v16, v9, v16\n"
"v_mul_f32 v17, v9, v17\n"
"v_mfma_f32_32x32x16_bf16 v[32:47], a[144:147], v[144:147], v[32:47]\n"
"v_mul_f32 v18, v9, v18\n"
"v_mul_f32 v19, v9, v19\n"
"v_add_f32 v72, 1.0, v72\n"
"v_add_f32 v73, 1.0, v73\n"
"v_add_f32 v74, 1.0, v74\n"
"v_add_f32 v75, 1.0, v75\n"
"v_mfma_f32_32x32x16_bf16 v[32:47], a[148:151], v[148:151], v[32:47]\n"
"v_rcp_f32 v72, v72\n"
"v_rcp_f32 v73, v73\n"
"v_rcp_f32 v74, v74\n"
"v_mfma_f32_32x32x16_bf16 v[32:47], a[152:155], v[152:155], v[32:47]\n"
"v_rcp_f32 v75, v75\n"
"v_mul_f32 v16, v72, v16\n"
"v_mul_f32 v17, v73, v17\n"
"v_mul_f32 v18, v74, v18\n"
"v_mul_f32 v19, v75, v19\n"
"v_mfma_f32_32x32x16_bf16 v[32:47], a[156:159], v[156:159], v[32:47]\n"
"v_mul_f32 v20, v9, v20\n"
"v_mul_f32 v21, v9, v21\n"
"v_mul_f32 v22, v9, v22\n"
"v_mul_f32 v23, v9, v23\n"
"v_mul_f32 v76, v20, v20\n"
"v_mul_f32 v77, v21, v21\n"
"v_mfma_f32_32x32x16_bf16 v[32:47], a[160:163], v[160:163], v[32:47]\n"
"v_mul_f32 v78, v22, v22\n"
"v_mul_f32 v79, v23, v23\n"
"v_fma_f32 v76, v76, s93, v13\n"
"v_fma_f32 v77, v77, s93, v13\n"
"v_fma_f32 v78, v78, s93, v13\n"
"v_fma_f32 v79, v79, s93, v13\n"
"v_mfma_f32_32x32x16_bf16 v[32:47], a[164:167], v[164:167], v[32:47]\n"
"v_mul_f32 v76, v76, v20\n"
"v_mul_f32 v77, v77, v21\n"
"v_mul_f32 v78, v78, v22\n"
"v_mul_f32 v79, v79, v23\n"
"v_exp_f32 v76, v76\n"
"v_mfma_f32_32x32x16_bf16 v[32:47], a[168:171], v[168:171], v[32:47]\n"
"v_exp_f32 v77, v77\n"
"v_exp_f32 v78, v78\n"
"v_exp_f32 v79, v79\n"
"v_mfma_f32_32x32x16_bf16 v[32:47], a[172:175], v[172:175], v[32:47]\n"
"v_mul_f32 v20, v9, v20\n"
"v_mul_f32 v21, v9, v21\n"
"v_mul_f32 v22, v9, v22\n"
"v_mul_f32 v23, v9, v23\n"
"v_add_f32 v76, 1.0, v76\n"
"v_add_f32 v77, 1.0, v77\n"
"v_mfma_f32_32x32x16_bf16 v[32:47], a[176:179], v[176:179], v[32:47]\n"
"v_add_f32 v78, 1.0, v78\n"
"v_add_f32 v79, 1.0, v79\n"
"v_rcp_f32 v76, v76\n"
"v_rcp_f32 v77, v77\n"
"v_mfma_f32_32x32x16_bf16 v[32:47], a[180:183], v[180:183], v[32:47]\n"
"v_rcp_f32 v78, v78\n"
"v_rcp_f32 v79, v79\n"
"v_mul_f32 v20, v76, v20\n"
"v_mul_f32 v21, v77, v21\n"
"v_mfma_f32_32x32x16_bf16 v[32:47], a[184:187], v[184:187], v[32:47]\n"
"v_mul_f32 v22, v78, v22\n"
"v_mul_f32 v23, v79, v23\n"
"v_mul_f32 v24, v9, v24\n"
"v_mul_f32 v25, v9, v25\n"
"v_mul_f32 v26, v9, v26\n"
"v_mul_f32 v27, v9, v27\n"
"v_mfma_f32_32x32x16_bf16 v[32:47], a[188:191], v[188:191], v[32:47]\n"
"v_mul_f32 v80, v24, v24\n"
"v_mul_f32 v81, v25, v25\n"
"v_mul_f32 v82, v26, v26\n"
"v_mul_f32 v83, v27, v27\n"
"v_fma_f32 v80, v80, s93, v13\n"
"v_fma_f32 v81, v81, s93, v13\n"
"v_mfma_f32_32x32x16_bf16 v[32:47], a[192:195], v[192:195], v[32:47]\n"
"v_fma_f32 v82, v82, s93, v13\n"
"v_fma_f32 v83, v83, s93, v13\n"
"v_mul_f32 v80, v80, v24\n"
"v_mul_f32 v81, v81, v25\n"
"v_mul_f32 v82, v82, v26\n"
"v_mul_f32 v83, v83, v27\n"
"v_mfma_f32_32x32x16_bf16 v[32:47], a[196:199], v[196:199], v[32:47]\n"
"v_exp_f32 v80, v80\n"
"v_exp_f32 v81, v81\n"
"v_exp_f32 v82, v82\n"
"v_mfma_f32_32x32x16_bf16 v[32:47], a[200:203], v[200:203], v[32:47]\n"
"v_exp_f32 v83, v83\n"
"v_mul_f32 v24, v9, v24\n"
"v_mul_f32 v25, v9, v25\n"
"v_mul_f32 v26, v9, v26\n"
"v_mul_f32 v27, v9, v27\n"
"v_mfma_f32_32x32x16_bf16 v[32:47], a[204:207], v[204:207], v[32:47]\n"
"v_add_f32 v80, 1.0, v80\n"
"v_add_f32 v81, 1.0, v81\n"
"v_add_f32 v82, 1.0, v82\n"
"v_add_f32 v83, 1.0, v83\n"
"v_rcp_f32 v80, v80\n"
"s_waitcnt vmcnt(0)\n"
"v_cmp_ge_i32 vcc, s44, v8\n"
"s_and_b64 vcc, vcc, s[96:97]\n"
"s_cbranch_vccnz Lgn2%=\n"
"v_mfma_f32_32x32x16_bf16 v[32:47], a[208:211], v[208:211], v[32:47]\n"
"s_add_u32 m0, s49, 0\n"
"s_add_u32 s89, s48, 0\n"
"buffer_load_dwordx4 v0, s[40:43], s89 offen sc1 lds\n"
"v_rcp_f32 v81, v81\n"
"v_rcp_f32 v82, v82\n"
"v_rcp_f32 v83, v83\n"
"v_mfma_f32_32x32x16_bf16 v[32:47], a[212:215], v[212:215], v[32:47]\n"
"s_add_u32 m0, s49, 1040\n"
"s_add_u32 s89, s48, 1024\n"
"buffer_load_dwordx4 v0, s[40:43], s89 offen sc1 lds\n"
"v_mul_f32 v24, v80, v24\n"
"v_mul_f32 v25, v81, v25\n"
"v_mul_f32 v26, v82, v26\n"
"v_mul_f32 v27, v83, v27\n"
"v_mul_f32 v28, v9, v28\n"
"v_mul_f32 v29, v9, v29\n"
"v_mfma_f32_32x32x16_bf16 v[32:47], a[216:219], v[216:219], v[32:47]\n"
"s_add_u32 m0, s49, 2080\n"
"s_add_u32 s89, s48, 2048\n"
"buffer_load_dwordx4 v0, s[40:43], s89 offen sc1 lds\n"
"v_mul_f32 v30, v9, v30\n"
"v_mul_f32 v31, v9, v31\n"
"v_mul_f32 v84, v28, v28\n"
"v_mul_f32 v85, v29, v29\n"
"v_mul_f32 v86, v30, v30\n"
"v_mul_f32 v87, v31, v31\n"
"v_mfma_f32_32x32x16_bf16 v[32:47], a[220:223], v[220:223], v[32:47]\n"
"s_add_u32 m0, s49, 3120\n"
"s_add_u32 s89, s48, 3072\n"
"buffer_load_dwordx4 v0, s[40:43], s89 offen sc1 lds\n"
"v_fma_f32 v84, v84, s93, v13\n"
"v_fma_f32 v85, v85, s93, v13\n"
"v_fma_f32 v86, v86, s93, v13\n"
"v_fma_f32 v87, v87, s93, v13\n"
"v_mul_f32 v84, v84, v28\n"
"v_mul_f32 v85, v85, v29\n"
"v_mfma_f32_32x32x16_bf16 v[32:47], a[224:227], v[224:227], v[32:47]\n"
"s_add_u32 m0, s49, 4160\n"
"s_add_u32 s89, s68, 0\n"
"buffer_load_dwordx4 v0, s[40:43], s89 offen sc1 lds\n"
"v_mul_f32 v86, v86, v30\n"
"v_mul_f32 v87, v87, v31\n"
"v_exp_f32 v84, v84\n"
"v_exp_f32 v85, v85\n"
"v_mfma_f32_32x32x16_bf16 v[32:47], a[228:231], v[228:231], v[32:47]\n"
"s_add_u32 m0, s49, 5200\n"
"s_add_u32 s89, s68, 1024\n"
"buffer_load_dwordx4 v0, s[40:43], s89 offen sc1 lds\n"
"v_exp_f32 v86, v86\n"
"v_exp_f32 v87, v87\n"
"v_mul_f32 v28, v9, v28\n"
"v_mul_f32 v29, v9, v29\n"
"v_mfma_f32_32x32x16_bf16 v[32:47], a[232:235], v[232:235], v[32:47]\n"
"s_add_u32 m0, s49, 6240\n"
"s_add_u32 s89, s68, 2048\n"
"buffer_load_dwordx4 v0, s[40:43], s89 offen sc1 lds\n"
"v_mul_f32 v30, v9, v30\n"
"v_mul_f32 v31, v9, v31\n"
"v_add_f32 v84, 1.0, v84\n"
"v_add_f32 v85, 1.0, v85\n"
"v_add_f32 v86, 1.0, v86\n"
"v_add_f32 v87, 1.0, v87\n"
"v_mfma_f32_32x32x16_bf16 v[32:47], a[236:239], v[236:239], v[32:47]\n"
"s_add_u32 m0, s49, 7280\n"
"s_add_u32 s89, s68, 3072\n"
"buffer_load_dwordx4 v0, s[40:43], s89 offen sc1 lds\n"
"ds_read_b32 v11, v7\n"
"v_rcp_f32 v84, v84\n"
"v_rcp_f32 v85, v85\n"
"v_rcp_f32 v86, v86\n"
"v_mfma_f32_32x32x16_bf16 v[32:47], a[240:243], v[240:243], v[32:47]\n"
"s_mov_b32 m0, s54\n"
"s_xor_b32 s54, s54, 0x10000\n"
"buffer_load_dwordx4 v12, s[40:43], s53 offen sc1 lds\n"
"v_mov_b32 v8, s44\n"
"v_rcp_f32 v87, v87\n"
"v_mul_f32 v28, v84, v28\n"
"v_mul_f32 v29, v85, v29\n"
"v_mul_f32 v30, v86, v30\n"
"v_mul_f32 v31, v87, v31\n"
"v_mfma_f32_32x32x16_bf16 v[32:47], a[244:247], v[244:247], v[32:47]\n"
"ds_write_b32 v6, v8\n"
"s_add_u32 s62, s62, s64\n"
"s_add_u32 s63, s63, s65\n"
"s_xor_b32 s49, s49, 0x10000\n"
"s_lshr_b32 s89, s62, 8\n"
"v_mfma_f32_32x32x16_bf16 v[32:47], a[248:251], v[248:251], v[32:47]\n"
"s_xor_b32 s89, s89, s67\n"
"s_and_b32 s89, s89, s55\n"
"s_add_u32 s48, s62, s89\n"
"s_add_u32 s53, s63, s89\n"
"s_add_u32 s68, s48, s66\n"
"Lgp2%=:\n"
"v_mfma_f32_32x32x16_bf16 v[32:47], a[252:255], v[252:255], v[32:47]\n"
"s_waitcnt lgkmcnt(0)\n"
"v_cmp_ne_u32 vcc, s44, v11\n"
"s_nop 7\n"
"s_nop 1\n"
"v_pk_mul_f32 v[32:33], v[16:17], v[32:33]\n"
"v_cvt_pk_bf16_f32 v48, v32, v33\n"
"v_pk_mul_f32 v[34:35], v[18:19], v[34:35]\n"
"v_cvt_pk_bf16_f32 v49, v34, v35\n"
"v_pk_mul_f32 v[36:37], v[20:21], v[36:37]\n"
"v_cvt_pk_bf16_f32 v50, v36, v37\n"
"v_pk_mul_f32 v[38:39], v[22:23], v[38:39]\n"
"v_cvt_pk_bf16_f32 v51, v38, v39\n"
"v_pk_mul_f32 v[40:41], v[24:25], v[40:41]\n"
"v_cvt_pk_bf16_f32 v52, v40, v41\n"
"v_pk_mul_f32 v[42:43], v[26:27], v[42:43]\n"
"v_cvt_pk_bf16_f32 v53, v42, v43\n"
"v_pk_mul_f32 v[44:45], v[28:29], v[44:45]\n"
"v_cvt_pk_bf16_f32 v54, v44, v45\n"
"v_pk_mul_f32 v[46:47], v[30:31], v[46:47]\n"
"v_cvt_pk_bf16_f32 v55, v46, v47\n"
"buffer_store_dwordx4 v[48:51], v14, s[40:43], s50 offen offset:0 sc1\n"
"buffer_store_dwordx4 v[52:55], v14, s[40:43], s50 offen offset:512 sc1\n"
"s_add_u32 s44, s44, 1\n"
"s_waitcnt vmcnt(2)\n"
"s_cbranch_vccz Lgloop%=\n"
"s_waitcnt vmcnt(0)\n"
"buffer_store_dword v10, v3, s[40:43], s46 offen sc1\n"
"s_mov_b32 s46, 0x80000000\n"
"s_branch Lgloop%=\n"
"Lgn2%=:\n"
"v_mfma_f32_32x32x16_bf16 v[32:47], a[208:211], v[208:211], v[32:47]\n"
"v_mov_b32 v8, 0x7fffffff\n"
"buffer_atomic_smin v8, v5, s[40:43], s47 offen sc0 sc1\n"
"v_rcp_f32 v81, v81\n"
"v_rcp_f32 v82, v82\n"
"v_rcp_f32 v83, v83\n"
"v_mfma_f32_32x32x16_bf16 v[32:47], a[212:215], v[212:215], v[32:47]\n"
"v_mul_f32 v24, v80, v24\n"
"v_mul_f32 v25, v81, v25\n"
"v_mul_f32 v26, v82, v26\n"
"v_mul_f32 v27, v83, v27\n"
"v_mul_f32 v28, v9, v28\n"
"v_mul_f32 v29, v9, v29\n"
"v_mfma_f32_32x32x16_bf16 v[32:47], a[216:219], v[216:219], v[32:47]\n"
"v_mul_f32 v30, v9, v30\n"
"v_mul_f32 v31, v9, v31\n"
"v_mul_f32 v84, v28, v28\n"
"v_mul_f32 v85, v29, v29\n"
"v_mul_f32 v86, v30, v30\n"
"v_mul_f32 v87, v31, v31\n"
"v_mfma_f32_32x32x16_bf16 v[32:47], a[220:223], v[220:223], v[32:47]\n"
"v_fma_f32 v84, v84, s93, v13\n"
"v_fma_f32 v85, v85, s93, v13\n"
"v_fma_f32 v86, v86, s93, v13\n"
"v_fma_f32 v87, v87, s93, v13\n"
"v_mul_f32 v84, v84, v28\n"
"v_mul_f32 v85, v85, v29\n"
"v_mfma_f32_32x32x16_bf16 v[32:47], a[224:227], v[224:227], v[32:47]\n"
"v_mul_f32 v86, v86, v30\n"
"v_mul_f32 v87, v87, v31\n"
"v_exp_f32 v84, v84\n"
"v_exp_f32 v85, v85\n"
"v_mfma_f32_32x32x16_bf16 v[32:47], a[228:231], v[228:231], v[32:47]\n"
"v_exp_f32 v86, v86\n"
"v_exp_f32 v87, v87\n"
"v_mul_f32 v28, v9, v28\n"
"v_mul_f32 v29, v9, v29\n"
"v_mfma_f32_32x32x16_bf16 v[32:47], a[232:235], v[232:235], v[32:47]\n"
"v_mul_f32 v30, v9, v30\n"
"v_mul_f32 v31, v9, v31\n"
"v_add_f32 v84, 1.0, v84\n"
"v_add_f32 v85, 1.0, v85\n"
"v_add_f32 v86, 1.0, v86\n"
"v_add_f32 v87, 1.0, v87\n"
"v_mfma_f32_32x32x16_bf16 v[32:47], a[236:239], v[236:239], v[32:47]\n"
"ds_read_b32 v11, v7\n"
"v_rcp_f32 v84, v84\n"
"v_rcp_f32 v85, v85\n"
"v_rcp_f32 v86, v86\n"
"v_mfma_f32_32x32x16_bf16 v[32:47], a[240:243], v[240:243], v[32:47]\n"
"v_rcp_f32 v87, v87\n"
"v_mul_f32 v28, v84, v28\n"
"v_mul_f32 v29, v85, v29\n"
"v_mul_f32 v30, v86, v30\n"
"v_mul_f32 v31, v87, v31\n"
"v_mfma_f32_32x32x16_bf16 v[32:47], a[244:247], v[244:247], v[32:47]\n"
"v_mfma_f32_32x32x16_bf16 v[32:47], a[248:251], v[248:251], v[32:47]\n"
"v_mfma_f32_32x32x16_bf16 v[32:47], a[252:255], v[252:255], v[32:47]\n"
"s_waitcnt lgkmcnt(0)\n"
"s_nop 7\n"
"s_nop 2\n"
"v_pk_mul_f32 v[32:33], v[16:17], v[32:33]\n"
"v_cvt_pk_bf16_f32 v48, v32, v33\n"
"v_pk_mul_f32 v[34:35], v[18:19], v[34:35]\n"
"v_cvt_pk_bf16_f32 v49, v34, v35\n"
"v_pk_mul_f32 v[36:37], v[20:21], v[36:37]\n"
"v_cvt_pk_bf16_f32 v50, v36, v37\n"
"v_pk_mul_f32 v[38:39], v[22:23], v[38:39]\n"
"v_cvt_pk_bf16_f32 v51, v38, v39\n"
"v_pk_mul_f32 v[40:41], v[24:25], v[40:41]\n"
"v_cvt_pk_bf16_f32 v52, v40, v41\n"
"v_pk_mul_f32 v[42:43], v[26:27], v[42:43]\n"
"v_cvt_pk_bf16_f32 v53, v42, v43\n"
"v_pk_mul_f32 v[44:45], v[28:29], v[44:45]\n"
"v_cvt_pk_bf16_f32 v54, v44, v45\n"
"v_pk_mul_f32 v[46:47], v[30:31], v[46:47]\n"
"v_cvt_pk_bf16_f32 v55, v46, v47\n"
"buffer_store_dwordx4 v[48:51], v14, s[40:43], s50 offen offset:0 sc1\n"
"buffer_store_dwordx4 v[52:55], v14, s[40:43], s50 offen offset:512 sc1\n"
"s_waitcnt vmcnt(2)\n"
"v_cmp_ge_i32 vcc, s44, v8\n"
"s_and_b64 vcc, vcc, s[96:97]\n"
"s_cbranch_vccnz Lgn3%=\n"
"s_add_u32 m0, s49, 0\n"
"s_add_u32 s89, s48, 0\n"
"buffer_load_dwordx4 v0, s[40:43], s89 offen sc1 lds\n"
"s_add_u32 m0, s49, 1040\n"
"s_add_u32 s89, s48, 1024\n"
"buffer_load_dwordx4 v0, s[40:43], s89 offen sc1 lds\n"
"s_add_u32 m0, s49, 2080\n"
"s_add_u32 s89, s48, 2048\n"
"buffer_load_dwordx4 v0, s[40:43], s89 offen sc1 lds\n"
"s_add_u32 m0, s49, 3120\n"
"s_add_u32 s89, s48, 3072\n"
"buffer_load_dwordx4 v0, s[40:43], s89 offen sc1 lds\n"
"s_add_u32 m0, s49, 4160\n"
"s_add_u32 s89, s68, 0\n"
"buffer_load_dwordx4 v0, s[40:43], s89 offen sc1 lds\n"
"s_add_u32 m0, s49, 5200\n"
"s_add_u32 s89, s68, 1024\n"
"buffer_load_dwordx4 v0, s[40:43], s89 offen sc1 lds\n"
"s_add_u32 m0, s49, 6240\n"
"s_add_u32 s89, s68, 2048\n"
"buffer_load_dwordx4 v0, s[40:43], s89 offen sc1 lds\n"
"s_add_u32 m0, s49, 7280\n"
"s_add_u32 s89, s68, 3072\n"
"buffer_load_dwordx4 v0, s[40:43], s89 offen sc1 lds\n"
"s_mov_b32 m0, s54\n"
"s_xor_b32 s54, s54, 0x10000\n"
"buffer_load_dwordx4 v12, s[40:43], s53 offen sc1 lds\n"
"v_mov_b32 v8, s44\n"
"ds_write_b32 v6, v8\n"
"s_add_u32 s62, s62, s64\n"
"s_add_u32 s63, s63, s65\n"
"s_xor_b32 s49, s49, 0x10000\n"
"s_lshr_b32 s89, s62, 8\n"
"s_xor_b32 s89, s89, s67\n"
"s_and_b32 s89, s89, s55\n"
"s_add_u32 s48, s62, s89\n"
"s_add_u32 s53, s63, s89\n"
"s_add_u32 s68, s48, s66\n"
"s_waitcnt vmcnt(9)\n"
"buffer_store_dword v10, v3, s[40:43], s46 offen sc1\n"
"s_mov_b32 s46, 0x80000000\n"
"s_add_u32 s44, s44, 1\n"
"s_waitcnt vmcnt(1)\n"
"s_branch Lgloop%=\n"
"Lgn3%=:\n"
"v_mov_b32 v8, 0x7fffffff\n"
"buffer_atomic_smin v8, v5, s[40:43], s47 offen sc0 sc1\n"
"s_waitcnt vmcnt(1)\n"
"buffer_store_dword v10, v3, s[40:43], s46 offen sc1\n"
"s_mov_b32 s46, 0x80000000\n"
"s_cmp_eq_u32 s44, 63\n"
"s_cbranch_scc1 Lgend%=\n"
"s_waitcnt vmcnt(1)\n"
"v_cmp_ge_i32 vcc, s44, v8\n"
"s_and_b64 vcc, vcc, s[96:97]\n"
"s_cbranch_vccz Lgsr%=\n"
"Lgnext%=:\n"
"s_mov_b32 s88, 0\n"
"Lgsp%=:\n"
"v_mov_b32 v8, 0x7fffffff\n"
"buffer_atomic_smin v8, v5, s[40:43], s47 offen sc0 sc1\n"
"s_add_u32 s88, s88, 1\n"
"s_cmp_gt_u32 s88, 131072\n"
"s_cbranch_scc1 Lgend%=\n"
"s_waitcnt vmcnt(0)\n"
"v_cmp_ge_i32 vcc, s44, v8\n"
"s_and_b64 vcc, vcc, s[96:97]\n"
"s_cbranch_vccz Lgsr%=\n"
"s_sleep 1\n"
"s_branch Lgsp%=\n"
"Lgsr%=:\n"
"s_add_u32 m0, s49, 0\n"
"s_add_u32 s89, s48, 0\n"
"buffer_load_dwordx4 v0, s[40:43], s89 offen sc1 lds\n"
"s_add_u32 m0, s49, 1040\n"
"s_add_u32 s89, s48, 1024\n"
"buffer_load_dwordx4 v0, s[40:43], s89 offen sc1 lds\n"
"s_add_u32 m0, s49, 2080\n"
"s_add_u32 s89, s48, 2048\n"
"buffer_load_dwordx4 v0, s[40:43], s89 offen sc1 lds\n"
"s_add_u32 m0, s49, 3120\n"
"s_add_u32 s89, s48, 3072\n"
"buffer_load_dwordx4 v0, s[40:43], s89 offen sc1 lds\n"
"s_add_u32 m0, s49, 4160\n"
"s_add_u32 s89, s68, 0\n"
"buffer_load_dwordx4 v0, s[40:43], s89 offen sc1 lds\n"
"s_add_u32 m0, s49, 5200\n"
"s_add_u32 s89, s68, 1024\n"
"buffer_load_dwordx4 v0, s[40:43], s89 offen sc1 lds\n"
"s_add_u32 m0, s49, 6240\n"
"s_add_u32 s89, s68, 2048\n"
"buffer_load_dwordx4 v0, s[40:43], s89 offen sc1 lds\n"
"s_add_u32 m0, s49, 7280\n"
"s_add_u32 s89, s68, 3072\n"
"buffer_load_dwordx4 v0, s[40:43], s89 offen sc1 lds\n"
"s_mov_b32 m0, s54\n"
"s_xor_b32 s54, s54, 0x10000\n"
"buffer_load_dwordx4 v12, s[40:43], s53 offen sc1 lds\n"
"v_mov_b32 v8, s44\n"
"ds_write_b32 v6, v8\n"
"s_add_u32 s62, s62, s64\n"
"s_add_u32 s63, s63, s65\n"
"s_xor_b32 s49, s49, 0x10000\n"
"s_lshr_b32 s89, s62, 8\n"
"s_xor_b32 s89, s89, s67\n"
"s_and_b32 s89, s89, s55\n"
"s_add_u32 s48, s62, s89\n"
"s_add_u32 s53, s63, s89\n"
"s_add_u32 s68, s48, s66\n"
"s_waitcnt vmcnt(0)\n"
"s_add_u32 s44, s44, 1\n"
"s_branch Lgloop%=\n"
"Lgend%=:\n"
"s_waitcnt vmcnt(0) lgkmcnt(0)\n"
"LKGU:\n"
        OPS);
      }
    } else if(role==4 || role==7){
      for(int s=cp; s<64; s+=4){
        v4u d[8], so={0,0,0,0};
#pragma unroll
        for(int n=0;n<8;n++) d[n]=*(const v4u*)(x+(size_t)(s*32+4*w+16*(n>>2)+(n&3))*512+lane*8);
        if(role==4 && s<4){
          unsigned t2[2]={~0u,~0u};
          for(int r=0; r<4 || (r<64 && (unsigned)__builtin_amdgcn_s_memrealtime()-tE<CALT); r++){
  #pragma unroll
            for(int k=0;k<2;k++){ const unsigned o=OFF_CH+p*0x200000u+0x100000u+k*8192+(r&1)*4096+w*1024;
              const unsigned long long t0=clk(); if(lane<32) __builtin_amdgcn_raw_buffer_store_b32(r,rs,lane*4,o,SC1);
              asm volatile("s_waitcnt vmcnt(0)" ::: "memory"); t2[k]=min(t2[k],(unsigned)(clk()-t0)); } }
          unsigned* sm=(unsigned*)smem; if(lane==0){ sm[w*2]=t2[0]; sm[w*2+1]=t2[1]; }
          __syncthreads();
          if(tid<32) __builtin_amdgcn_raw_buffer_store_b32((sm[1]+sm[3]+sm[5]+sm[7]<sm[0]+sm[2]+sm[4]+sm[6])!=(hf!=0)? tag|1u : tag&~1u,rs,tid*4,OFF_FLAG+F_PAR+p*128,SC1);
        }
#pragma unroll
        for(int n=0;n<8;n++){ float a=0.f, q=0.f;
#pragma unroll
          for(int e=0;e<4;e++){ const float lo=__uint_as_float(d[n][e]<<16), hi=__uint_as_float(d[n][e]&0xffff0000u); a+=lo+hi; q+=lo*lo+hi*hi; }
#pragma unroll
          for(int m=1;m<8;m<<=1){ a+=__shfl_xor(a,m); q+=__shfl_xor(q,m); }
          if((lane&3)==(n>>1)){ so[(n&1)*2]=__float_as_uint(a); so[(n&1)*2+1]=__float_as_uint(q); } }
        if(!(lane&4)) __builtin_amdgcn_raw_buffer_store_b128(so,rs,(lane>>3)*32+(lane&1)*16+(lane&2)*512,OFF_SA+s*2048+w*256,SC1);
        asm volatile("s_waitcnt vmcnt(0)" ::: "memory");
        if(lane==0) __builtin_amdgcn_raw_buffer_store_b64(v2u{(unsigned)s+3,s<60? tag : ep},rs,0,OFF_FLAG+F_X0+(w*4+cp)*FS,SC1);
        if(s==0){
          bool ok=false; unsigned pa=0;
          for(unsigned it=0; it<SPINCAP && !ok; it++){ unsigned r[4];
#pragma unroll
            for(int i=0;i<4;i++) r[i]=__builtin_amdgcn_raw_buffer_load_b32(rs,tid*128,OFF_FLAG+F_UP+i*32768,SC1);
            pa=__builtin_amdgcn_raw_buffer_load_b32(rs,0,OFF_FLAG+F_PAR,SC1);
            ok=__builtin_amdgcn_ballot_w64(r[0]!=tag || r[1]!=tag || r[2]!=tag || r[3]!=tag || (pa^tag)>1u)==0; if(!ok) __builtin_amdgcn_s_sleep(4); }
          unsigned* sm=(unsigned*)smem; if(lane==0) sm[w]=ok;
          __syncthreads();
          if(sm[0]&sm[1]&sm[2]&sm[3]){
            if(tid==0) __builtin_amdgcn_raw_buffer_store_b64(v2u{tag,pa&1u},rs,0,OFF_FLAG+F_EP,SC1);

#pragma unroll
            for(int i=0;i<4;i++) __builtin_amdgcn_raw_buffer_store_b32(ep,rs,tid*128,OFF_FLAG+F_UP+i*32768,SC1); }
        }
      }
    } else if(role==3){
      const int nb=j;
      VC[0]=lane*16; VC[1]=(lane%14)*FS; VC[2]=(448*w+(lane>>3))*1024+nb*128+(((lane&7)^((g&1)<<1))*16); VC[3]=w*32768+(8*g+q4)*128+((q4>>1)<<5)+8*p4;
      VC[4]=w*4160+lane*16; VC[5]=(c>>2)*1040+((c&3)*16+4*w+g)*16; VC[6]=(4*(w&1)+g)*1024+nb*128+c*8;
      SA[12]=phh+OFF_H+hf*131072+w*32768; SA[13]=w*32768; SA[14]=OFF_FLAG+F_H+p*16384+SDH*8192+w*14*FS; SA[15]=p? 0x80000000u : OFF_FLAG+F_X2+SD2*8192+(w*16+hf*8+nb)*FS;
      setrs(SB,0,ws,WS_USED); if(p) setrs(SB,4,out,2048*512*2); else setrs(SB,4,ws,WS_USED);
      SA[5]=0; SB[8]=p? 0x8000 : 0x80000; SB[9]=p? 0 : 0x2000; SB[10]=pv+OFF_V+(2*hf+(w>>1))*16384; SB[11]=p? (2*hf+(w>>1))*8192 : OFF_V+(4+2*hf+(w>>1))*16384;
      setrs(SC,0,Wd,1792*512*2); SC[4]=p? 0x80000000u : OFF_V+9*16384+(4*hf+w)*256+nb*32; VC[7]=(c==15)? g*8 : 0x40000000;
      asm volatile(
"s_waitcnt lgkmcnt(0)\n"
"s_nop 4\n"
"s_lshl_b32 s93, s38, 17\n"
"s_lshl_b32 s94, s38, 14\n"
"s_add_u32 m0, s53, 0\n"
"s_mov_b32 s89, 0x0\n"
"buffer_load_dwordx4 v2, s[72:75], s89 offen lds\n"
"s_add_u32 m0, s53, 1024\n"
"s_mov_b32 s89, 0x2000\n"
"buffer_load_dwordx4 v2, s[72:75], s89 offen lds\n"
"s_add_u32 m0, s53, 2048\n"
"s_mov_b32 s89, 0x4000\n"
"buffer_load_dwordx4 v2, s[72:75], s89 offen lds\n"
"s_add_u32 m0, s53, 3072\n"
"s_mov_b32 s89, 0x6000\n"
"buffer_load_dwordx4 v2, s[72:75], s89 offen lds\n"
"s_add_u32 m0, s53, 4096\n"
"s_mov_b32 s89, 0x8000\n"
"buffer_load_dwordx4 v2, s[72:75], s89 offen lds\n"
"s_add_u32 m0, s53, 5120\n"
"s_mov_b32 s89, 0xa000\n"
"buffer_load_dwordx4 v2, s[72:75], s89 offen lds\n"
"s_add_u32 m0, s53, 6144\n"
"s_mov_b32 s89, 0xc000\n"
"buffer_load_dwordx4 v2, s[72:75], s89 offen lds\n"
"s_add_u32 m0, s53, 7168\n"
"s_mov_b32 s89, 0xe000\n"
"buffer_load_dwordx4 v2, s[72:75], s89 offen lds\n"
"s_add_u32 m0, s53, 8192\n"
"s_mov_b32 s89, 0x10000\n"
"buffer_load_dwordx4 v2, s[72:75], s89 offen lds\n"
"s_add_u32 m0, s53, 9216\n"
"s_mov_b32 s89, 0x12000\n"
"buffer_load_dwordx4 v2, s[72:75], s89 offen lds\n"
"s_add_u32 m0, s53, 10240\n"
"s_mov_b32 s89, 0x14000\n"
"buffer_load_dwordx4 v2, s[72:75], s89 offen lds\n"
"s_add_u32 m0, s53, 11264\n"
"s_mov_b32 s89, 0x16000\n"
"buffer_load_dwordx4 v2, s[72:75], s89 offen lds\n"
"s_add_u32 m0, s53, 12288\n"
"s_mov_b32 s89, 0x18000\n"
"buffer_load_dwordx4 v2, s[72:75], s89 offen lds\n"
"s_add_u32 m0, s53, 13312\n"
"s_mov_b32 s89, 0x1a000\n"
"buffer_load_dwordx4 v2, s[72:75], s89 offen lds\n"
"s_add_u32 m0, s53, 14336\n"
"s_mov_b32 s89, 0x1c000\n"
"buffer_load_dwordx4 v2, s[72:75], s89 offen lds\n"
"s_add_u32 m0, s53, 15360\n"
"s_mov_b32 s89, 0x1e000\n"
"buffer_load_dwordx4 v2, s[72:75], s89 offen lds\n"
"s_add_u32 m0, s53, 16384\n"
"s_mov_b32 s89, 0x20000\n"
"buffer_load_dwordx4 v2, s[72:75], s89 offen lds\n"
"s_add_u32 m0, s53, 17408\n"
"s_mov_b32 s89, 0x22000\n"
"buffer_load_dwordx4 v2, s[72:75], s89 offen lds\n"
"s_add_u32 m0, s53, 18432\n"
"s_mov_b32 s89, 0x24000\n"
"buffer_load_dwordx4 v2, s[72:75], s89 offen lds\n"
"s_add_u32 m0, s53, 19456\n"
"s_mov_b32 s89, 0x26000\n"
"buffer_load_dwordx4 v2, s[72:75], s89 offen lds\n"
"s_add_u32 m0, s53, 20480\n"
"s_mov_b32 s89, 0x28000\n"
"buffer_load_dwordx4 v2, s[72:75], s89 offen lds\n"
"s_add_u32 m0, s53, 21504\n"
"s_mov_b32 s89, 0x2a000\n"
"buffer_load_dwordx4 v2, s[72:75], s89 offen lds\n"
"s_add_u32 m0, s53, 22528\n"
"s_mov_b32 s89, 0x2c000\n"
"buffer_load_dwordx4 v2, s[72:75], s89 offen lds\n"
"s_add_u32 m0, s53, 23552\n"
"s_mov_b32 s89, 0x2e000\n"
"buffer_load_dwordx4 v2, s[72:75], s89 offen lds\n"
"s_add_u32 m0, s53, 24576\n"
"s_mov_b32 s89, 0x30000\n"
"buffer_load_dwordx4 v2, s[72:75], s89 offen lds\n"
"s_add_u32 m0, s53, 25600\n"
"s_mov_b32 s89, 0x32000\n"
"buffer_load_dwordx4 v2, s[72:75], s89 offen lds\n"
"s_add_u32 m0, s53, 26624\n"
"s_mov_b32 s89, 0x34000\n"
"buffer_load_dwordx4 v2, s[72:75], s89 offen lds\n"
"s_add_u32 m0, s53, 27648\n"
"s_mov_b32 s89, 0x36000\n"
"buffer_load_dwordx4 v2, s[72:75], s89 offen lds\n"
"s_add_u32 m0, s53, 28672\n"
"s_mov_b32 s89, 0x38000\n"
"buffer_load_dwordx4 v2, s[72:75], s89 offen lds\n"
"s_add_u32 m0, s53, 29696\n"
"s_mov_b32 s89, 0x3a000\n"
"buffer_load_dwordx4 v2, s[72:75], s89 offen lds\n"
"s_add_u32 m0, s53, 30720\n"
"s_mov_b32 s89, 0x3c000\n"
"buffer_load_dwordx4 v2, s[72:75], s89 offen lds\n"
"s_add_u32 m0, s53, 31744\n"
"s_mov_b32 s89, 0x3e000\n"
"buffer_load_dwordx4 v2, s[72:75], s89 offen lds\n"
"v_xor_b32 v17, 32, v3\n"
"v_xor_b32 v18, 64, v3\n"
"v_xor_b32 v19, 96, v3\n"
"s_waitcnt vmcnt(24)\n"
"ds_read_b64_tr_b16 a[0:1], v3 offset:0\n"
"ds_read_b64_tr_b16 a[2:3], v3 offset:512\n"
"ds_read_b64_tr_b16 a[4:5], v17 offset:0\n"
"ds_read_b64_tr_b16 a[6:7], v17 offset:512\n"
"ds_read_b64_tr_b16 a[8:9], v18 offset:0\n"
"ds_read_b64_tr_b16 a[10:11], v18 offset:512\n"
"ds_read_b64_tr_b16 a[12:13], v19 offset:0\n"
"ds_read_b64_tr_b16 a[14:15], v19 offset:512\n"
"s_waitcnt lgkmcnt(0)\n"
"ds_read_b64_tr_b16 a[16:17], v3 offset:4096\n"
"ds_read_b64_tr_b16 a[18:19], v3 offset:4608\n"
"ds_read_b64_tr_b16 a[20:21], v17 offset:4096\n"
"ds_read_b64_tr_b16 a[22:23], v17 offset:4608\n"
"ds_read_b64_tr_b16 a[24:25], v18 offset:4096\n"
"ds_read_b64_tr_b16 a[26:27], v18 offset:4608\n"
"ds_read_b64_tr_b16 a[28:29], v19 offset:4096\n"
"ds_read_b64_tr_b16 a[30:31], v19 offset:4608\n"
"s_waitcnt lgkmcnt(0)\n"
"s_add_u32 m0, s53, 0\n"
"s_mov_b32 s89, 0x40000\n"
"buffer_load_dwordx4 v2, s[72:75], s89 offen lds\n"
"s_add_u32 m0, s53, 1024\n"
"s_mov_b32 s89, 0x42000\n"
"buffer_load_dwordx4 v2, s[72:75], s89 offen lds\n"
"s_add_u32 m0, s53, 2048\n"
"s_mov_b32 s89, 0x44000\n"
"buffer_load_dwordx4 v2, s[72:75], s89 offen lds\n"
"s_add_u32 m0, s53, 3072\n"
"s_mov_b32 s89, 0x46000\n"
"buffer_load_dwordx4 v2, s[72:75], s89 offen lds\n"
"s_add_u32 m0, s53, 4096\n"
"s_mov_b32 s89, 0x48000\n"
"buffer_load_dwordx4 v2, s[72:75], s89 offen lds\n"
"s_add_u32 m0, s53, 5120\n"
"s_mov_b32 s89, 0x4a000\n"
"buffer_load_dwordx4 v2, s[72:75], s89 offen lds\n"
"s_add_u32 m0, s53, 6144\n"
"s_mov_b32 s89, 0x4c000\n"
"buffer_load_dwordx4 v2, s[72:75], s89 offen lds\n"
"s_add_u32 m0, s53, 7168\n"
"s_mov_b32 s89, 0x4e000\n"
"buffer_load_dwordx4 v2, s[72:75], s89 offen lds\n"
"s_waitcnt vmcnt(24)\n"
"ds_read_b64_tr_b16 a[32:33], v3 offset:8192\n"
"ds_read_b64_tr_b16 a[34:35], v3 offset:8704\n"
"ds_read_b64_tr_b16 a[36:37], v17 offset:8192\n"
"ds_read_b64_tr_b16 a[38:39], v17 offset:8704\n"
"ds_read_b64_tr_b16 a[40:41], v18 offset:8192\n"
"ds_read_b64_tr_b16 a[42:43], v18 offset:8704\n"
"ds_read_b64_tr_b16 a[44:45], v19 offset:8192\n"
"ds_read_b64_tr_b16 a[46:47], v19 offset:8704\n"
"s_waitcnt lgkmcnt(0)\n"
"ds_read_b64_tr_b16 a[48:49], v3 offset:12288\n"
"ds_read_b64_tr_b16 a[50:51], v3 offset:12800\n"
"ds_read_b64_tr_b16 a[52:53], v17 offset:12288\n"
"ds_read_b64_tr_b16 a[54:55], v17 offset:12800\n"
"ds_read_b64_tr_b16 a[56:57], v18 offset:12288\n"
"ds_read_b64_tr_b16 a[58:59], v18 offset:12800\n"
"ds_read_b64_tr_b16 a[60:61], v19 offset:12288\n"
"ds_read_b64_tr_b16 a[62:63], v19 offset:12800\n"
"s_waitcnt lgkmcnt(0)\n"
"s_add_u32 m0, s53, 8192\n"
"s_mov_b32 s89, 0x50000\n"
"buffer_load_dwordx4 v2, s[72:75], s89 offen lds\n"
"s_add_u32 m0, s53, 9216\n"
"s_mov_b32 s89, 0x52000\n"
"buffer_load_dwordx4 v2, s[72:75], s89 offen lds\n"
"s_add_u32 m0, s53, 10240\n"
"s_mov_b32 s89, 0x54000\n"
"buffer_load_dwordx4 v2, s[72:75], s89 offen lds\n"
"s_add_u32 m0, s53, 11264\n"
"s_mov_b32 s89, 0x56000\n"
"buffer_load_dwordx4 v2, s[72:75], s89 offen lds\n"
"s_add_u32 m0, s53, 12288\n"
"s_mov_b32 s89, 0x58000\n"
"buffer_load_dwordx4 v2, s[72:75], s89 offen lds\n"
"s_add_u32 m0, s53, 13312\n"
"s_mov_b32 s89, 0x5a000\n"
"buffer_load_dwordx4 v2, s[72:75], s89 offen lds\n"
"s_add_u32 m0, s53, 14336\n"
"s_mov_b32 s89, 0x5c000\n"
"buffer_load_dwordx4 v2, s[72:75], s89 offen lds\n"
"s_add_u32 m0, s53, 15360\n"
"s_mov_b32 s89, 0x5e000\n"
"buffer_load_dwordx4 v2, s[72:75], s89 offen lds\n"
"s_waitcnt vmcnt(24)\n"
"ds_read_b64_tr_b16 a[64:65], v3 offset:16384\n"
"ds_read_b64_tr_b16 a[66:67], v3 offset:16896\n"
"ds_read_b64_tr_b16 a[68:69], v17 offset:16384\n"
"ds_read_b64_tr_b16 a[70:71], v17 offset:16896\n"
"ds_read_b64_tr_b16 a[72:73], v18 offset:16384\n"
"ds_read_b64_tr_b16 a[74:75], v18 offset:16896\n"
"ds_read_b64_tr_b16 a[76:77], v19 offset:16384\n"
"ds_read_b64_tr_b16 a[78:79], v19 offset:16896\n"
"s_waitcnt lgkmcnt(0)\n"
"ds_read_b64_tr_b16 a[80:81], v3 offset:20480\n"
"ds_read_b64_tr_b16 a[82:83], v3 offset:20992\n"
"ds_read_b64_tr_b16 a[84:85], v17 offset:20480\n"
"ds_read_b64_tr_b16 a[86:87], v17 offset:20992\n"
"ds_read_b64_tr_b16 a[88:89], v18 offset:20480\n"
"ds_read_b64_tr_b16 a[90:91], v18 offset:20992\n"
"ds_read_b64_tr_b16 a[92:93], v19 offset:20480\n"
"ds_read_b64_tr_b16 a[94:95], v19 offset:20992\n"
"s_waitcnt lgkmcnt(0)\n"
"s_add_u32 m0, s53, 16384\n"
"s_mov_b32 s89, 0x60000\n"
"buffer_load_dwordx4 v2, s[72:75], s89 offen lds\n"
"s_add_u32 m0, s53, 17408\n"
"s_mov_b32 s89, 0x62000\n"
"buffer_load_dwordx4 v2, s[72:75], s89 offen lds\n"
"s_add_u32 m0, s53, 18432\n"
"s_mov_b32 s89, 0x64000\n"
"buffer_load_dwordx4 v2, s[72:75], s89 offen lds\n"
"s_add_u32 m0, s53, 19456\n"
"s_mov_b32 s89, 0x66000\n"
"buffer_load_dwordx4 v2, s[72:75], s89 offen lds\n"
"s_add_u32 m0, s53, 20480\n"
"s_mov_b32 s89, 0x68000\n"
"buffer_load_dwordx4 v2, s[72:75], s89 offen lds\n"
"s_add_u32 m0, s53, 21504\n"
"s_mov_b32 s89, 0x6a000\n"
"buffer_load_dwordx4 v2, s[72:75], s89 offen lds\n"
"s_add_u32 m0, s53, 22528\n"
"s_mov_b32 s89, 0x6c000\n"
"buffer_load_dwordx4 v2, s[72:75], s89 offen lds\n"
"s_add_u32 m0, s53, 23552\n"
"s_mov_b32 s89, 0x6e000\n"
"buffer_load_dwordx4 v2, s[72:75], s89 offen lds\n"
"s_waitcnt vmcnt(24)\n"
"ds_read_b64_tr_b16 a[96:97], v3 offset:24576\n"
"ds_read_b64_tr_b16 a[98:99], v3 offset:25088\n"
"ds_read_b64_tr_b16 a[100:101], v17 offset:24576\n"
"ds_read_b64_tr_b16 a[102:103], v17 offset:25088\n"
"ds_read_b64_tr_b16 a[104:105], v18 offset:24576\n"
"ds_read_b64_tr_b16 a[106:107], v18 offset:25088\n"
"ds_read_b64_tr_b16 a[108:109], v19 offset:24576\n"
"ds_read_b64_tr_b16 a[110:111], v19 offset:25088\n"
"s_waitcnt lgkmcnt(0)\n"
"ds_read_b64_tr_b16 a[112:113], v3 offset:28672\n"
"ds_read_b64_tr_b16 a[114:115], v3 offset:29184\n"
"ds_read_b64_tr_b16 a[116:117], v17 offset:28672\n"
"ds_read_b64_tr_b16 a[118:119], v17 offset:29184\n"
"ds_read_b64_tr_b16 a[120:121], v18 offset:28672\n"
"ds_read_b64_tr_b16 a[122:123], v18 offset:29184\n"
"ds_read_b64_tr_b16 a[124:125], v19 offset:28672\n"
"ds_read_b64_tr_b16 a[126:127], v19 offset:29184\n"
"s_waitcnt lgkmcnt(0)\n"
"s_waitcnt vmcnt(16)\n"
"ds_read_b64_tr_b16 a[128:129], v3 offset:0\n"
"ds_read_b64_tr_b16 a[130:131], v3 offset:512\n"
"ds_read_b64_tr_b16 a[132:133], v17 offset:0\n"
"ds_read_b64_tr_b16 a[134:135], v17 offset:512\n"
"ds_read_b64_tr_b16 a[136:137], v18 offset:0\n"
"ds_read_b64_tr_b16 a[138:139], v18 offset:512\n"
"ds_read_b64_tr_b16 a[140:141], v19 offset:0\n"
"ds_read_b64_tr_b16 a[142:143], v19 offset:512\n"
"s_waitcnt lgkmcnt(0)\n"
"ds_read_b64_tr_b16 a[144:145], v3 offset:4096\n"
"ds_read_b64_tr_b16 a[146:147], v3 offset:4608\n"
"ds_read_b64_tr_b16 a[148:149], v17 offset:4096\n"
"ds_read_b64_tr_b16 a[150:151], v17 offset:4608\n"
"ds_read_b64_tr_b16 a[152:153], v18 offset:4096\n"
"ds_read_b64_tr_b16 a[154:155], v18 offset:4608\n"
"ds_read_b64_tr_b16 a[156:157], v19 offset:4096\n"
"ds_read_b64_tr_b16 a[158:159], v19 offset:4608\n"
"s_waitcnt lgkmcnt(0)\n"
"s_waitcnt vmcnt(8)\n"
"ds_read_b64_tr_b16 a[160:161], v3 offset:8192\n"
"ds_read_b64_tr_b16 a[162:163], v3 offset:8704\n"
"ds_read_b64_tr_b16 a[164:165], v17 offset:8192\n"
"ds_read_b64_tr_b16 a[166:167], v17 offset:8704\n"
"ds_read_b64_tr_b16 a[168:169], v18 offset:8192\n"
"ds_read_b64_tr_b16 a[170:171], v18 offset:8704\n"
"ds_read_b64_tr_b16 a[172:173], v19 offset:8192\n"
"ds_read_b64_tr_b16 a[174:175], v19 offset:8704\n"
"s_waitcnt lgkmcnt(0)\n"
"ds_read_b64_tr_b16 a[176:177], v3 offset:12288\n"
"ds_read_b64_tr_b16 a[178:179], v3 offset:12800\n"
"ds_read_b64_tr_b16 a[180:181], v17 offset:12288\n"
"ds_read_b64_tr_b16 a[182:183], v17 offset:12800\n"
"ds_read_b64_tr_b16 a[184:185], v18 offset:12288\n"
"ds_read_b64_tr_b16 a[186:187], v18 offset:12800\n"
"ds_read_b64_tr_b16 a[188:189], v19 offset:12288\n"
"ds_read_b64_tr_b16 a[190:191], v19 offset:12800\n"
"s_waitcnt lgkmcnt(0)\n"
"s_waitcnt vmcnt(0)\n"
"ds_read_b64_tr_b16 a[192:193], v3 offset:16384\n"
"ds_read_b64_tr_b16 a[194:195], v3 offset:16896\n"
"ds_read_b64_tr_b16 a[196:197], v17 offset:16384\n"
"ds_read_b64_tr_b16 a[198:199], v17 offset:16896\n"
"ds_read_b64_tr_b16 a[200:201], v18 offset:16384\n"
"ds_read_b64_tr_b16 a[202:203], v18 offset:16896\n"
"ds_read_b64_tr_b16 a[204:205], v19 offset:16384\n"
"ds_read_b64_tr_b16 a[206:207], v19 offset:16896\n"
"s_waitcnt lgkmcnt(0)\n"
"ds_read_b64_tr_b16 a[208:209], v3 offset:20480\n"
"ds_read_b64_tr_b16 a[210:211], v3 offset:20992\n"
"ds_read_b64_tr_b16 a[212:213], v17 offset:20480\n"
"ds_read_b64_tr_b16 a[214:215], v17 offset:20992\n"
"ds_read_b64_tr_b16 a[216:217], v18 offset:20480\n"
"ds_read_b64_tr_b16 a[218:219], v18 offset:20992\n"
"ds_read_b64_tr_b16 a[220:221], v19 offset:20480\n"
"ds_read_b64_tr_b16 a[222:223], v19 offset:20992\n"
"s_waitcnt lgkmcnt(0)\n"
"s_barrier\n"
"s_mov_b32 s95, 0x3f803f80\n"
"v_lshrrev_b32 v2, 2, v0\n"
"v_and_b32 v2, 0x7f, v2\n"
"s_mov_b32 s46, -1\n"
"s_mov_b32 s33, 4917504\n"
"s_mov_b32 s88, 0\n"
"Ldgo%=:\n"
"buffer_load_dwordx2 v[10:11], off, s[40:43], s33 sc1\n"
"s_add_u32 s88, s88, 1\n"
"s_cmp_gt_u32 s88, 131072\n"
"s_cbranch_scc1 Ldend%=\n"
"Ldgp%=:\n"
"s_waitcnt vmcnt(0)\n"
"v_cmp_ne_u32 vcc, s36, v10\n"
"s_cbranch_vccnz Ldgo%=\n"
"v_readfirstlane_b32 s99, v11\n"
"s_lshl_b32 s33, s99, 13\n"
"s_xor_b32 s99, s99, s37\n"
"s_xor_b32 s54, s54, s33\n"
"s_xor_b32 s55, s55, s33\n"
"s_bfe_u32 s48, s52, 0x10015\n"
"s_xor_b32 s48, s48, s99\n"
"s_lshl_b32 s48, s48, 13\n"
"s_add_u32 s48, s48, s52\n"
"s_add_u32 s52, s52, 262144\n"
"s_branch Ldwait%=\n"
"Ldloop%=:\n"
"s_bfe_u32 s90, s66, 0x10015\n"
"s_xor_b32 s90, s90, s99\n"
"s_lshl_b32 s90, s90, 13\n"
"s_add_u32 s90, s90, s66\n"
"s_xor_b32 s90, s90, s45\n"
"s_add_u32 s46, s46, 1\n"
"buffer_load_dwordx2 v[12:13], v6, s[56:59], s90 offen sc1\n"
"s_add_u32 s66, s66, 0x80000\n"
"s_and_b32 s90, s90, s65\n"
"s_add_u32 s69, s67, s90\n"
"s_add_u32 s70, s76, s90\n"
"s_bfe_u32 s48, s52, 0x10015\n"
"s_xor_b32 s48, s48, s99\n"
"s_lshl_b32 s48, s48, 13\n"
"s_add_u32 s48, s48, s52\n"
"s_add_u32 s52, s52, 262144\n"
"s_waitcnt vmcnt(14)\n"
"v_mfma_f32_16x16x32_bf16 v[16:19], a[0:3], v[128:131], 0\n"
"v_mfma_f32_16x16x32_bf16 v[20:23], a[4:7], v[128:131], 0\n"
"v_mfma_f32_16x16x32_bf16 v[24:27], a[8:11], v[128:131], 0\n"
"v_mfma_f32_16x16x32_bf16 v[28:31], a[12:15], v[128:131], 0\n"
"s_waitcnt vmcnt(13)\n"
"v_mfma_f32_16x16x32_bf16 v[16:19], a[16:19], v[132:135], v[16:19]\n"
"v_mfma_f32_16x16x32_bf16 v[20:23], a[20:23], v[132:135], v[20:23]\n"
"v_mfma_f32_16x16x32_bf16 v[24:27], a[24:27], v[132:135], v[24:27]\n"
"v_mfma_f32_16x16x32_bf16 v[28:31], a[28:31], v[132:135], v[28:31]\n"
"s_waitcnt vmcnt(12)\n"
"v_mfma_f32_16x16x32_bf16 v[16:19], a[32:35], v[136:139], v[16:19]\n"
"v_mfma_f32_16x16x32_bf16 v[20:23], a[36:39], v[136:139], v[20:23]\n"
"v_mfma_f32_16x16x32_bf16 v[24:27], a[40:43], v[136:139], v[24:27]\n"
"v_mfma_f32_16x16x32_bf16 v[28:31], a[44:47], v[136:139], v[28:31]\n"
"s_waitcnt vmcnt(11)\n"
"v_mfma_f32_16x16x32_bf16 v[16:19], a[48:51], v[140:143], v[16:19]\n"
"v_mfma_f32_16x16x32_bf16 v[20:23], a[52:55], v[140:143], v[20:23]\n"
"v_mfma_f32_16x16x32_bf16 v[24:27], a[56:59], v[140:143], v[24:27]\n"
"v_mfma_f32_16x16x32_bf16 v[28:31], a[60:63], v[140:143], v[28:31]\n"
"s_waitcnt vmcnt(10)\n"
"v_mfma_f32_16x16x32_bf16 v[16:19], a[64:67], v[144:147], v[16:19]\n"
"v_mfma_f32_16x16x32_bf16 v[20:23], a[68:71], v[144:147], v[20:23]\n"
"v_mfma_f32_16x16x32_bf16 v[24:27], a[72:75], v[144:147], v[24:27]\n"
"v_mfma_f32_16x16x32_bf16 v[28:31], a[76:79], v[144:147], v[28:31]\n"
"s_waitcnt vmcnt(9)\n"
"v_mfma_f32_16x16x32_bf16 v[16:19], a[80:83], v[148:151], v[16:19]\n"
"v_mfma_f32_16x16x32_bf16 v[20:23], a[84:87], v[148:151], v[20:23]\n"
"v_mfma_f32_16x16x32_bf16 v[24:27], a[88:91], v[148:151], v[24:27]\n"
"v_mfma_f32_16x16x32_bf16 v[28:31], a[92:95], v[148:151], v[28:31]\n"
"s_waitcnt vmcnt(8)\n"
"v_mfma_f32_16x16x32_bf16 v[16:19], a[96:99], v[152:155], v[16:19]\n"
"v_mfma_f32_16x16x32_bf16 v[20:23], a[100:103], v[152:155], v[20:23]\n"
"v_mfma_f32_16x16x32_bf16 v[24:27], a[104:107], v[152:155], v[24:27]\n"
"v_mfma_f32_16x16x32_bf16 v[28:31], a[108:111], v[152:155], v[28:31]\n"
"v_mov_b32 v8, 0x7fffffff\n"
"s_mov_b64 exec, 0x3fff\n"
"buffer_atomic_smin v8, v1, s[40:43], s54 offen sc0 sc1\n"
"s_mov_b64 exec, -1\n"
"s_waitcnt vmcnt(8)\n"
"v_mfma_f32_16x16x32_bf16 v[16:19], a[112:115], v[156:159], v[16:19]\n"
"v_mfma_f32_16x16x32_bf16 v[20:23], a[116:119], v[156:159], v[20:23]\n"
"v_mfma_f32_16x16x32_bf16 v[24:27], a[120:123], v[156:159], v[24:27]\n"
"v_mfma_f32_16x16x32_bf16 v[28:31], a[124:127], v[156:159], v[28:31]\n"
"s_waitcnt vmcnt(7)\n"
"v_mfma_f32_16x16x32_bf16 v[16:19], a[128:131], v[160:163], v[16:19]\n"
"v_mfma_f32_16x16x32_bf16 v[20:23], a[132:135], v[160:163], v[20:23]\n"
"v_mfma_f32_16x16x32_bf16 v[24:27], a[136:139], v[160:163], v[24:27]\n"
"v_mfma_f32_16x16x32_bf16 v[28:31], a[140:143], v[160:163], v[28:31]\n"
"s_waitcnt vmcnt(6)\n"
"v_mfma_f32_16x16x32_bf16 v[16:19], a[144:147], v[164:167], v[16:19]\n"
"v_mfma_f32_16x16x32_bf16 v[20:23], a[148:151], v[164:167], v[20:23]\n"
"v_mfma_f32_16x16x32_bf16 v[24:27], a[152:155], v[164:167], v[24:27]\n"
"v_mfma_f32_16x16x32_bf16 v[28:31], a[156:159], v[164:167], v[28:31]\n"
"s_waitcnt vmcnt(5)\n"
"v_mfma_f32_16x16x32_bf16 v[16:19], a[160:163], v[168:171], v[16:19]\n"
"v_mfma_f32_16x16x32_bf16 v[20:23], a[164:167], v[168:171], v[20:23]\n"
"v_mfma_f32_16x16x32_bf16 v[24:27], a[168:171], v[168:171], v[24:27]\n"
"v_mfma_f32_16x16x32_bf16 v[28:31], a[172:175], v[168:171], v[28:31]\n"
"s_waitcnt vmcnt(4)\n"
"v_mfma_f32_16x16x32_bf16 v[16:19], a[176:179], v[172:175], v[16:19]\n"
"v_mfma_f32_16x16x32_bf16 v[20:23], a[180:183], v[172:175], v[20:23]\n"
"v_mfma_f32_16x16x32_bf16 v[24:27], a[184:187], v[172:175], v[24:27]\n"
"v_mfma_f32_16x16x32_bf16 v[28:31], a[188:191], v[172:175], v[28:31]\n"
"s_waitcnt vmcnt(3)\n"
"v_mfma_f32_16x16x32_bf16 v[16:19], a[192:195], v[176:179], v[16:19]\n"
"v_mfma_f32_16x16x32_bf16 v[20:23], a[196:199], v[176:179], v[20:23]\n"
"v_mfma_f32_16x16x32_bf16 v[24:27], a[200:203], v[176:179], v[24:27]\n"
"v_mfma_f32_16x16x32_bf16 v[28:31], a[204:207], v[176:179], v[28:31]\n"
"s_waitcnt vmcnt(2)\n"
"v_mfma_f32_16x16x32_bf16 v[16:19], a[208:211], v[180:183], v[16:19]\n"
"v_mfma_f32_16x16x32_bf16 v[20:23], a[212:215], v[180:183], v[20:23]\n"
"v_mfma_f32_16x16x32_bf16 v[24:27], a[216:219], v[180:183], v[24:27]\n"
"v_mfma_f32_16x16x32_bf16 v[28:31], a[220:223], v[180:183], v[28:31]\n"
"s_waitcnt vmcnt(1)\n"
"v_lshlrev_b32 v80, 16, v12\n"
"v_and_b32 v81, 0xffff0000, v12\n"
"v_lshlrev_b32 v82, 16, v13\n"
"v_and_b32 v83, 0xffff0000, v13\n"
"v_mov_b64 v[88:89], 0\n"
"v_mov_b32 v14, s46\n"
"ds_write_b128 v4, v[16:19] offset:0\n"
"ds_write_b128 v4, v[20:23] offset:1040\n"
"ds_write_b128 v4, v[24:27] offset:2080\n"
"ds_write_b128 v4, v[28:31] offset:3120\n"
"s_waitcnt lgkmcnt(0)\n"
"s_barrier\n"
"ds_read_b128 v[48:51], v5 offset:0\n"
"ds_read_b128 v[52:55], v5 offset:4160\n"
"ds_read_b128 v[56:59], v5 offset:8320\n"
"ds_read_b128 v[60:63], v5 offset:12480\n"
"v_xor_b32 v4, 0x8000, v4\n"
"v_xor_b32 v5, 0x8000, v5\n"
"s_waitcnt lgkmcnt(2)\n"
"v_pk_add_f32 v[48:49], v[48:49], v[52:53]\n"
"v_pk_add_f32 v[50:51], v[50:51], v[54:55]\n"
"s_waitcnt lgkmcnt(0)\n"
"v_pk_add_f32 v[56:57], v[56:57], v[60:61]\n"
"v_pk_add_f32 v[58:59], v[58:59], v[62:63]\n"
"v_pk_add_f32 v[48:49], v[48:49], v[56:57]\n"
"v_pk_add_f32 v[50:51], v[50:51], v[58:59]\n"
"v_pk_add_f32 v[48:49], v[48:49], v[80:81]\n"
"v_pk_add_f32 v[50:51], v[50:51], v[82:83]\n"
"v_cvt_pk_bf16_f32 v10, v48, v49\n"
"v_cvt_pk_bf16_f32 v11, v50, v51\n"
"v_dot2c_f32_bf16 v88, s95, v10\n"
"v_dot2c_f32_bf16 v89, v10, v10\n"
"v_dot2c_f32_bf16 v88, s95, v11\n"
"v_dot2c_f32_bf16 v89, v11, v11\n"
"buffer_store_dwordx2 v[10:11], v6, s[60:63], s69 offen sc1\n"
"s_add_u32 s67, s67, s64\n"
"s_nop 0\n"
"v_add_f32_dpp v88, v88, v88 row_shr:1 row_mask:0xf bank_mask:0xf bound_ctrl:0\n"
"v_add_f32_dpp v89, v89, v89 row_shr:1 row_mask:0xf bank_mask:0xf bound_ctrl:0\n"
"s_nop 0\n"
"v_add_f32_dpp v88, v88, v88 row_shr:2 row_mask:0xf bank_mask:0xf bound_ctrl:0\n"
"v_add_f32_dpp v89, v89, v89 row_shr:2 row_mask:0xf bank_mask:0xf bound_ctrl:0\n"
"s_nop 0\n"
"v_add_f32_dpp v88, v88, v88 row_shr:4 row_mask:0xf bank_mask:0xf bound_ctrl:0\n"
"v_add_f32_dpp v89, v89, v89 row_shr:4 row_mask:0xf bank_mask:0xf bound_ctrl:0\n"
"s_nop 0\n"
"v_add_f32_dpp v88, v88, v88 row_shr:8 row_mask:0xf bank_mask:0xf bound_ctrl:0\n"
"v_add_f32_dpp v89, v89, v89 row_shr:8 row_mask:0xf bank_mask:0xf bound_ctrl:0\n"
"buffer_store_dwordx2 v[88:89], v7, s[40:43], s70 offen sc1\n"
"s_add_u32 s76, s76, 0x80000\n"
"s_waitcnt vmcnt(2)\n"
"v_cmp_ge_i32 vcc, s46, v8\n"
"s_cbranch_vccnz Ldslow%=\n"
"s_add_u32 s89, s48, 0\n"
"buffer_load_dwordx4 v[128:131], v0, s[40:43], s89 offen offset:0 sc1\n"
"buffer_load_dwordx4 v[132:135], v0, s[40:43], s89 offen offset:1024 sc1\n"
"buffer_load_dwordx4 v[136:139], v0, s[40:43], s89 offen offset:2048 sc1\n"
"buffer_load_dwordx4 v[140:143], v0, s[40:43], s89 offen offset:3072 sc1\n"
"s_add_u32 s89, s48, 4096\n"
"buffer_load_dwordx4 v[144:147], v0, s[40:43], s89 offen offset:0 sc1\n"
"buffer_load_dwordx4 v[148:151], v0, s[40:43], s89 offen offset:1024 sc1\n"
"buffer_load_dwordx4 v[152:155], v0, s[40:43], s89 offen offset:2048 sc1\n"
"buffer_load_dwordx4 v[156:159], v0, s[40:43], s89 offen offset:3072 sc1\n"
"s_add_u32 s89, s48, 16384\n"
"buffer_load_dwordx4 v[160:163], v0, s[40:43], s89 offen offset:0 sc1\n"
"buffer_load_dwordx4 v[164:167], v0, s[40:43], s89 offen offset:1024 sc1\n"
"buffer_load_dwordx4 v[168:171], v0, s[40:43], s89 offen offset:2048 sc1\n"
"buffer_load_dwordx4 v[172:175], v0, s[40:43], s89 offen offset:3072 sc1\n"
"s_add_u32 s89, s48, 20480\n"
"buffer_load_dwordx4 v[176:179], v0, s[40:43], s89 offen offset:0 sc1\n"
"buffer_load_dwordx4 v[180:183], v0, s[40:43], s89 offen offset:1024 sc1\n"
"s_waitcnt vmcnt(14)\n"
"buffer_store_dword v14, v2, s[40:43], s55 offen sc1\n"
"s_branch Ldloop%=\n"
"Ldslow%=:\n"
"v_mov_b32 v8, 0x7fffffff\n"
"s_mov_b64 exec, 0x3fff\n"
"buffer_atomic_smin v8, v1, s[40:43], s54 offen sc0 sc1\n"
"s_mov_b64 exec, -1\n"
"s_waitcnt vmcnt(1)\n"
"buffer_store_dword v14, v2, s[40:43], s55 offen sc1\n"
"s_cmp_eq_u32 s46, 63\n"
"s_cbranch_scc1 Ldend%=\n"
"s_waitcnt vmcnt(1)\n"
"v_cmp_ge_i32 vcc, s46, v8\n"
"s_cbranch_vccz Ldr%=\n"
"Ldwait%=:\n"
"s_mov_b32 s88, 0\n"
"Ldp%=:\n"
"v_mov_b32 v8, 0x7fffffff\n"
"s_mov_b64 exec, 0x3fff\n"
"buffer_atomic_smin v8, v1, s[40:43], s54 offen sc0 sc1\n"
"s_mov_b64 exec, -1\n"
"s_add_u32 s88, s88, 1\n"
"s_cmp_gt_u32 s88, 131072\n"
"s_cbranch_scc1 Ldend%=\n"
"s_waitcnt vmcnt(0)\n"
"v_cmp_ge_i32 vcc, s46, v8\n"
"s_cbranch_vccz Ldr%=\n"
"s_sleep 1\n"
"s_branch Ldp%=\n"
"Ldr%=:\n"
"s_add_u32 s89, s48, 0\n"
"buffer_load_dwordx4 v[128:131], v0, s[40:43], s89 offen offset:0 sc1\n"
"buffer_load_dwordx4 v[132:135], v0, s[40:43], s89 offen offset:1024 sc1\n"
"buffer_load_dwordx4 v[136:139], v0, s[40:43], s89 offen offset:2048 sc1\n"
"buffer_load_dwordx4 v[140:143], v0, s[40:43], s89 offen offset:3072 sc1\n"
"s_add_u32 s89, s48, 4096\n"
"buffer_load_dwordx4 v[144:147], v0, s[40:43], s89 offen offset:0 sc1\n"
"buffer_load_dwordx4 v[148:151], v0, s[40:43], s89 offen offset:1024 sc1\n"
"buffer_load_dwordx4 v[152:155], v0, s[40:43], s89 offen offset:2048 sc1\n"
"buffer_load_dwordx4 v[156:159], v0, s[40:43], s89 offen offset:3072 sc1\n"
"s_add_u32 s89, s48, 16384\n"
"buffer_load_dwordx4 v[160:163], v0, s[40:43], s89 offen offset:0 sc1\n"
"buffer_load_dwordx4 v[164:167], v0, s[40:43], s89 offen offset:1024 sc1\n"
"buffer_load_dwordx4 v[168:171], v0, s[40:43], s89 offen offset:2048 sc1\n"
"buffer_load_dwordx4 v[172:175], v0, s[40:43], s89 offen offset:3072 sc1\n"
"s_add_u32 s89, s48, 20480\n"
"buffer_load_dwordx4 v[176:179], v0, s[40:43], s89 offen offset:0 sc1\n"
"buffer_load_dwordx4 v[180:183], v0, s[40:43], s89 offen offset:1024 sc1\n"
"s_branch Ldloop%=\n"
"Ldend%=:\n"
"s_waitcnt vmcnt(0) lgkmcnt(0)\n"
"LKDN:\n"
      OPS);
    }
  }
}

extern "C" void kernel_launch(void* const* d_in, const int* in_sizes, int n_in, void* d_out, int out_size, void* d_ws, size_t ws_size, hipStream_t stream){
  unsigned char* ws=(unsigned char*)(((size_t)d_ws+0x1fffff)&~(size_t)0x1fffff)+((size_t)WSO<<20);
  (void)hipFuncSetAttribute((const void*)mega,hipFuncAttributeMaxDynamicSharedMemorySize,LDS_BYTES);
  const Tab tab={{5,388,325,468,6,2054,4102,6150,517,3932161,837,3997713,262,2310,4358,6406,1029,4063489,1349,4129041,518,2566,4614,6662,1541,4194817,1861,4260369,774,2822,4870,6918,2053,4326145,2373,4391697,1030,3078,5126,7174,2565,4457473,2885,4523025,1286,3334,5382,7430,3077,4588801,3397,4654353,1542,3590,5638,7686,3589,4720129,3909,4785681,1798,3846,5894,7942,4101,4851457,4421,4917009,16777218,2070,558628897,6166,4613,5048321,4933,5113873,33620226,2326,575602977,6422,5125,5245185,5445,5310737,50463234,2582,592577057,6678,5637,5442049,5957,5507601,67306242,2838,609551137,6934,6149,5638913,6469,5704465,84149250,3094,626525217,7190,6661,5835777,6981,5901329,100992258,3350,643499297,7446,7173,6032641,7493,6098193,117835266,3606,660473377,7702,7685,6229505,8005,6295057,134678274,3862,677447457,7958,21,6426369,341,6491921,151521282,285802514,694421537,828702769,533,1835011,853,1900563,168429826,302711058,711330081,845611313,1045,1966339,1365,2031891,185338370,319619602,728238625,862519857,1557,2097667,1877,2163219,202246914,336528146,745147169,879428401,2069,2228995,2389,2294547,219155458,353436690,762055713,896336945,2581,2360323,2901,2425875,236064002,370345234,778964257,913245489,3093,2491651,3413,2557203,2623267,387188242,7343649,930154033,3605,2688515,3925,2754067,22,404031250,7474977,947062577,4117,2819843,4437,2885395,278,420874258,4118,963905585,4629,2949155,4949,3014707,534,437717266,4374,980748593,5141,3080483,5461,3146035,790,454560274,4630,997591601,5653,3211811,5973,3277363,1046,471403282,4886,1014434609,6165,3343139,6485,3408691,1302,488246290,5142,1031277617,6677,3474467,6997,3540019,1558,505089298,5398,1048120625,7189,3605795,7509,3671347,1814,3737395,5654,7999025,7701,3802659,8021,3868211,263,327,5910,8064817
  }};
  mega<<<256,256,LDS_BYTES,stream>>>((const u16*)d_in[0],(const u16*)d_in[1],(const u16*)d_in[2],(const u16*)d_in[3],(const u16*)d_in[4],(const u16*)d_in[5],
      (const u16*)d_in[8],(const u16*)d_in[9],(const u16*)d_in[10],(u16*)d_out,ws,tab);
}
```
